```python
import torch

def _softmax(x, axis=-1):
    return torch.softmax(x, dim=axis)
def _gelu(x):
    return torch.nn.functional.gelu(x, approximate="tanh")
def _silu(x):
    return torch.nn.functional.silu(x)
def _sigmoid(x):
    return torch.sigmoid(x)
def _layernorm(x, axis=-1, eps=1e-5):
    m = x.mean(dim=axis, keepdim=True)
    v = x.var(dim=axis, keepdim=True, unbiased=False)
    return (x - m) / torch.sqrt(v + eps)
def _scatter_add(tgt, idx, src, axis=0):
    return tgt.clone().index_add_(axis, idx, src)
def _segment_sum(src, seg, n):
    out = torch.zeros((n,) + src.shape[1:], dtype=src.dtype)
    return out.index_add_(0, seg, src)
def _pad(x, pw):
    flat = tuple(v for pair in reversed(pw) for v in pair)
    return torch.nn.functional.pad(x, flat)

kernel_name = 'interleave_layers__sliding_local_dilated__linear_kernel_hedgehog__prompt'

def setup_inputs():

    _w = (lambda a: (lambda t: t.to(torch.bfloat16) if t.is_floating_point() else t)(torch.as_tensor(a)))
    def _A_setup():
        B, S, D = 1, 2048, 640
        return {
            "x": _w((lambda *s: torch.randn(*s, dtype=torch.bfloat16) * 0.05)(B, S, D)),
            "wqkv": _w((lambda *s: torch.randn(*s, dtype=torch.bfloat16) * 0.05)(D, 3 * D)),
            "wo": _w((lambda *s: torch.randn(*s, dtype=torch.bfloat16) * 0.05)(D, D)),
        }
    _A_in = _A_setup()

    def _B_setup():
        B, S, D, H = 1, 2048, 640, 32
        Dh = D // H
        return {
            "x": _w((lambda *s: torch.randn(*s, dtype=torch.bfloat16) * 0.05)(B, S, D)),
            "wqkv": _w((lambda *s: torch.randn(*s, dtype=torch.bfloat16) * 0.4)(D, 3 * D)),
            "w_phi": _w((lambda *s: torch.randn(*s, dtype=torch.bfloat16) * 0.4)(H, Dh, Dh) * (Dh ** -0.5)),
            "wo": _w((lambda *s: torch.randn(*s, dtype=torch.bfloat16) * 0.1)(D, D)),
        }
    _B_in = _B_setup()

    return {
        "A_x": _A_in["x"],
        "A_wqkv": _A_in["wqkv"],
        "A_wo": _A_in["wo"],
        "B_x": _B_in["x"],
        "B_wqkv": _B_in["wqkv"],
        "B_w_phi": _B_in["w_phi"],
        "B_wo": _B_in["wo"],
    }

def reference(A_x, A_wqkv, A_wo, B_x, B_wqkv, B_w_phi, B_wo):
    def _A_ref(x, wqkv, wo):
        B, S, D = x.shape
        H = 32; Dh = D // H
        W, dil = 8, 2
        qkv = (lambda x, *d: x.permute(*d))((x @ wqkv).reshape(B, S, 3, H, Dh), 2, 0, 3, 1, 4)
        q, k, v = qkv[0], qkv[1], qkv[2]
        scale = Dh ** -0.5
        pos = torch.arange(S)
        off = torch.arange(W) * dil
        idx = pos[:, None] - off[None, :]
        mask = idx >= 0
        idx_c = torch.where(mask, idx, 0).reshape(-1)
        kg = k[:, :, idx_c, :].reshape(B, H, S, W, Dh)
        vg = v[:, :, idx_c, :].reshape(B, H, S, W, Dh)
        logits = torch.einsum("bhsd,bhswd->bhsw", q, kg) * scale
        logits = torch.where(mask[None, None, :, :], logits, -1e9)
        attn = _softmax(logits, axis=-1)
        o = torch.einsum("bhsw,bhswd->bhsd", attn, vg)
        return (lambda x, *d: x.permute(*d))(o, 0, 2, 1, 3).reshape(B, S, D) @ wo

    def _B_ref(x, wqkv, w_phi, wo):
        B, S, D = x.shape
        H = 32; Dh = D // H
        qkv = (lambda x, *d: x.permute(*d))((x @ wqkv).reshape(B, S, 3, H, Dh), 2, 0, 3, 1, 4)
        q, k, v = qkv[0], qkv[1], qkv[2]
        pq = torch.einsum("bhsd,hdf->bhsf", q, w_phi)
        pk = torch.einsum("bhsd,hdf->bhsf", k, w_phi)
        phi_q = torch.concatenate([_softmax(pq, axis=-1), _softmax(-pq, axis=-1)], axis=-1)
        phi_k = torch.concatenate([_softmax(pk, axis=-1), _softmax(-pk, axis=-1)], axis=-1)
        kv = torch.einsum("bhsf,bhsd->bhfd", phi_k, v)
        z  = torch.einsum("bhsf->bhf", phi_k)
        num = torch.einsum("bhqf,bhfd->bhqd", phi_q, kv)
        den = torch.einsum("bhqf,bhf->bhq", phi_q, z)[..., None] + 1e-6
        o = num / den
        return (lambda x, *d: x.permute(*d))(o, 0, 2, 1, 3).reshape(B, S, D) @ wo

    x = A_x
    x = x + _A_ref(_layernorm(x, axis=-1), A_wqkv, A_wo)
    x = x + _B_ref(_layernorm(x, axis=-1), B_wqkv, B_w_phi, B_wo)
    x = x + _A_ref(_layernorm(x, axis=-1), A_wqkv, A_wo)
    x = x + _B_ref(_layernorm(x, axis=-1), B_wqkv, B_w_phi, B_wo)
    return x
```

```cpp
#include <hip/hip_runtime.h>
#include <hip/hip_bf16.h>
#include <stdint.h>

typedef unsigned short u16;
constexpr int S = 2048, D = 640, H = 32, DH = 20;

__device__ __forceinline__ float bf2f(u16 b) { return __uint_as_float((unsigned)b << 16); }
__device__ __forceinline__ u16 f2bf(float f) { unsigned u = __float_as_uint(f); u += 0x7FFFu + ((u >> 16) & 1u); return (u16)(u >> 16); }

__global__ void k_cvt_in(const u16* x, float* xf) { int i = blockIdx.x * 256 + threadIdx.x; if (i < S * D) xf[i] = bf2f(x[i]); }
__global__ void k_cvt_out(const float* xf, u16* o) { int i = blockIdx.x * 256 + threadIdx.x; if (i < S * D) o[i] = f2bf(xf[i]); }

__global__ void k_ln(const float* x, float* xn) {
  int row = blockIdx.x; __shared__ float red[256];
  float s = 0; for (int c = threadIdx.x; c < D; c += 256) s += x[row * D + c];
  red[threadIdx.x] = s; __syncthreads();
  for (int o = 128; o > 0; o >>= 1) { if (threadIdx.x < o) red[threadIdx.x] += red[threadIdx.x + o]; __syncthreads(); }
  float m = red[0] / D; __syncthreads();
  float v = 0; for (int c = threadIdx.x; c < D; c += 256) { float d = x[row * D + c] - m; v += d * d; }
  red[threadIdx.x] = v; __syncthreads();
  for (int o = 128; o > 0; o >>= 1) { if (threadIdx.x < o) red[threadIdx.x] += red[threadIdx.x + o]; __syncthreads(); }
  float r = rsqrtf(red[0] / D + 1e-5f);
  for (int c = threadIdx.x; c < D; c += 256) xn[row * D + c] = (x[row * D + c] - m) * r;
}
__global__ void k_gemm(const float* A, const u16* W, const float* R, float* C, int N, int K) {
  int n = blockIdx.x * 64 + (threadIdx.x & 63); int m0 = blockIdx.y * 16 + (threadIdx.x >> 6) * 4;
  float acc[4] = {0, 0, 0, 0};
  for (int k = 0; k < K; ++k) { float w = bf2f(W[k * N + n]);
    for (int j = 0; j < 4; ++j) acc[j] += A[(m0 + j) * K + k] * w; }
  for (int j = 0; j < 4; ++j) { float r = bf2f(f2bf(acc[j])); if (R) r = bf2f(f2bf(r + R[(m0 + j) * N + n])); C[(m0 + j) * N + n] = r; }
}
__global__ void k_attnA(const float* qkv, float* o) {
  int i = blockIdx.x * 256 + threadIdx.x; if (i >= S * H) return; int s = i / H, h = i % H;
  const float* q = qkv + s * 3 * D + h * DH; float lg[8]; float mx = -1e30f;
  for (int w = 0; w < 8; ++w) { int p = s - 2 * w; float a = -1e9f; if (p >= 0) { a = 0; const float* k = qkv + p * 3 * D + D + h * DH; for (int d = 0; d < DH; ++d) a += q[d] * k[d]; a *= 0.22360679775f; } lg[w] = a; mx = fmaxf(mx, a); }
  float sum = 0; for (int w = 0; w < 8; ++w) { lg[w] = __expf(lg[w] - mx); sum += lg[w]; }
  for (int d = 0; d < DH; ++d) { float a = 0; for (int w = 0; w < 8; ++w) { int p = s - 2 * w; if (p < 0) p = 0; a += lg[w] * qkv[p * 3 * D + 2 * D + h * DH + d]; } o[s * D + h * DH + d] = a / sum; }
}
__global__ void k_phi(const float* qkv, const u16* wphi, float* phq, float* phk) {
  int i = blockIdx.x * 256 + threadIdx.x; if (i >= S * H * 2) return; int which = i & 1; int s = (i >> 1) / H, h = (i >> 1) % H;
  const float* q = qkv + s * 3 * D + which * D + h * DH; float p[DH]; float mx = -1e30f, mn = 1e30f;
  for (int f = 0; f < DH; ++f) { float a = 0; for (int d = 0; d < DH; ++d) a += q[d] * bf2f(wphi[h * 400 + d * 20 + f]); p[f] = a; mx = fmaxf(mx, a); mn = fminf(mn, a); }
  float s1 = 0, s2 = 0; for (int f = 0; f < DH; ++f) { s1 += __expf(p[f] - mx); s2 += __expf(mn - p[f]); }
  float* out = (which ? phk : phq) + (s * H + h) * 40;
  for (int f = 0; f < DH; ++f) { out[f] = __expf(p[f] - mx) / s1; out[20 + f] = __expf(mn - p[f]) / s2; }
}
__global__ void k_kv(const float* phk, const float* qkv, float* kv) {
  int h = blockIdx.x; int t = threadIdx.x; if (t >= 840) return; int f = t / 21, d = t % 21; float a = 0;
  for (int s = 0; s < S; ++s) { float v = d < 20 ? qkv[s * 3 * D + 2 * D + h * DH + d] : 1.f; a += phk[(s * H + h) * 40 + f] * v; }
  kv[h * 840 + t] = a;
}
__global__ void k_lin(const float* phq, const float* kv, float* o) {
  int i = blockIdx.x * 256 + threadIdx.x; if (i >= S * H) return; int s = i / H, h = i % H;
  const float* p = phq + (s * H + h) * 40; float den = 1e-6f; for (int f = 0; f < 40; ++f) den += p[f] * kv[h * 840 + f * 21 + 20];
  for (int d = 0; d < DH; ++d) { float a = 0; for (int f = 0; f < 40; ++f) a += p[f] * kv[h * 840 + f * 21 + d]; o[s * D + h * DH + d] = a / den; }
}

extern "C" void kernel_launch(void* const* d_in, const int* in_sizes, int n_in, void* d_out, int out_size, void* d_ws, size_t ws_size, hipStream_t stream) {
  const u16* Ax = (const u16*)d_in[0]; const u16* Awqkv = (const u16*)d_in[1]; const u16* Awo = (const u16*)d_in[2];
  const u16* Bwqkv = (const u16*)d_in[4]; const u16* Bphi = (const u16*)d_in[5]; const u16* Bwo = (const u16*)d_in[6];
  float* ws = (float*)d_ws; float* x = ws; float* x2 = x + S * D; float* xn = x2 + S * D; float* qkv = xn + S * D; float* o = qkv + S * 3 * D; float* phq = o + S * D; float* phk = phq + S * H * 40; float* kv = phk + S * H * 40;
  k_cvt_in<<<S * D / 256, 256, 0, stream>>>(Ax, x);
  for (int l = 0; l < 4; ++l) {
    k_ln<<<S, 256, 0, stream>>>(x, xn);
    k_gemm<<<dim3(3 * D / 64, S / 16), 256, 0, stream>>>(xn, (l & 1) ? Bwqkv : Awqkv, nullptr, qkv, 3 * D, D);
    if (l & 1) { k_phi<<<S * H * 2 / 256, 256, 0, stream>>>(qkv, Bphi, phq, phk); k_kv<<<H, 896, 0, stream>>>(phk, qkv, kv); k_lin<<<S * H / 256, 256, 0, stream>>>(phq, kv, o); }
    else k_attnA<<<S * H / 256, 256, 0, stream>>>(qkv, o);
    k_gemm<<<dim3(D / 64, S / 16), 256, 0, stream>>>(o, (l & 1) ? Bwo : Awo, x, x2, D, D);
    float* t = x; x = x2; x2 = t;
  }
  k_cvt_out<<<S * D / 256, 256, 0, stream>>>(x, (u16*)d_out);
}
```

```cpp
#include <hip/hip_runtime.h>
#include <stdint.h>

typedef unsigned short u16;
typedef unsigned v4u __attribute__((ext_vector_type(4)));
typedef unsigned v2u __attribute__((ext_vector_type(2)));
typedef float f32x2 __attribute__((ext_vector_type(2)));
typedef float f32x4 __attribute__((ext_vector_type(4)));
typedef float f32x16 __attribute__((ext_vector_type(16)));
typedef __bf16 bf16x8 __attribute__((ext_vector_type(8)));
typedef __bf16 bf16x2 __attribute__((ext_vector_type(2)));
typedef short s16x4 __attribute__((ext_vector_type(4)));
#define LAS __attribute__((address_space(3)))
#define GAS __attribute__((address_space(1)))
typedef __amdgpu_buffer_rsrc_t rsrc_t;

constexpr unsigned WQ_SZ = 153600, WO_SZ = 102400, XB = 2621440;
constexpr unsigned WS_CTRL = 0;
constexpr unsigned WS_WQ = 4096;
constexpr unsigned WS_WO = WS_WQ + 32 * WQ_SZ;
constexpr unsigned WS_CSP = WS_WO + 16 * WO_SZ;
constexpr unsigned WS_X = WS_CSP + 2 * 16 * 16 * 512;
constexpr unsigned WS_O = WS_X + 4 * XB;
constexpr unsigned WS_KV = WS_O + 4 * XB;
constexpr unsigned WS_HALO = WS_KV + 2 * 256 * 4096;
constexpr unsigned WS_END = WS_HALO + 2 * 256 * 4096;
constexpr unsigned WS_STAMP = WS_END;
constexpr unsigned WS_TOTAL = WS_STAMP + 256 * 4 * 64 * 8;
constexpr unsigned L_P1 = 0, KSTEP = 2816, L_P0 = 112640, L_ONES = 153600, L_CS = 153856, L_KVA = 154880;
constexpr unsigned L_SCR = 73216;
constexpr unsigned L_K = L_SCR, L_V = L_SCR + 2 * 144 * 48;
constexpr unsigned L_RED = 90112;
constexpr unsigned LDS_BYTES = 160000;
constexpr unsigned SENT = 0xFFFFFFFFu;
constexpr int SC1 = 16;
constexpr float LOG2E = 1.4426950408889634f;

struct Params { const u16 *x, *wqA, *woA, *wqB, *wphi, *woB; u16* out; unsigned char* ws; };

__device__ __forceinline__ unsigned pk2(float a, float b) { bf16x2 v = {(__bf16)a, (__bf16)b}; return __builtin_bit_cast(unsigned, v); }
__device__ __forceinline__ float lo16(unsigned u) { return __uint_as_float(u << 16); }
__device__ __forceinline__ float hi16(unsigned u) { return __uint_as_float(u & 0xffff0000u); }
__device__ __forceinline__ float dot2(unsigned a, unsigned b, float c) { return __builtin_amdgcn_fdot2_f32_bf16(__builtin_bit_cast(bf16x2, a), __builtin_bit_cast(bf16x2, b), c, false); }
__device__ __forceinline__ f32x16 mfma32(v4u a, v4u b, f32x16 c) { return __builtin_amdgcn_mfma_f32_32x32x16_bf16(__builtin_bit_cast(bf16x8, a), __builtin_bit_cast(bf16x8, b), c, 0, 0, 0); }
__device__ __forceinline__ f32x4 mfma16(v4u a, v4u b, f32x4 c) { return __builtin_amdgcn_mfma_f32_16x16x32_bf16(__builtin_bit_cast(bf16x8, a), __builtin_bit_cast(bf16x8, b), c, 0, 0, 0); }
__device__ __forceinline__ v4u lds128(LAS unsigned char* L, unsigned a) { return *(LAS v4u*)(L + a); }
__device__ __forceinline__ v2u lds64(LAS unsigned char* L, unsigned a) { return *(LAS v2u*)(L + a); }
__device__ __forceinline__ void sts128(LAS unsigned char* L, unsigned a, v4u v) { *(LAS v4u*)(L + a) = v; }
__device__ __forceinline__ v2u ldstr(LAS unsigned char* L, unsigned a) { return __builtin_bit_cast(v2u, __builtin_amdgcn_ds_read_tr16_b64_v4i16((LAS s16x4*)(L + a))); }
__device__ __forceinline__ v4u ldg(rsrc_t rs, unsigned voff, unsigned soff, int) { return __builtin_amdgcn_raw_buffer_load_b128(rs, voff, soff, 0); }
__device__ __forceinline__ v4u ldg_sc1(rsrc_t rs, unsigned voff, unsigned soff) { return __builtin_amdgcn_raw_buffer_load_b128(rs, voff, soff, SC1); }
__device__ __forceinline__ v4u nosent(v4u v) { v.x = min(v.x, SENT - 1); v.y = min(v.y, SENT - 1); v.z = min(v.z, SENT - 1); v.w = min(v.w, SENT - 1); return v; }
__device__ __forceinline__ void stg_sc1(rsrc_t rs, unsigned voff, unsigned soff, v4u v) { __builtin_amdgcn_raw_buffer_store_b128(nosent(v), rs, voff, soff, SC1); }
__device__ __forceinline__ unsigned mx4(unsigned m, v4u d) { return max(max(m, max(d.x, d.y)), max(d.z, d.w)); }
#define BAR() asm volatile("s_waitcnt lgkmcnt(0)\n\ts_barrier" ::: "memory")
#define DRAIN() asm volatile("s_waitcnt vmcnt(0)" ::: "memory")
#define FENCE() asm volatile("" ::: "memory")
__device__ __forceinline__ void dma1(rsrc_t rs, unsigned voff, unsigned soff, unsigned ldsa) {
  asm volatile("s_mov_b32 m0, %0\n\ts_nop 0\n\tbuffer_load_dwordx4 %1, %2, %3 offen lds" :: "s"(__builtin_amdgcn_readfirstlane(ldsa)), "v"(voff), "s"(rs), "s"(__builtin_amdgcn_readfirstlane(soff)) : "memory");
}
__device__ __forceinline__ bool give_up(unsigned& sp, unsigned char* ws) {
  ++sp;
  if (sp > 8) __builtin_amdgcn_s_sleep(2);
  if ((sp & 63u) == 0u) {
    GAS unsigned* t = (GAS unsigned*)(ws + WS_CTRL);
    if (sp >= (1u << 16)) { __hip_atomic_store(t, 1u, __ATOMIC_RELAXED, __HIP_MEMORY_SCOPE_AGENT); return true; }
    if (__builtin_amdgcn_readfirstlane(__hip_atomic_load(t, __ATOMIC_RELAXED, __HIP_MEMORY_SCOPE_AGENT)) == 1u) return true;
  }
  return false;
}
__device__ __forceinline__ int xfeat(int c, int j) { int c8 = c / 10, l = c % 10; return 80 * c8 + (l < 8 ? 20 * (l >> 1) + 8 * (l & 1) + j : 40 * (l - 8) + 20 * (j >> 2) + 16 + (j & 3)); }
__device__ __forceinline__ int ocol(int cgp, int s, int j) { return 40 * cgp + (s < 4 ? 20 * (s >> 1) + 8 * (s & 1) + j : 20 * (j >> 2) + 16 + (j & 3)); }

__device__ __forceinline__ int fresh_lane() { int l; asm volatile("v_mbcnt_lo_u32_b32 %0, -1, 0\n\tv_mbcnt_hi_u32_b32 %0, -1, %0" : "=v"(l)); return l; }
#define RELANE() const int lane = fresh_lane(), h = lane >> 5, tok = lane & 31, tid = w * 64 + lane; const unsigned l16 = lane * 16; (void)h; (void)tok; (void)tid; (void)l16
#define STAMP(i) do {} while (0)

__device__ __forceinline__ void publish_x(rsrc_t rs, unsigned xbuf, int rg, int cg, int w, int lane, const unsigned (&xr)[10]) {
  const int g = lane >> 4, tok = lane & 15, half = cg >> 3, c8 = cg & 7;
  const unsigned sb = xbuf + ((rg * 4 + half * 2 + (w >> 1)) * 40 + 5 * c8) * 1024;
  const unsigned vb = ((w & 1) * 16 + tok) * 16;
  stg_sc1(rs, vb + g * 1024, sb, v4u{xr[0], xr[1], xr[2], xr[3]});
  stg_sc1(rs, vb + g * 1024 + 512, sb, v4u{xr[4], xr[5], xr[6], xr[7]});
  auto s0 = __builtin_amdgcn_permlane16_swap(xr[8], xr[8], false, false);
  auto s1 = __builtin_amdgcn_permlane16_swap(xr[9], xr[9], false, false);
  if (!(g & 1)) stg_sc1(rs, vb + 4096 + (g >> 1) * 512, sb, v4u{xr[8], xr[9], s0[1], s1[1]});
}
__device__ __forceinline__ void load_x_in(const u16* x, int rg, int cg, int w, int lane, unsigned (&xr)[10]) {
  const int g = lane >> 4, tok = lane & 15;
  const GAS v2u* src = (const GAS v2u*)(x + (size_t)(rg * 128 + (cg >> 3) * 64 + w * 16 + tok) * 640 + (cg & 7) * 80 + g * 20);
#pragma unroll
  for (int i = 0; i < 5; ++i) { v2u t = src[i]; xr[2 * i] = t.x; xr[2 * i + 1] = t.y; }
}

__global__ void __launch_bounds__(256) k_prep(Params p) {
  __shared__ __attribute__((aligned(16))) unsigned char smem[3 * 10240];
  LAS unsigned char* L = (LAS unsigned char*)smem;
  const int b = blockIdx.x, tid = threadIdx.x, lane = tid & 63, w = __builtin_amdgcn_readfirstlane(tid >> 6);
  const int rg = (b & 7) * 2 + (b >> 7), cg = (b >> 3) & 15;
  const rsrc_t rs = __builtin_amdgcn_make_buffer_rsrc(p.ws, 0, WS_TOTAL, 0x00020000);
  if (tid == 0 && b == 0) *(GAS unsigned*)(p.ws + WS_CTRL) = 0u;
  unsigned xr[10];
  load_x_in(p.x, rg, cg, w, lane, xr);
#pragma unroll
  for (int m = 0; m < 2; ++m) {
    const u16* W = m ? p.wqB : p.wqA;
#pragma unroll
    for (int i = 0; i < 3; ++i) {
      const int idx = tid + 256 * i;
      if (idx < 600) { const int r = idx / 15, cc = idx % 15;
        v4u v = *(const GAS v4u*)(W + (size_t)xfeat(5 * rg + (r >> 3), r & 7) * 1920 + (cc / 5) * 640 + 40 * cg + 8 * (cc % 5));
        sts128(L, m * 10240 + r * 256 + cc * 16, v); }
    }
  }
  { const u16* W = (cg >> 3) ? p.woB : p.woA;
#pragma unroll
    for (int i = 0; i < 2; ++i) {
      const int idx = tid + 256 * i;
      if (idx < 400) { const int r = idx / 10, cc = idx % 10;
        v4u v = *(const GAS v4u*)(W + (size_t)ocol(rg, r >> 3, r & 7) * 640 + 80 * (cg & 7) + 8 * cc);
        sts128(L, 20480 + r * 160 + cc * 16, v); }
    }
  }
  publish_x(rs, WS_X, rg, cg, w, lane, xr);
  __syncthreads();
  {
    const int seg = w >> 1, h = w & 1, g = lane >> 4, c = lane & 15;
    v4u bf[2];
#pragma unroll
    for (int ct = 0; ct < 2; ++ct) {
      unsigned e[8];
#pragma unroll
      for (int j = 0; j < 8; ++j) { const int d = 8 * g + j, f = 16 * ct + c; e[j] = (d < 20 && f < 20) ? p.wphi[(2 * cg + h) * 400 + d * 20 + f] : 0; }
      bf[ct] = v4u{e[0] | (e[1] << 16), e[2] | (e[3] << 16), e[4] | (e[5] << 16), e[6] | (e[7] << 16)};
    }
    v4u af[3];
#pragma unroll
    for (int rt = 0; rt < 3; ++rt) { const int r = min(16 * rt + c, 39); const unsigned a = 10240 + r * 256 + seg * 80 + h * 40 + g * 16;
      v2u t0 = lds64(L, a), t1 = lds64(L, a + 8); af[rt] = v4u{t0.x, t0.y, t1.x, t1.y}; }
#pragma unroll
    for (int rt = 0; rt < 3; ++rt)
#pragma unroll
      for (int ct = 0; ct < 2; ++ct) {
        f32x4 d = mfma16(af[rt], bf[ct], f32x4{0.f, 0.f, 0.f, 0.f});
#pragma unroll
        for (int r = 0; r < 4; ++r) { const int row = 16 * rt + 4 * g + r, f = 16 * ct + c;
          if (row < 40 && f < 20) *(LAS u16*)(L + 10240 + row * 256 + seg * 80 + h * 40 + f * 2) = (u16)(pk2(d[r], 0.f) & 0xffffu); }
      }
  }
  __syncthreads();
  {
    const int T = w, gam = lane >> 4, q = (lane >> 2) & 3, pp = lane & 3, a = 2 * (gam & 1) + (pp >> 1), h = pp & 1, row = lane & 31;
    const int eoff = (T == 0) ? 20 * h + 4 * a : (T == 1) ? 40 + 20 * h + 4 * a : (T == 2) ? 80 + 20 * h + 4 * a : (a == 0 ? 40 : a == 1 ? 80 : 0) + 20 * h + 16;
#pragma unroll
    for (int m = 0; m < 2; ++m) {
      float cs = 0.f;
#pragma unroll
      for (int it = 0; it < 3; ++it) {
        const int kap = 2 * it + (gam >> 1), kc = min(kap, 4);
        const unsigned ad = m * 10240 + (kc * 8 + q) * 256 + eoff * 2;
        v2u t0 = ldstr(L, ad), t1 = ldstr(L, ad + 1024);
        if (kap < 5 && (T < 3 || row < 24)) {
          const int c = 5 * rg + kap, s = c >> 1, kh = c & 1;
          const unsigned o = (T == 0) ? L_P0 + s * 1024 + kh * 512 + row * 16 : (T < 3) ? s * KSTEP + (T - 1) * 1024 + kh * 512 + row * 16 : s * KSTEP + 2048 + kh * 384 + row * 16;
          *(GAS v4u*)(p.ws + WS_WQ + (m * 16 + cg) * WQ_SZ + o) = v4u{t0.x, t0.y, t1.x, t1.y};
          cs = dot2(t0.x, 0x3F803F80u, cs); cs = dot2(t0.y, 0x3F803F80u, cs); cs = dot2(t1.x, 0x3F803F80u, cs); cs = dot2(t1.y, 0x3F803F80u, cs);
        }
      }
      cs += __shfl_xor(cs, 32);
      if (lane < 32) *(GAS float*)(p.ws + WS_CSP + ((m * 16 + cg) * 16 + rg) * 512 + (T * 32 + row) * 4) = cs;
    }
  }
  {
    const int q = (lane >> 2) & 3, pp = lane & 3;
#pragma unroll
    for (int it = 0; it < 2; ++it) {
      const int pc = it * 16 + (tid >> 4), pcc = min(pc, 24), s = pcc / 5, ft = pcc % 5;
      const unsigned ad = 20480 + (s * 8 + q) * 160 + 40 * pp + 8 * ft;
      v2u t0 = ldstr(L, ad), t1 = ldstr(L, ad + 640);
      if (pc < 25) *(GAS v4u*)(p.ws + WS_WO + cg * WO_SZ + (((rg >> 2) * 5 + s) * 5 + ft) * 1024 + (rg & 3) * 256 + (lane & 15) * 16) = v4u{t0.x, t0.y, t1.x, t1.y};
    }
  }
  {
    constexpr unsigned P0 = WS_X + XB, PN = (WS_END - P0) / 16;
    for (unsigned i = b * 256 + tid; i < PN; i += 65536) *(GAS v4u*)(p.ws + P0 + i * 16) = v4u{SENT, SENT, SENT, SENT};
  }
}

__device__ __forceinline__ void phi_map(const float (&pv)[20], unsigned (&ph)[20]) {
  float mx = pv[0], mn = pv[0];
#pragma unroll
  for (int i = 1; i < 20; ++i) { mx = fmaxf(mx, pv[i]); mn = fminf(mn, pv[i]); }
  float ep[20], en[20], sp = 0.f, sn = 0.f;
#pragma unroll
  for (int i = 0; i < 20; ++i) { ep[i] = __builtin_amdgcn_exp2f(pv[i] - mx); en[i] = __builtin_amdgcn_exp2f(mn - pv[i]); sp += ep[i]; sn += en[i]; }
  const float rp = __builtin_amdgcn_rcpf(sp), rn = __builtin_amdgcn_rcpf(sn);
#pragma unroll
  for (int i = 0; i < 10; ++i) { ph[i] = pk2(ep[2 * i] * rp, ep[2 * i + 1] * rp); ph[10 + i] = pk2(en[2 * i] * rn, en[2 * i + 1] * rn); }
}

__global__ void __launch_bounds__(256, 1) k_main(Params p) {
  extern __shared__ __attribute__((aligned(16))) unsigned char smem[];
  LAS unsigned char* L = (LAS unsigned char*)smem;
  const int b = blockIdx.x, w = __builtin_amdgcn_readfirstlane(threadIdx.x >> 6);
  const int rg = (b & 7) * 2 + (b >> 7), cg = (b >> 3) & 15, half = cg >> 3, c8 = cg & 7;
  const rsrc_t rs = __builtin_amdgcn_make_buffer_rsrc(p.ws, 0, WS_TOTAL, 0x00020000);
  unsigned xr[10];
  { RELANE();
    STAMP(0);
    load_x_in(p.x, rg, cg, w, lane, xr);
    float cs = 0.f; const int m = tid >> 7, idx = tid & 127, row = idx & 31;
#pragma unroll
    for (int r = 0; r < 16; ++r) cs += *(const GAS float*)(p.ws + WS_CSP + ((m * 16 + cg) * 16 + r) * 512 + idx * 4);
    *(LAS float*)(L + L_CS + ((m * 2 + ((row >> 2) & 1)) * 64 + (idx >> 5) * 16 + 4 * (row >> 3) + (row & 3)) * 4) = cs;
    if (tid < 64) *(LAS unsigned*)(L + L_ONES + tid * 4) = 0x3F803F80u;
  }

  for (int l = 0; l < 4; ++l) {
    const int m = l & 1;
    v4u xf[40], wo[25];
    float mean, rstd, kk[20], vv[20], q4[4], qq[20], o[20];
    { RELANE();
    unsigned t3 = (tok < 24) ? 2048 + h * 384 + tok * 16 : L_ONES + (((lane >> 2) & 1) * 4 + (h ? 0 : 8)) * 16;
    const unsigned t3step = (tok < 24) ? KSTEP : 0;
    { const unsigned wsrc = WS_WQ + (m * 16 + cg) * WQ_SZ;
      for (int i = w; i < 150; i += 4) dma1(rs, l16, wsrc + i * 1024, i * 1024); }
    STAMP(1 + 12 * l);
    const unsigned xs = WS_X + l * XB + (rg * 4 + w) * 40960;
#pragma unroll
    for (int s = 0; s < 40; ++s) xf[s] = ldg_sc1(rs, l16 + (s & 3) * 1024, xs + (s >> 2) * 4096);
    FENCE();
    __builtin_amdgcn_sched_barrier(0);
    f32x16 a1 = {}, a2 = {}, a3 = {};
    float ss0 = 0.f, ss1 = 0.f;
    v4u w1, w2, w3;
#pragma unroll
    for (int s = 0; s < 40; ++s) {
      if (s % 5 == 0) {
        unsigned mm = 0;
#pragma unroll
        for (int i = 0; i < 5; ++i) mm = mx4(mm, xf[s + i]);
        if (__builtin_expect(__builtin_amdgcn_ballot_w64(mm == SENT) != 0, 0)) {
          unsigned sp = 0;
          do {
            FENCE();
            mm = 0;
#pragma unroll
            for (int i = 0; i < 5; ++i) { xf[s + i] = ldg_sc1(rs, l16, xs + (s + i) * 1024); }
#pragma unroll
            for (int i = 0; i < 5; ++i) mm = mx4(mm, xf[s + i]);
          } while (__builtin_amdgcn_ballot_w64(mm == SENT) != 0 && !give_up(sp, p.ws));
        }
        if (s == 0) { BAR(); STAMP(2 + 12 * l); w1 = lds128(L, l16); w2 = lds128(L, l16 + 1024); w3 = lds128(L, t3); }
        if (s >= 10 && s <= 30) {
          const unsigned wos = WS_WO + ((m * 8 + c8) * 4 + w) * 25600;
#pragma unroll
          for (int i = s - 10; i < s - 5; ++i) wo[i] = ldg(rs, l16 + (i & 3) * 1024, wos + (i >> 2) * 4096, 0);
        }
      }
      v4u n1, n2, n3;
      if (s < 39) { t3 += t3step; n1 = lds128(L, l16 + (s + 1) * KSTEP); n2 = lds128(L, l16 + (s + 1) * KSTEP + 1024); n3 = lds128(L, t3); }
      a1 = mfma32(w1, xf[s], a1);
      a2 = mfma32(w2, xf[s], a2);
      a3 = mfma32(w3, xf[s], a3);
      ss0 = dot2(xf[s].x, xf[s].x, ss0); ss1 = dot2(xf[s].y, xf[s].y, ss1); ss0 = dot2(xf[s].z, xf[s].z, ss0); ss1 = dot2(xf[s].w, xf[s].w, ss1);
      if (s < 39) { w1 = n1; w2 = n2; w3 = n3; }
    }
    STAMP(3 + 12 * l);
    float ss = ss0 + ss1;
    { auto t = __builtin_amdgcn_permlane32_swap(__float_as_uint(ss), __float_as_uint(ss), false, false); ss = __uint_as_float(t[0]) + __uint_as_float(t[1]); }
    mean = a3[12] * (1.f / 640.f);
    rstd = __builtin_amdgcn_rsqf(fmaxf(ss * (1.f / 640.f) - mean * mean, 0.f) + 1e-5f);
    const unsigned csb = L_CS + (m * 2 + h) * 256;
    { const float rk = m ? rstd * LOG2E : rstd;
#pragma unroll
      for (int i = 0; i < 4; ++i) {
        f32x4 c1 = __builtin_bit_cast(f32x4, lds128(L, csb + 64 + i * 16)), c2 = __builtin_bit_cast(f32x4, lds128(L, csb + 128 + i * 16));
#pragma unroll
        for (int r = 0; r < 4; ++r) { kk[4 * i + r] = rk * (a1[4 * i + r] - mean * c1[r]); vv[4 * i + r] = rstd * (a2[4 * i + r] - mean * c2[r]); }
      }
      f32x4 c0 = __builtin_bit_cast(f32x4, lds128(L, csb + 192)), c1 = __builtin_bit_cast(f32x4, lds128(L, csb + 208)), c2 = __builtin_bit_cast(f32x4, lds128(L, csb + 224));
#pragma unroll
      for (int r = 0; r < 4; ++r) { kk[16 + r] = rk * (a3[r] - mean * c0[r]); vv[16 + r] = rstd * (a3[4 + r] - mean * c1[r]); q4[r] = a3[8 + r] - mean * c2[r]; }
    }
    }
    BAR();
    if (m == 0) {
      RELANE();
      v4u kc0 = {pk2(kk[0], kk[1]), pk2(kk[2], kk[3]), pk2(kk[4], kk[5]), pk2(kk[6], kk[7])};
      v4u kc1 = {pk2(kk[8], kk[9]), pk2(kk[10], kk[11]), pk2(kk[12], kk[13]), pk2(kk[14], kk[15])};
      v4u kc2 = {pk2(kk[16], kk[17]), pk2(kk[18], kk[19]), 0u, 0u};
      const unsigned r_ = h * 144 + 16 + 32 * w + tok;
      sts128(L, L_K + r_ * 48, kc0); sts128(L, L_K + r_ * 48 + 16, kc1); sts128(L, L_K + r_ * 48 + 32, kc2);
      v4u vc[5];
#pragma unroll
      for (int i = 0; i < 5; ++i) { vc[i] = v4u{__float_as_uint(vv[4 * i]), __float_as_uint(vv[4 * i + 1]), __float_as_uint(vv[4 * i + 2]), __float_as_uint(vv[4 * i + 3])}; sts128(L, L_V + r_ * 80 + i * 16, vc[i]); }
      if (w == 3 && tok >= 16) {
        const unsigned hs = WS_HALO + (((l >> 1) * 16 + rg) * 16 + cg) * 4096, hv = (h * 16 + (tok - 16)) * 16;
        stg_sc1(rs, hv, hs, kc0); stg_sc1(rs, hv + 512, hs, kc1); stg_sc1(rs, hv + 1024, hs, kc2);
#pragma unroll
        for (int i = 0; i < 5; ++i) stg_sc1(rs, hv + 1536 + i * 512, hs, vc[i]);
      }
    } else {
      RELANE();
      unsigned ph[20];
      phi_map(kk, ph);
      const unsigned t_ = 32 * w + tok, key = (t_ & 7) ^ (((t_ >> 3) & 1) << 2), rb = L_SCR + (h * 128 + t_) * 128;
#pragma unroll
      for (int c = 0; c < 5; ++c) sts128(L, rb + ((c ^ key) << 4), v4u{ph[4 * c], ph[4 * c + 1], ph[4 * c + 2], ph[4 * c + 3]});
      sts128(L, rb + ((5 ^ key) << 4), v4u{pk2(vv[0], vv[1]), pk2(vv[2], vv[3]), pk2(vv[4], vv[5]), pk2(vv[6], vv[7])});
      sts128(L, rb + ((6 ^ key) << 4), v4u{pk2(vv[8], vv[9]), pk2(vv[10], vv[11]), pk2(vv[12], vv[13]), pk2(vv[14], vv[15])});
      sts128(L, rb + ((7 ^ key) << 4), v4u{pk2(vv[16], vv[17]), pk2(vv[18], vv[19]), 0x00003F80u, 0u});
      BAR();
      const int hh = w >> 1, dt = w & 1, g = lane >> 4, q = (lane >> 2) & 3, p4 = lane & 3;
      const unsigned k0 = q ^ ((g & 1) << 2), rb0 = L_SCR + (hh * 128 + 8 * g + q) * 128;
      const unsigned cB = dt ? 7 : 5 + (p4 >> 1);
      const unsigned aA0 = rb0 + ((p4 ^ k0) << 4), aA2 = rb0 + ((4 ^ k0) << 4) + (p4 & 1) * 8, aB = rb0 + ((cB ^ k0) << 4) + (p4 & 1) * 8;
      f32x4 d0 = {}, d1 = {}, d2 = {};
#pragma unroll
      for (int ks = 0; ks < 4; ++ks) {
        const unsigned o = ks * 4096;
        v2u x0 = ldstr(L, aA0 + o), x1 = ldstr(L, (aA0 ^ 64) + 512 + o), y0 = ldstr(L, aA0 + 8 + o), y1 = ldstr(L, (aA0 ^ 64) + 520 + o);
        v2u z0 = ldstr(L, aA2 + o), z1 = ldstr(L, (aA2 ^ 64) + 512 + o), b0 = ldstr(L, aB + o), b1 = ldstr(L, (aB ^ 64) + 512 + o);
        const v4u bb = {b0.x, b0.y, b1.x, b1.y};
        d0 = mfma16(v4u{x0.x, x0.y, x1.x, x1.y}, bb, d0);
        d1 = mfma16(v4u{y0.x, y0.y, y1.x, y1.y}, bb, d1);
        d2 = mfma16(v4u{z0.x, z0.y, z1.x, z1.y}, bb, d2);
      }
      const int d = 16 * dt + (lane & 15);
      const unsigned e0 = pk2(d2[0], d2[1]), e1 = pk2(d2[2], d2[3]);
      auto s0 = __builtin_amdgcn_permlane16_swap(e0, e0, false, false);
      auto s1 = __builtin_amdgcn_permlane16_swap(e1, e1, false, false);
      const unsigned ks_ = WS_KV + (((l >> 1) * 16 + cg) * 16 + rg) * 4096;
      if (d <= 20) {
        stg_sc1(rs, ((hh * 5 + g) * 24 + d) * 16, ks_, v4u{pk2(d0[0], d0[1]), pk2(d0[2], d0[3]), pk2(d1[0], d1[1]), pk2(d1[2], d1[3])});
        if (g == 0) stg_sc1(rs, ((hh * 5 + 4) * 24 + d) * 16, ks_, v4u{e0, e1, s0[1], s1[1]});
      }
    }
    { RELANE();
    STAMP(4 + 12 * l);
    f32x16 a0 = {};
    const unsigned csb = L_CS + (m * 2 + h) * 256;
#pragma unroll
    for (int s = 0; s < 40; ++s) a0 = mfma32(lds128(L, L_P0 + l16 + s * 1024), xf[s], a0);
    { const float rq = rstd * (m ? LOG2E : LOG2E * 0.22360679774997896f);
#pragma unroll
      for (int i = 0; i < 4; ++i) {
        f32x4 c0 = __builtin_bit_cast(f32x4, lds128(L, csb + i * 16));
#pragma unroll
        for (int r = 0; r < 4; ++r) qq[4 * i + r] = rq * (a0[4 * i + r] - mean * c0[r]);
      }
#pragma unroll
      for (int r = 0; r < 4; ++r) qq[16 + r] = rq * q4[r];
    }
    STAMP(5 + 12 * l);
    }
    if (m == 0) {
      RELANE();
      unsigned qp[10];
#pragma unroll
      for (int i = 0; i < 10; ++i) qp[i] = pk2(qq[2 * i], qq[2 * i + 1]);
      BAR();
      if (w == 0) {
        v4u hd[4] = {};
        if (rg > 0) {
          const unsigned hs = WS_HALO + (((l >> 1) * 16 + rg - 1) * 16 + cg) * 4096;
          unsigned sp = 0, mm;
          do {
            FENCE();
            mm = 0;
#pragma unroll
            for (int i = 0; i < 4; ++i) hd[i] = ldg_sc1(rs, l16 + i * 1024, hs);
#pragma unroll
            for (int i = 0; i < 4; ++i) mm = mx4(mm, hd[i]);
          } while (__builtin_amdgcn_ballot_w64(mm == SENT) != 0 && !give_up(sp, p.ws));
        }
        const unsigned r_ = ((lane >> 4) & 1) * 144 + (lane & 15);
        sts128(L, L_K + r_ * 48 + h * 16, hd[0]);
        sts128(L, h ? L_V + r_ * 80 : L_K + r_ * 48 + 32, hd[1]);
        sts128(L, L_V + r_ * 80 + 16 + h * 16, hd[2]);
        sts128(L, L_V + r_ * 80 + 48 + h * 16, hd[3]);
      }
      const unsigned r0 = h * 144 + 2 + 32 * w + tok;
      float lg[8];
#pragma unroll
      for (int j = 0; j < 8; ++j) {
        const unsigned a = L_K + r0 * 48 + (7 - j) * 96;
        v4u c0 = lds128(L, a), c1 = lds128(L, a + 16); v2u c2 = lds64(L, a + 32);
        float s0 = dot2(c0.x, qp[0], 0.f), s1 = dot2(c0.y, qp[1], 0.f);
        s0 = dot2(c0.z, qp[2], s0); s1 = dot2(c0.w, qp[3], s1); s0 = dot2(c1.x, qp[4], s0); s1 = dot2(c1.y, qp[5], s1);
        s0 = dot2(c1.z, qp[6], s0); s1 = dot2(c1.w, qp[7], s1); s0 = dot2(c2.x, qp[8], s0); s1 = dot2(c2.y, qp[9], s1);
        lg[j] = (rg * 128 + 32 * w + tok >= 2 * j) ? s0 + s1 : -1e30f;
      }
      float mx = lg[0];
#pragma unroll
      for (int j = 1; j < 8; ++j) mx = fmaxf(mx, lg[j]);
      float sum = 0.f;
#pragma unroll
      for (int j = 0; j < 8; ++j) { lg[j] = __builtin_amdgcn_exp2f(lg[j] - mx); sum += lg[j]; }
      const float inv = __builtin_amdgcn_rcpf(sum);
      f32x2 oa[10] = {};
#pragma unroll
      for (int j = 0; j < 8; ++j) {
        const unsigned a = L_V + r0 * 80 + (7 - j) * 160;
        const f32x2 pj = {lg[j], lg[j]};
#pragma unroll
        for (int i = 0; i < 5; ++i) { f32x4 v = __builtin_bit_cast(f32x4, lds128(L, a + i * 16));
          oa[2 * i] = __builtin_elementwise_fma(pj, f32x2{v[0], v[1]}, oa[2 * i]); oa[2 * i + 1] = __builtin_elementwise_fma(pj, f32x2{v[2], v[3]}, oa[2 * i + 1]); }
      }
#pragma unroll
      for (int i = 0; i < 10; ++i) { o[2 * i] = oa[i][0] * inv; o[2 * i + 1] = oa[i][1] * inv; }
    } else {
      RELANE();
      const int hh = tid >= 120, s_ = (tid % 120) / 24, d_ = tid % 24;
      const bool valid = tid < 240 && d_ <= 20;
      const unsigned kvv = (valid ? tid : 0) * 16, ks_ = WS_KV + ((l >> 1) * 16 + cg) * 65536;
      v4u kp[16];
#pragma unroll
      for (int r = 0; r < 16; ++r) kp[r] = ldg_sc1(rs, kvv, ks_ + r * 4096);
      FENCE();
      __builtin_amdgcn_sched_barrier(0);
      unsigned ph[20];
      phi_map(qq, ph);
      { unsigned mm = 0;
#pragma unroll
        for (int r = 0; r < 16; ++r) mm = mx4(mm, kp[r]);
        if (__builtin_expect(__builtin_amdgcn_ballot_w64(mm == SENT) != 0, 0)) {
          unsigned sp = 0;
          do {
            FENCE();
            mm = 0;
#pragma unroll
            for (int r = 0; r < 16; ++r) kp[r] = ldg_sc1(rs, kvv, ks_ + r * 4096);
#pragma unroll
            for (int r = 0; r < 16; ++r) mm = mx4(mm, kp[r]);
          } while (__builtin_amdgcn_ballot_w64(mm == SENT) != 0 && !give_up(sp, p.ws));
        }
      }
      float sm[8] = {};
      unsigned clo = 0x00003F80u, chi = 0x3F800000u;
      asm volatile("" : "+s"(clo), "+s"(chi));
#pragma unroll
      for (int r = 0; r < 16; ++r)
#pragma unroll
        for (int j = 0; j < 4; ++j) { sm[2 * j] = dot2(kp[r][j], clo, sm[2 * j]); sm[2 * j + 1] = dot2(kp[r][j], chi, sm[2 * j + 1]); }
      if (valid) sts128(L, L_KVA + (((s_ * 2 + (d_ >> 4)) * 2 + hh) * 16 + (d_ & 15)) * 16, v4u{pk2(sm[0], sm[1]), pk2(sm[2], sm[3]), pk2(sm[4], sm[5]), pk2(sm[6], sm[7])});
      BAR();
      const bool match = ((tok >> 2) & 1) == h;
      const unsigned aa = L_KVA + h * 256 + (4 * (tok >> 3) + (tok & 3)) * 16;
      f32x16 n0 = {}, n1 = {};
#pragma unroll
      for (int s = 0; s < 5; ++s) {
        v4u f0 = lds128(L, aa + s * 1024), f1 = lds128(L, aa + s * 1024 + 512);
        if (!match) { f0 = v4u{0u, 0u, 0u, 0u}; f1 = f0; }
        const v4u bb = {ph[4 * s], ph[4 * s + 1], ph[4 * s + 2], ph[4 * s + 3]};
        n0 = mfma32(f0, bb, n0); n1 = mfma32(f1, bb, n1);
      }
      const float inv = __builtin_amdgcn_rcpf(n1[4] + 1e-6f);
#pragma unroll
      for (int i = 0; i < 16; ++i) o[i] = n0[i] * inv;
#pragma unroll
      for (int i = 0; i < 4; ++i) o[16 + i] = n1[i] * inv;
    }
    {
      RELANE();
      STAMP(6 + 12 * l);
      const unsigned os = WS_O + l * XB + ((rg * 8 + 2 * w) * 4 + (cg >> 2)) * 5120 + (cg & 3) * 256;
      const unsigned ov = (tok >> 4) * 20480 + (tok & 15) * 16;
      stg_sc1(rs, ov + h * 2048, os, v4u{pk2(o[0], o[1]), pk2(o[2], o[3]), pk2(o[4], o[5]), pk2(o[6], o[7])});
      stg_sc1(rs, ov + h * 2048 + 1024, os, v4u{pk2(o[8], o[9]), pk2(o[10], o[11]), pk2(o[12], o[13]), pk2(o[14], o[15])});
      const unsigned e0 = pk2(o[16], o[17]), e1 = pk2(o[18], o[19]);
      auto s0 = __builtin_amdgcn_permlane32_swap(e0, e0, false, false);
      auto s1 = __builtin_amdgcn_permlane32_swap(e1, e1, false, false);
      if (h == 0) stg_sc1(rs, ov + 4096, os, v4u{e0, e1, s0[1], s1[1]});
    }
    DRAIN();
    RELANE();
    STAMP(7 + 12 * l);
    v4u of[20];
    unsigned osb[4];
#pragma unroll
    for (int i = 0; i < 4; ++i) {
      osb[i] = WS_O + l * XB + ((rg * 8 + half * 4 + ((w + 1 + i) & 3)) * 4 + w) * 5120;
#pragma unroll
      for (int s = 0; s < 5; ++s) of[i * 5 + s] = ldg_sc1(rs, l16 + (s & 3) * 1024, osb[i] + (s >> 2) * 4096);
    }
    FENCE();
    __builtin_amdgcn_sched_barrier(0);
    f32x4 acc[5];
#pragma unroll
    for (int i = 0; i < 4; ++i) {
      unsigned mm = 0;
#pragma unroll
      for (int s = 0; s < 5; ++s) mm = mx4(mm, of[i * 5 + s]);
      if (__builtin_expect(__builtin_amdgcn_ballot_w64(mm == SENT) != 0, 0)) {
        unsigned sp = 0;
        do {
          FENCE();
          mm = 0;
#pragma unroll
          for (int s = 0; s < 5; ++s) of[i * 5 + s] = ldg_sc1(rs, l16, osb[i] + s * 1024);
#pragma unroll
          for (int s = 0; s < 5; ++s) mm = mx4(mm, of[i * 5 + s]);
        } while (__builtin_amdgcn_ballot_w64(mm == SENT) != 0 && !give_up(sp, p.ws));
      }
      if (i == 0) { BAR(); STAMP(8 + 12 * l); }
#pragma unroll
      for (int ft = 0; ft < 5; ++ft) acc[ft] = f32x4{0.f, 0.f, 0.f, 0.f};
#pragma unroll
      for (int s = 0; s < 5; ++s)
#pragma unroll
        for (int ft = 0; ft < 5; ++ft) acc[ft] = mfma16(wo[s * 5 + ft], of[i * 5 + s], acc[ft]);
      if (i < 3) {
        const unsigned ra = L_RED + (((w + 1 + i) & 3) * 3 + i) * 5120 + l16;
#pragma unroll
        for (int ft = 0; ft < 5; ++ft) sts128(L, ra + ft * 1024, __builtin_bit_cast(v4u, acc[ft]));
      }
    }
    BAR();
    STAMP(9 + 12 * l);
#pragma unroll
    for (int i = 0; i < 3; ++i)
#pragma unroll
      for (int ft = 0; ft < 5; ++ft) acc[ft] += __builtin_bit_cast(f32x4, lds128(L, L_RED + (w * 3 + i) * 5120 + ft * 1024 + l16));
#pragma unroll
    for (int ft = 0; ft < 5; ++ft) {
      xr[2 * ft] = pk2(lo16(xr[2 * ft]) + acc[ft][0], hi16(xr[2 * ft]) + acc[ft][1]);
      xr[2 * ft + 1] = pk2(lo16(xr[2 * ft + 1]) + acc[ft][2], hi16(xr[2 * ft + 1]) + acc[ft][3]);
    }
    if (l < 3) {
      publish_x(rs, WS_X + (l + 1) * XB, rg, cg, w, lane, xr);
      BAR();
      STAMP(10 + 12 * l);
      DRAIN();
    } else {
      GAS v2u* dst = (GAS v2u*)(p.out + (size_t)(rg * 128 + half * 64 + w * 16 + (lane & 15)) * 640 + c8 * 80 + (lane >> 4) * 20);
#pragma unroll
      for (int i = 0; i < 5; ++i) dst[i] = v2u{xr[2 * i], xr[2 * i + 1]};
    }
    STAMP(11 + 12 * l);
  }
}

extern "C" void kernel_launch(void* const* d_in, const int* in_sizes, int n_in, void* d_out, int out_size, void* d_ws, size_t ws_size, hipStream_t stream) {
  Params p;
  p.x = (const u16*)d_in[0]; p.wqA = (const u16*)d_in[1]; p.woA = (const u16*)d_in[2];
  p.wqB = (const u16*)d_in[4]; p.wphi = (const u16*)d_in[5]; p.woB = (const u16*)d_in[6];
  p.out = (u16*)d_out; p.ws = (unsigned char*)d_ws;
  (void)hipFuncSetAttribute((const void*)k_main, hipFuncAttributeMaxDynamicSharedMemorySize, LDS_BYTES);
  k_prep<<<256, 256, 0, stream>>>(p);
  k_main<<<256, 256, LDS_BYTES, stream>>>(p);
}
```

```cpp
#include <hip/hip_runtime.h>
#include <stdint.h>

typedef unsigned short u16;
typedef unsigned v4u __attribute__((ext_vector_type(4)));
typedef unsigned v2u __attribute__((ext_vector_type(2)));
typedef float f32x2 __attribute__((ext_vector_type(2)));
typedef float f32x4 __attribute__((ext_vector_type(4)));
typedef float f32x16 __attribute__((ext_vector_type(16)));
typedef __bf16 bf16x8 __attribute__((ext_vector_type(8)));
typedef __bf16 bf16x2 __attribute__((ext_vector_type(2)));
typedef short s16x4 __attribute__((ext_vector_type(4)));
#define LAS __attribute__((address_space(3)))
#define GAS __attribute__((address_space(1)))
typedef __amdgpu_buffer_rsrc_t rsrc_t;
#define DEV __device__ __forceinline__

#define DSC1 " sc1"
#define POIS 2
constexpr unsigned WQ_SZ = 153600, WO_SZ = 102400, XB = 2621440, STB = 131072;
constexpr unsigned WS_CTRL = 0;
constexpr unsigned WS_FLAG = 4096;
constexpr unsigned WS_WQ = WS_FLAG + 2 * 256 * 128;
constexpr unsigned WS_WO = WS_WQ + 32 * WQ_SZ;
constexpr unsigned WS_CSP = WS_WO + 16 * WO_SZ;
constexpr unsigned WS_ST0 = WS_CSP + 2 * 16 * 16 * 512;
constexpr unsigned WS_X = WS_ST0 + STB;
constexpr unsigned WS_O = WS_X + 4 * XB;
constexpr unsigned WS_KV = WS_O + 4 * XB;
constexpr unsigned WS_HALO = WS_KV + 2 * 256 * 4096;
constexpr unsigned WS_ST1 = WS_HALO + 2 * 256 * 4096;
constexpr unsigned WS_END = WS_ST1 + 3 * STB;
constexpr unsigned WS_STAMP = 40u << 20;
constexpr unsigned WS_TOTAL = WS_STAMP + 256 * 4 * 64 * 8;
constexpr unsigned KSTEP = 2816, L_P0 = 112640, L_CS = 153600, L_KVA = L_CS + 4096;
constexpr unsigned L_SCR = 73216;
constexpr unsigned L_K = L_SCR, L_V = L_SCR + 2 * 144 * 48;
constexpr unsigned L_RED = 90112;
constexpr unsigned LDS_BYTES = L_KVA + 5120;
constexpr unsigned SENT = 0xFFFFFFFFu;
constexpr unsigned CODE_BYTES = 24576;
constexpr int SC1 = 16;
constexpr float LOG2E = 1.4426950408889634f;

struct Params { const u16 *x, *wqA, *woA, *wqB, *wphi, *woB; u16* out; unsigned char* ws; };

DEV unsigned pk2(float a, float b) { bf16x2 v = {(__bf16)a, (__bf16)b}; return __builtin_bit_cast(unsigned, v); }
DEV float lo16(unsigned u) { return __builtin_bit_cast(float, u << 16); }
DEV float hi16(unsigned u) { return __builtin_bit_cast(float, u & 0xffff0000u); }
DEV unsigned f2u(float f) { return __builtin_bit_cast(unsigned, f); }
DEV float u2f(unsigned u) { return __builtin_bit_cast(float, u); }
DEV unsigned umax(unsigned a, unsigned b) { return __builtin_elementwise_max(a, b); }
DEV unsigned umin(unsigned a, unsigned b) { return __builtin_elementwise_min(a, b); }
DEV float dot2(unsigned a, unsigned b, float c) { return __builtin_amdgcn_fdot2_f32_bf16(__builtin_bit_cast(bf16x2, a), __builtin_bit_cast(bf16x2, b), c, false); }
DEV f32x2 pkfma(f32x2 a, f32x2 b, f32x2 c) { return __builtin_elementwise_fma(a, b, c); }
DEV f32x2 pkmul(f32x2 a, f32x2 b) { return a * b; }
DEV f32x2 pkadd(f32x2 a, f32x2 b) { return a + b; }
DEV f32x16 mfma32(v4u a, v4u b, f32x16 c) { return __builtin_amdgcn_mfma_f32_32x32x16_bf16(__builtin_bit_cast(bf16x8, a), __builtin_bit_cast(bf16x8, b), c, 0, 0, 0); }
DEV f32x4 mfma16(v4u a, v4u b, f32x4 c) { return __builtin_amdgcn_mfma_f32_16x16x32_bf16(__builtin_bit_cast(bf16x8, a), __builtin_bit_cast(bf16x8, b), c, 0, 0, 0); }
DEV v4u lds128(LAS unsigned char* L, unsigned a) { return *(LAS v4u*)(L + a); }
DEV v2u lds64(LAS unsigned char* L, unsigned a) { return *(LAS v2u*)(L + a); }
DEV void sts128(LAS unsigned char* L, unsigned a, v4u v) { *(LAS v4u*)(L + a) = v; }
DEV v2u ldstr(LAS unsigned char* L, unsigned a) { return __builtin_bit_cast(v2u, __builtin_amdgcn_ds_read_tr16_b64_v4i16((LAS s16x4*)(L + a))); }
DEV v4u ldg(rsrc_t rs, unsigned voff, unsigned soff) { return __builtin_amdgcn_raw_buffer_load_b128(rs, voff, soff, 0); }
DEV v4u ldg_sc1(rsrc_t rs, unsigned voff, unsigned soff) { return __builtin_amdgcn_raw_buffer_load_b128(rs, voff, soff, SC1); }
DEV v2u ldg64_sc1(rsrc_t rs, unsigned voff, unsigned soff) { return __builtin_amdgcn_raw_buffer_load_b64(rs, voff, soff, SC1); }
DEV void stg_sc1(rsrc_t rs, unsigned voff, unsigned soff, v4u v) { __builtin_amdgcn_raw_buffer_store_b128(v, rs, voff, soff, SC1); }
DEV unsigned mx4(unsigned m, v4u d) { return umax(umax(umax(m, d.x), d.y), umax(d.z, d.w)); }
DEV unsigned max3u(unsigned a, unsigned b, unsigned c) { return umax(umax(a, b), c); }
DEV bool any_sent(unsigned m) { return __builtin_amdgcn_ballot_w64(m == SENT) != 0; }
#define BAR() asm volatile("s_waitcnt lgkmcnt(0)\n\ts_barrier" ::: "memory")
#define DRAIN() asm volatile("s_waitcnt vmcnt(0)" ::: "memory")
#define FENCE() asm volatile("" ::: "memory")
#define SB() __builtin_amdgcn_sched_barrier(0)
DEV void dma4(rsrc_t rs, unsigned voff, unsigned soff, unsigned ldsa) {
  asm volatile("s_mov_b32 m0, %0\n\ts_nop 0\n\tbuffer_load_dwordx4 %1, %2, %3 offen sc1 lds\n\tbuffer_load_dwordx4 %1, %2, %3 offen offset:1024 sc1 lds\n\t"
               "buffer_load_dwordx4 %1, %2, %3 offen offset:2048 sc1 lds\n\tbuffer_load_dwordx4 %1, %2, %3 offen offset:3072 sc1 lds"
               :: "s"(__builtin_amdgcn_readfirstlane(ldsa)), "v"(voff), "s"(rs), "s"(__builtin_amdgcn_readfirstlane(soff)) : "memory");
}
DEV void dma1(rsrc_t rs, unsigned voff, unsigned soff, unsigned ldsa) {
  asm volatile("s_mov_b32 m0, %0\n\ts_nop 0\n\tbuffer_load_dwordx4 %1, %2, %3 offen sc1 lds"
               :: "s"(__builtin_amdgcn_readfirstlane(ldsa)), "v"(voff), "s"(rs), "s"(__builtin_amdgcn_readfirstlane(soff)) : "memory");
}
DEV void dma_range(rsrc_t rs, unsigned l16, unsigned wsrc, int w, int k0, int k1) {
  int k = k0 + 4 * w;
  for (; k + 4 <= k1; k += 16) dma4(rs, l16, wsrc + k * 1024, k * 1024);
  for (; k < k1; ++k) dma1(rs, l16, wsrc + k * 1024, k * 1024);
}
struct Tmo { unsigned char* ws; unsigned epoch; };
DEV bool give_up(unsigned& sp, const Tmo& tm) {
  ++sp;
  if (sp > 8) __builtin_amdgcn_s_sleep(2);
  if ((sp & 63u) == 0u) {
    GAS unsigned* t = (GAS unsigned*)(tm.ws + WS_CTRL);
    if (sp >= (1u << 16)) { __hip_atomic_store(t, tm.epoch, __ATOMIC_RELAXED, __HIP_MEMORY_SCOPE_AGENT); return true; }
    if (__builtin_amdgcn_readfirstlane(__hip_atomic_load(t, __ATOMIC_RELAXED, __HIP_MEMORY_SCOPE_AGENT)) == tm.epoch) return true;
  }
  return false;
}
#define MAPV 1
#define XW 10
DEV int map_rg(int b) { return ((b & 7) >> 1) * 4 + (b >> 6); }
DEV int map_cg(int b) { return (b & 1) * 8 + ((b >> 3) & 7); }
DEV int o_half(int cg) { return (cg >> 2) & 1; }
DEV int o_c8(int cg) { return (cg >> 3) * 4 + (cg & 3); }
DEV int xfeat(int c, int j) { int c8 = c / 10, l = c % 10; return 80 * c8 + (l < 8 ? 20 * (l >> 1) + 8 * (l & 1) + j : 40 * (l - 8) + 20 * (j >> 2) + 16 + (j & 3)); }
DEV int ocol(int cgp, int s, int j) { return 40 * cgp + (s < 4 ? 20 * (s >> 1) + 8 * (s & 1) + j : 20 * (j >> 2) + 16 + (j & 3)); }
DEV int fresh_lane() { int l; asm volatile("v_mbcnt_lo_u32_b32 %0, -1, 0\n\tv_mbcnt_hi_u32_b32 %0, -1, %0" : "=v"(l)); return l; }
#define RELANE() const int lane = fresh_lane(), h = lane >> 5, tok = lane & 31, tid = w * 64 + lane; const unsigned l16 = lane * 16; (void)h; (void)tok; (void)tid; (void)l16
#define STAMP(i) do {} while (0)
#define LDS_ALLOC LDS_BYTES
#define CNT(i) do {} while (0)

DEV void publish_x(rsrc_t rs, unsigned xbuf, unsigned stbuf, int rg, int cg, int w, int lane, const unsigned (&xr)[10]) {
  const int g = lane >> 4, tok = lane & 15, half = o_half(cg), c8 = o_c8(cg);
  const unsigned sb = xbuf + ((rg * 4 + half * 2 + (w >> 1)) * 40 + 5 * c8) * 1024;
  const unsigned vb = ((w & 1) * 16 + tok) * 16;
  stg_sc1(rs, vb + g * 1024, sb, v4u{xr[0], xr[1], xr[2], xr[3]});
  stg_sc1(rs, vb + g * 1024 + 512, sb, v4u{xr[4], xr[5], xr[6], xr[7]});
  auto s0 = __builtin_amdgcn_permlane16_swap(xr[8], xr[8], false, false);
  auto s1 = __builtin_amdgcn_permlane16_swap(xr[9], xr[9], false, false);
  if (!(g & 1)) stg_sc1(rs, vb + 4096 + (g >> 1) * 512, sb, v4u{xr[8], xr[9], s0[1], s1[1]});
  SB();
  float sa = 0.f, sq = 0.f;
#pragma unroll
  for (int i = 0; i < 10; ++i) { sa = dot2(xr[i], 0x3F803F80u, sa); sq = dot2(xr[i], xr[i], sq); }
  { auto a = __builtin_amdgcn_permlane16_swap(f2u(sa), f2u(sa), false, false); sa = u2f(a[0]) + u2f(a[1]);
    auto b = __builtin_amdgcn_permlane16_swap(f2u(sq), f2u(sq), false, false); sq = u2f(b[0]) + u2f(b[1]); }
  { auto a = __builtin_amdgcn_permlane32_swap(f2u(sa), f2u(sa), false, false); sa = u2f(a[0]) + u2f(a[1]);
    auto b = __builtin_amdgcn_permlane32_swap(f2u(sq), f2u(sq), false, false); sq = u2f(b[0]) + u2f(b[1]); }
  if (g == 0) __builtin_amdgcn_raw_buffer_store_b64(v2u{f2u(sa), f2u(sq)}, rs, tok * 8, stbuf + ((rg * 8 + c8) * 128 + half * 64 + w * 16) * 8, SC1);
}
DEV void load_x_in(const u16* x, int rg, int cg, int w, int lane, unsigned (&xr)[10]) {
  const int g = lane >> 4, tok = lane & 15;
  const GAS v2u* src = (const GAS v2u*)(x + (size_t)(rg * 128 + o_half(cg) * 64 + w * 16 + tok) * 640 + o_c8(cg) * 80 + g * 20);
#pragma unroll
  for (int i = 0; i < 5; ++i) { v2u t = src[i]; xr[2 * i] = t.x; xr[2 * i + 1] = t.y; }
}

DEV void poison_x(rsrc_t rs, unsigned xbuf, unsigned stbuf, int rg, int cg, int w, int lane) {
  const int g = lane >> 4, tok = lane & 15, half = o_half(cg), c8 = o_c8(cg);
  const unsigned sb = xbuf + ((rg * 4 + half * 2 + (w >> 1)) * 40 + 5 * c8) * 1024, vb = ((w & 1) * 16 + tok) * 16;
  const v4u P = {SENT, SENT, SENT, SENT};
  stg_sc1(rs, vb + g * 1024, sb, P); stg_sc1(rs, vb + g * 1024 + 512, sb, P); stg_sc1(rs, vb + 4096 + (g >> 1) * 512, sb, P);
  __builtin_amdgcn_raw_buffer_store_b64(v2u{SENT, SENT}, rs, tok * 8, stbuf + ((rg * 8 + c8) * 128 + half * 64 + w * 16) * 8, SC1);
}
DEV void poison_o(rsrc_t rs, unsigned obuf, int rg, int cg, int w, int lane) {
  const int h = lane >> 5, tok = lane & 31;
  const unsigned os = obuf + ((rg * 8 + 2 * w) * 4 + (cg >> 2)) * 5120 + (cg & 3) * 256, ov = (tok >> 4) * 20480 + (tok & 15) * 16;
  const v4u P = {SENT, SENT, SENT, SENT};
  stg_sc1(rs, ov + h * 2048, os, P); stg_sc1(rs, ov + h * 2048 + 1024, os, P); stg_sc1(rs, ov + 4096, os, P);
}
DEV void wait_flags(rsrc_t rs, unsigned voff, unsigned soff, const Tmo& tm) {
  unsigned sp = 0, v;
  do { FENCE(); v = __builtin_amdgcn_raw_buffer_load_b32(rs, voff, soff, SC1); } while (__builtin_amdgcn_ballot_w64(v != tm.epoch) != 0 && !give_up(sp, tm));
}
DEV void build_wq_read(LAS unsigned char* L, unsigned stage, int w, int lane, v4u (&fr)[3]) {
  const int gam = lane >> 4, q = (lane >> 2) & 3, pp = lane & 3, a = 2 * (gam & 1) + (pp >> 1), h = pp & 1;
  const int eoff = (w < 3) ? 40 * w + 20 * h + 4 * a : (a == 0 ? 40 : a == 1 ? 80 : 0) + 20 * h + 16;
#pragma unroll
  for (int it = 0; it < 3; ++it) {
    const int kap = 2 * it + (gam >> 1), kc = kap < 4 ? kap : 4;
    const unsigned ad = stage + (kc * 8 + q) * 256 + eoff * 2;
    const v2u t0 = ldstr(L, ad), t1 = ldstr(L, ad + 1024);
    fr[it] = v4u{t0.x, t0.y, t1.x, t1.y};
  }
}
DEV float sum8(v4u v, float c) { c = dot2(v.x, 0x3F803F80u, c); c = dot2(v.y, 0x3F803F80u, c); c = dot2(v.z, 0x3F803F80u, c); return dot2(v.w, 0x3F803F80u, c); }
DEV void build_wq_store(rsrc_t rs, int m, int rg, int cg, int w, int lane, int r0, int r1, const v4u (&fr)[3]) {
  const int row = lane & 31, hi = lane >> 5;
  const unsigned tb = w == 0 ? L_P0 : (w - 1) * 1024, ts = w == 0 ? 1024 : KSTEP, th = w == 3 ? 384 : 512;
  const bool in = row >= r0 && row < r1;
#pragma unroll
  for (int it = 0; it < 3; ++it) {
    const int c = 5 * rg + 2 * it + hi;
    stg_sc1(rs, (in && (it < 2 || !hi)) ? (c >> 1) * ts + (c & 1) * th + row * 16 : 0x80000000u, WS_WQ + (m * 16 + cg) * WQ_SZ + tb, fr[it]);
  }
  float cs = sum8(fr[1], sum8(fr[0], 0.f));
  cs = hi ? cs : sum8(fr[2], cs);
  auto t = __builtin_amdgcn_permlane32_swap(f2u(cs), f2u(cs), false, false);
  __builtin_amdgcn_raw_buffer_store_b32(f2u(u2f(t[0]) + u2f(t[1])), rs, (in && !hi) ? row * 4 : 0x80000000u, WS_CSP + ((m * 16 + cg) * 16 + rg) * 512 + w * 128, SC1);
}
DEV void load_cs(LAS unsigned char* L, rsrc_t rs, int m, int cg, int tid) {
  if (tid < 128) {
    float cs = 0.f;
#pragma unroll
    for (int r = 0; r < 16; ++r) cs += u2f(__builtin_amdgcn_raw_buffer_load_b32(rs, tid * 4 + (r & 7) * 512, WS_CSP + (m * 16 + cg) * 8192 + (r >> 3) * 4096, SC1));
    const unsigned ch = pk2(cs, 0.f) & 0xffffu, cl = pk2(cs - lo16(ch), 0.f) & 0xffffu;
    sts128(L, L_CS + m * 2048 + tid * 16, v4u{ch | (cl << 16), ch, 0u, 0u});
  }
}

DEV void poll_probe(rsrc_t rs, unsigned voff, unsigned soff, const Tmo& tm) {
  unsigned sp = 0, v;
  do { FENCE(); v = __builtin_amdgcn_raw_buffer_load_b32(rs, voff, soff, SC1); } while (any_sent(v) && !give_up(sp, tm));
}
DEV void probe_issue(unsigned& d, rsrc_t rs, unsigned voff, unsigned soff) {
  asm volatile("buffer_load_dword %0, %1, %2, %3 offen sc1" : "=v"(d) : "v"(voff), "s"(rs), "s"(soff) : "memory");
}
template <int N> DEV void probe_wait(unsigned& d) { asm volatile("s_waitcnt vmcnt(%1)" : "+v"(d) : "i"(N) : "memory"); }
template <int I> struct IC { static constexpr int v = I; };
template <int I, int N, class F> DEV void sfor(F&& f) { if constexpr (I < N) { f(IC<I>{}); sfor<I + 1, N>(f); } }
#define SFOR(var, n) sfor<0, n>([&](auto var##_) __attribute__((always_inline)) { constexpr int var = decltype(var##_)::v;
#define SEND })
template <bool NORM, class F> DEV float phi_map(const float (&a)[20], float s, float post, unsigned (&ph)[20], F slot) {
  float mx = __builtin_fmaxf(a[0], a[1]), mn = __builtin_fminf(a[0], a[1]);
#pragma unroll
  for (int i = 2; i < 20; i += 2) { mx = __builtin_fmaxf(__builtin_fmaxf(mx, a[i]), a[i + 1]); mn = __builtin_fminf(__builtin_fminf(mn, a[i]), a[i + 1]); }
  const float bp = -s * mx, bn = s * mn, ns = -s;
  float sp = 0.f;
  SFOR(sg, 2)
    const f32x2 sc = sg ? f32x2{ns, ns} : f32x2{s, s}, bi = sg ? f32x2{bn, bn} : f32x2{bp, bp};
    f32x2 e[10];
    slot(IC<5 * sg>{});
#pragma unroll
    for (int i = 0; i < 10; ++i) e[i] = pkfma(f32x2{a[2 * i], a[2 * i + 1]}, sc, bi);
    SB();
    SFOR(i, 10)
      if constexpr (i == 3 || i == 7) slot(IC<5 * sg + 1 + (i >> 2)>{});
      e[i] = f32x2{__builtin_amdgcn_exp2f(e[i][0]), __builtin_amdgcn_exp2f(e[i][1])};
    SEND;
    f32x2 s0 = pkadd(e[0], e[1]), s1 = pkadd(e[2], e[3]), s2 = pkadd(e[4], e[5]);
    s0 = pkadd(s0, e[6]); s1 = pkadd(s1, e[7]); s2 = pkadd(s2, e[8]); s0 = pkadd(s0, e[9]);
    slot(IC<5 * sg + 3>{});
    if constexpr (!NORM && sg == 0) {
#pragma unroll
      for (int i = 0; i < 10; ++i) ph[i] = pk2(e[i][0], e[i][1]);
    }
    s0 = pkadd(pkadd(s0, s1), s2);
    const float sm = s0[0] + s0[1];
    if constexpr (NORM || sg == 1) {
      const float r = __builtin_amdgcn_rcpf(sm) * (NORM ? post : sp);
#pragma unroll
      for (int i = 0; i < 10; ++i) { f32x2 t = pkmul(e[i], f32x2{r, r}); ph[10 * sg + i] = pk2(t[0], t[1]); }
    }
    sp = sg ? sp : sm;
    slot(IC<5 * sg + 4>{});
  SEND;
  return sp;
}

#define BARE() asm volatile("s_barrier" ::: "memory")
template <int OFF> DEV void dsr(v4u& d, unsigned addr) { asm volatile("ds_read_b128 %0, %1 offset:%2" : "=v"(d) : "v"(addr), "i"(OFF)); }
template <int R, int OFF, bool COH> DEV void lda(rsrc_t rs, unsigned voff, unsigned soff) {
  if constexpr (COH) asm volatile("buffer_load_dwordx4 a[%c0:%c1], %2, %3, %4 offen offset:%5 sc1" :: "i"(R), "i"(R + 3), "v"(voff), "s"(rs), "s"(soff), "i"(OFF) : "memory");
  else asm volatile("buffer_load_dwordx4 a[%c0:%c1], %2, %3, %4 offen offset:%5" :: "i"(R), "i"(R + 3), "v"(voff), "s"(rs), "s"(soff), "i"(OFF) : "memory");
}
DEV void sbump(unsigned& so) { asm volatile("s_add_u32 %0, %0, 0x1000" : "+s"(so) :: "scc"); }
template <bool Z, int R> DEV void mf32a(f32x16& c, const v4u& a) {
  if constexpr (Z) asm volatile("v_mfma_f32_32x32x16_bf16 %0, %1, a[%c2:%c3], 0" : "=&v"(c) : "v"(a), "i"(R), "i"(R + 3));
  else asm volatile("v_mfma_f32_32x32x16_bf16 %0, %1, a[%c2:%c3], %0" : "+v"(c) : "v"(a), "i"(R), "i"(R + 3));
}
template <bool Z> DEV void mf32v(f32x16& c, const v4u& a, const v4u& b) {
  if constexpr (Z) asm volatile("v_mfma_f32_32x32x16_bf16 %0, %1, %2, 0" : "=&v"(c) : "v"(a), "v"(b));
  else asm volatile("v_mfma_f32_32x32x16_bf16 %0, %1, %2, %0" : "+v"(c) : "v"(a), "v"(b));
}
template <bool Z, int R> DEV void mf16a(f32x4& c, const v4u& b) {
  if constexpr (Z) asm volatile("v_mfma_f32_16x16x32_bf16 %0, a[%c2:%c3], %1, 0" : "=&v"(c) : "v"(b), "i"(R), "i"(R + 3));
  else asm volatile("v_mfma_f32_16x16x32_bf16 %0, a[%c2:%c3], %1, %0" : "+v"(c) : "v"(b), "i"(R), "i"(R + 3));
}
template <bool Z> DEV void mf16v(f32x4& c, const v4u& a, const v4u& b) {
  if constexpr (Z) asm volatile("v_mfma_f32_16x16x32_bf16 %0, %1, %2, 0" : "=&v"(c) : "v"(a), "v"(b));
  else asm volatile("v_mfma_f32_16x16x32_bf16 %0, %1, %2, %0" : "+v"(c) : "v"(a), "v"(b));
}
template <int N> DEV void vmc() { asm volatile("s_waitcnt vmcnt(%0)" :: "i"(N) : "memory"); }
template <int I> DEV void dmaw(rsrc_t rw, unsigned l16, unsigned wb) {
  asm volatile("s_or_b32 m0, %0, %1\n\ts_nop 0\n\tbuffer_load_dwordx4 %2, %3, m0 offen" DSC1 " lds" :: "s"(wb), "i"(I * 4096), "v"(l16), "s"(rw) : "memory", "scc");
}
template <int N> DEV void lgkm() { asm volatile("s_waitcnt lgkmcnt(%0)" :: "i"(N)); }
#define MFMA_SETTLE() asm volatile("s_nop 7\n\ts_nop 3")
DEV bool any_nan(float x) { return __builtin_amdgcn_ballot_w64(x != x) != 0; }
#define PFD 3

template <int S, int NW, int VM, int VI> DEV void p2pair(f32x16& a0, v4u& w0, v4u& w1, unsigned bq, unsigned bcs, rsrc_t r, unsigned l16, unsigned so) {
#define P2_M0 "s_waitcnt lgkmcnt(%c[nw])\n\tv_mfma_f32_32x32x16_bf16 %[c], %[w0], a[%c[r0]:%c[r1]], "
#define P2_R0 "\n\tds_read_b128 %[w0], %[a0] offset:%c[o0]\n\t"
#define P2_M1 "v_mfma_f32_32x32x16_bf16 %[c], %[w1], a[%c[r2]:%c[r3]], %[c]"
#define P2_R1 "\n\tds_read_b128 %[w1], %[bq] offset:%c[o1]"
#define P2_IN [nw] "i"(NW), [r0] "i"(4 * S), [r1] "i"(4 * S + 3), [r2] "i"(4 * S + 4), [r3] "i"(4 * S + 7), [a0] "v"(S + 6 == 40 ? bcs : bq), [bq] "v"(bq), \
              [o0] "i"(S + 6 < 40 ? (S + 6) * 1024 : 0), [o1] "i"((S + 7) * 1024), [rs] "s"(r), [l16] "v"(l16), [so] "s"(so), [d0] "i"(160 + 4 * VI), [d1] "i"(163 + 4 * VI), \
              [vo] "i"((VI & 3) * 1024), [dm] "i"(VI * 4096)
#define P2_LD "buffer_load_dwordx4 a[%c[d0]:%c[d1]], %[l16], %[rs], %[so] offen offset:%c[vo] sc1\n\t"
#define P2_DMA "s_or_b32 m0, %[so], %[dm]\n\t"
#define P2_DMB "buffer_load_dwordx4 %[l16], %[rs], m0 offen" DSC1 " lds\n\t"
  if constexpr (S == 0) {
    static_assert(S != 0 || VM == 0, "");
    asm volatile(P2_M0 "0" P2_R0 P2_M1 P2_R1 : [c] "=&v"(a0), [w0] "+v"(w0), [w1] "+v"(w1) : P2_IN : "memory");
  } else if constexpr (S + 7 < 40) {
    if constexpr (VM == 0) asm volatile(P2_M0 "%[c]" P2_R0 P2_M1 P2_R1 : [c] "+v"(a0), [w0] "+v"(w0), [w1] "+v"(w1) : P2_IN : "memory");
    else if constexpr (VM == 1) asm volatile(P2_M0 "%[c]" P2_R0 P2_LD P2_M1 P2_R1 : [c] "+v"(a0), [w0] "+v"(w0), [w1] "+v"(w1) : P2_IN : "memory");
    else asm volatile(P2_M0 "%[c]\n\t" P2_DMA "ds_read_b128 %[w0], %[a0] offset:%c[o0]\n\t" P2_DMB P2_M1 P2_R1 : [c] "+v"(a0), [w0] "+v"(w0), [w1] "+v"(w1) : P2_IN : "memory", "scc");
  } else if constexpr (S + 6 == 40) {
    static_assert(S + 6 != 40 || VM == 0, "");
    asm volatile(P2_M0 "%[c]" P2_R0 P2_M1 : [c] "+v"(a0), [w0] "+v"(w0), [w1] "+v"(w1) : P2_IN : "memory");
  } else {
    static_assert(VM == 0, "");
    asm volatile(P2_M0 "%[c]\n\t" P2_M1 : [c] "+v"(a0), [w0] "+v"(w0), [w1] "+v"(w1) : P2_IN : "memory");
  }
}
template <bool Z, int RA, int RB> DEV void mf16aa(f32x4& c) {
  if constexpr (Z) asm volatile("v_mfma_f32_16x16x32_bf16 %0, a[%c1:%c2], a[%c3:%c4], 0" : "=v"(c) : "i"(RA), "i"(RA + 3), "i"(RB), "i"(RB + 3));
  else asm volatile("v_mfma_f32_16x16x32_bf16 %0, a[%c1:%c2], a[%c3:%c4], %0" : "+v"(c) : "i"(RA), "i"(RA + 3), "i"(RB), "i"(RB + 3));
}
template <int RB> DEV void mf16va(f32x4& c, const v4u& a) { asm volatile("v_mfma_f32_16x16x32_bf16 %0, %1, a[%c2:%c3], %0" : "+v"(c) : "v"(a), "i"(RB), "i"(RB + 3)); }
#define DSW(addr, off, val) asm volatile("ds_write_b128 %0, %1 offset:%2" :: "v"(addr), "v"(val), "i"(off) : "memory")
#define ADM 10
#define ODM 5
#define GDM 7
#define DLY_O 2
#define DLY_X 2
#define KPS 8

__global__ void __launch_bounds__(256, 1) k_main(Params p) {
  extern __shared__ __attribute__((aligned(16))) unsigned char smem[];
  LAS unsigned char* L = (LAS unsigned char*)smem;
  const int b = __builtin_amdgcn_workgroup_id_x(), w = __builtin_amdgcn_readfirstlane(__builtin_amdgcn_workitem_id_x() >> 6);
  const int rg = map_rg(b), cg = map_cg(b), half = o_half(cg), c8 = o_c8(cg);
  const rsrc_t rs = __builtin_amdgcn_make_buffer_rsrc(p.ws, 0, WS_TOTAL, 0x00020000);
  asm volatile("" ::: "a255");
  unsigned ad[12];
  auto addresses = [&](int m) __attribute__((always_inline)) {
    RELANE();
    if (m == 0) {
      const unsigned r_ = h * 144 + 16 + 32 * w + tok, q_ = ((lane >> 4) & 1) * 144 + (lane & 15);
      ad[0] = L_K + r_ * 48; ad[1] = L_V + r_ * 80; ad[2] = (h * 16 + (tok - 16)) * 16;
      ad[3] = L_K + q_ * 48 + h * 16; ad[4] = h ? L_V + q_ * 80 : L_K + q_ * 48 + 32; ad[5] = L_V + q_ * 80 + 16 + h * 16;
      ad[6] = ad[7] = ad[8] = ad[9] = ad[10] = ad[11] = 0;
    } else {
      const int g = lane >> 4, hh = w >> 1, dt = w & 1, q = (lane >> 2) & 3, p4 = lane & 3, r = lane & 15;
      const unsigned t_ = 32 * w + tok, key = (t_ & 7) ^ (((t_ >> 3) & 1) << 2);
      ad[0] = L_SCR + (h * 128 + t_) * 128 + (key << 4);
      const unsigned k0 = q ^ ((g & 1) << 2), rb0 = L_SCR + (hh * 128 + 8 * g + q) * 128, cB = dt ? 7 : 5 + (p4 >> 1);
      ad[1] = rb0 + ((p4 ^ k0) << 4); ad[2] = rb0 + ((4 ^ k0) << 4) + (p4 & 1) * 8; ad[3] = rb0 + ((cB ^ k0) << 4) + (p4 & 1) * 8;
      ad[4] = ((hh * 5 + g) * 24 + 16 * dt + r) * 16; ad[5] = ((hh * 5 + 4) * 24 + 16 * dt + r) * 16;
#pragma unroll
      for (int j = 0; j < 4; ++j) { const int slot = (w < 3 ? 64 * w : 176) + 16 * j + r, q24 = slot / 24, d_ = slot % 24, hk = q24 >= 5, s_ = q24 - 5 * hk;
        ad[6 + j] = L_KVA + (((s_ * 2 + (d_ >> 4)) * 2 + hk) * 16 + (d_ & 15)) * 16 + g * 8; }
      ad[10] = L_KVA + h * 256 + (4 * (tok >> 3) + (tok & 3)) * 16;
      ad[11] = g * 4096 + r * 16;
    }
    asm volatile("" : "+v"(ad[0]), "+v"(ad[1]), "+v"(ad[2]), "+v"(ad[3]), "+v"(ad[4]), "+v"(ad[5]), "+v"(ad[6]), "+v"(ad[7]), "+v"(ad[8]), "+v"(ad[9]), "+v"(ad[10]), "+v"(ad[11]));
  };
  unsigned xr[10];
  Tmo tm;
  { RELANE();
    const int l = 0; (void)l;
    STAMP(0);
    constexpr unsigned SG_B = L_P0 + 2048, SG_O = SG_B + 10240, SG_P = SG_O + 10240, OOR = 0x80000000u;
    const int j = (tid >> 4) & 7, cc = lane & 15, seg = (cc * 13) >> 6, g = lane >> 4;
    v4u ra[3], rb[3], ro[3], rp;
    const unsigned wcol = (seg * 640 + 40 * cg + 8 * (cc - 5 * seg)) * 2;
    unsigned wrow[3];
#pragma unroll
    for (int i = 0; i < 3; ++i) {
      const int kap = 2 * i + (w >> 1) < 4 ? 2 * i + (w >> 1) : 4, lc = 5 * (rg & 1) + kap;
      const int t8 = lc >> 3, rb0 = 80 * (rg >> 1) + (1 - t8) * (20 * (lc >> 1) + 8 * (lc & 1)) + t8 * (40 * (lc - 8) + 16);
      wrow[i] = (rb0 + j + t8 * (16 * (j >> 2))) * 3840 + wcol;
    }
    { const rsrc_t rA = __builtin_amdgcn_make_buffer_rsrc((void*)p.wqA, 0, 640 * 1920 * 2, 0x00020000);
#pragma unroll
      for (int i = 0; i < 3; ++i) ra[i] = ldg(rA, wrow[i], 0); }
    load_x_in(p.x, rg, cg, w, lane, xr);
    const unsigned ep = __builtin_amdgcn_raw_buffer_load_b32(rs, 0, WS_CTRL + 128, SC1);
    FENCE(); SB();
    { const rsrc_t rB = __builtin_amdgcn_make_buffer_rsrc((void*)p.wqB, 0, 640 * 1920 * 2, 0x00020000);
      const rsrc_t rO = __builtin_amdgcn_make_buffer_rsrc((void*)(half ? p.woB : p.woA), 0, 640 * 640 * 2, 0x00020000);
      const rsrc_t rP = __builtin_amdgcn_make_buffer_rsrc((void*)(p.wphi + cg * 800), 0, 1600, 0x00020000);
#pragma unroll
      for (int i = 0; i < 3; ++i) rb[i] = ldg(rB, wrow[i], 0);
#pragma unroll
      for (int i = 0; i < 3; ++i) { const int so = 2 * i + (w >> 1) < 4 ? 2 * i + (w >> 1) : 4, t4 = so >> 2;
        ro[i] = ldg(rO, (40 * rg + (1 - t4) * (20 * (so >> 1) + 8 * (so & 1)) + t4 * 16 + j + t4 * (16 * (j >> 2))) * 1280 + (80 * c8 + 8 * (cc < 10 ? cc : 0)) * 2, 0); }
      rp = ldg(rP, tid * 16, 0);
      const rsrc_t rc = __builtin_amdgcn_make_buffer_rsrc((void*)(__builtin_amdgcn_s_getpc() & ~(unsigned long long)127), 0, CODE_BYTES, 0x00020000);
      const v4u pf = ldg(rc, l16, (((b >> 3) * 4 + w) & 31) * 1024);
      FENCE(); SB();
      asm volatile("" :: "v"(ra[0]), "v"(ra[1]), "v"(ra[2]));
    FENCE(); SB();
    STAMP(53);
    const unsigned sa = tid * 16;
#pragma unroll
    for (int i = 0; i < 3; ++i) if (i < 2 || w < 2) sts128(L, sa + i * 4096, ra[i]);
    BAR();
    STAMP(54);
    { v4u fr[3];
      build_wq_read(L, 0, w, lane, fr);
      publish_x(rs, WS_X, WS_ST0, rg, cg, w, lane, xr);
      build_wq_store(rs, 0, rg, cg, w, lane, 0, w < 3 ? 32 : 24, fr); }
    tm.ws = p.ws; tm.epoch = __builtin_amdgcn_readfirstlane(ep) + 0x9E3779B1u;
    STAMP(55);
#pragma unroll
    for (int i = 0; i < 3; ++i) if (i < 2 || w < 2) { sts128(L, SG_B + sa + i * 4096, rb[i]); sts128(L, SG_O + sa + i * 4096, ro[i]); }
    if (w < 2) sts128(L, SG_P + sa, rp);
    asm volatile("" :: "v"(pf));
    }
    DRAIN();
    BAR();
    if (tid == 0) __builtin_amdgcn_raw_buffer_store_b32(tm.epoch, rs, 0, WS_FLAG + (cg * 16 + rg) * 128, SC1);
    STAMP(60);
    v4u fv[4], fb[3], fo[2];
    {
      const int sg = w >> 1, hw = w & 1;
      v4u bf[2], af[4];
#pragma unroll
      for (int ct = 0; ct < 2; ++ct) {
        const int q = (lane >> 2) & 3, pp = lane & 3, fcol = ct ? 16 : 4 * pp;
        const int d0 = 8 * g + q < 19 ? 8 * g + q : 19, d1 = 8 * g + 4 + q < 19 ? 8 * g + 4 + q : 19;
        const v2u t0 = ldstr(L, SG_P + hw * 800 + d0 * 40 + fcol * 2), t1 = ldstr(L, SG_P + hw * 800 + d1 * 40 + fcol * 2);
        bf[ct] = v4u{g < 3 ? t0.x : 0u, g < 3 ? t0.y : 0u, g < 2 ? t1.x : 0u, g < 2 ? t1.y : 0u};
      }
#pragma unroll
      for (int i = 0; i < 4; ++i) { const int r = (i < 2 ? 8 * (cc >> 2) : 32) + 4 * (i & 1) + (cc & 3); const unsigned a = SG_B + r * 256 + sg * 80 + hw * 40 + g * 16;
        const v2u t0 = lds64(L, a), t1 = lds64(L, a + 8); af[i] = v4u{t0.x, t0.y, t1.x, t1.y}; }
      build_wq_read(L, SG_B, w, lane, fb);
      { const int q = (lane >> 2) & 3, pp = lane & 3;
#pragma unroll
        for (int it = 0; it < 2; ++it) {
          const int pc = it * 16 + (tid >> 4), pcc = pc < 25 ? pc : 24, so = (pcc * 13) >> 6, ft = pcc - 5 * so;
          const unsigned a = SG_O + (so * 8 + q) * 256 + 40 * pp + 8 * ft;
          const v2u t0 = ldstr(L, a), t1 = ldstr(L, a + 1024);
          fo[it] = v4u{t0.x, t0.y, t1.x, t1.y};
        } }
      f32x4 d[8];
      asm volatile("s_nop 1" : "+v"(bf[0]), "+v"(bf[1]), "+v"(af[0]), "+v"(af[1]), "+v"(af[2]), "+v"(af[3]));
      SFOR(i, 8) mf16v<true>(d[i], af[i & 3], bf[i >> 2]); SEND;
      asm volatile("s_nop 7");
      SB();
#pragma unroll
      for (int i = 0; i < 4; ++i) fv[i] = v4u{pk2(d[2 * i][0], d[2 * i][1]), pk2(d[2 * i][2], d[2 * i][3]), pk2(d[2 * i + 1][0], d[2 * i + 1][1]), pk2(d[2 * i + 1][2], d[2 * i + 1][3])};
    }
    STAMP(58);
    { const int sg = w >> 1, hw = w & 1;
#pragma unroll
      for (int ct = 0; ct < 2; ++ct) {
        const int row = ct ? (sg ? 0 : 16) + 4 * hw + (cc & 3) : 8 * (cc >> 2) + 4 * hw + (cc & 3);
        const unsigned tb = ct ? 2048 : (sg ? 0 : L_P0), ts = (ct || sg) ? KSTEP : 1024, th = ct ? 384 : 512;
        const bool in = ct == 0 || cc < 4;
#pragma unroll
        for (int u = 0; u < 2; ++u) { const int c = 5 * rg + (u ? 4 : g);
          stg_sc1(rs, (in && (u == 0 || g == 0)) ? (c >> 1) * ts + (c & 1) * th + row * 16 : OOR, WS_WQ + (16 + cg) * WQ_SZ + tb, fv[2 * ct + u]); }
        float cs = sum8(fv[2 * ct], 0.f);
        cs = g ? cs : sum8(fv[2 * ct + 1], cs);
        { auto t = __builtin_amdgcn_permlane16_swap(f2u(cs), f2u(cs), false, false); cs = u2f(t[0]) + u2f(t[1]); }
        { auto t = __builtin_amdgcn_permlane32_swap(f2u(cs), f2u(cs), false, false); cs = u2f(t[0]) + u2f(t[1]); }
        __builtin_amdgcn_raw_buffer_store_b32(f2u(cs), rs, (in && g == 0) ? row * 4 : OOR, WS_CSP + ((16 + cg) * 16 + rg) * 512 + (ct ? 3 : sg) * 128, SC1);
      }
      if (w >= 2) build_wq_store(rs, 1, rg, cg, w, lane, w == 2 ? 0 : 8, w == 2 ? 32 : 16, fb);
#pragma unroll
      for (int it = 0; it < 2; ++it) { const int pc = it * 16 + (tid >> 4);
        stg_sc1(rs, pc < 25 ? pc * 1024 + cc * 16 : OOR, WS_WO + (half * 8 + c8) * WO_SZ + (rg >> 2) * 25600 + (rg & 3) * 256, fo[it]); }
    }
    poison_o(rs, WS_O, rg, cg, w, lane);
    stg_sc1(rs, l16, WS_KV + (cg * 16 + rg) * 4096 + w * 1024, v4u{SENT, SENT, SENT, SENT});
    stg_sc1(rs, l16, WS_HALO + (rg * 16 + cg) * 4096 + w * 1024, v4u{SENT, SENT, SENT, SENT});
    addresses(0);
    STAMP(61);
    if (w == 1) wait_flags(rs, (h == 0 ? cc * 16 + rg : cg * 16 + cc) * 128, WS_FLAG, tm);
    BAR();
    STAMP(62);
    const rsrc_t rw = __builtin_amdgcn_make_buffer_rsrc(p.ws + WS_WQ + cg * WQ_SZ, 0, WQ_SZ, 0x00020000);
    SFOR(i, 37) dmaw<i>(rw, l16, w * 1024); SEND;
    if (w < 2) dmaw<37>(rw, l16, w * 1024);
    float csv[16];
#pragma unroll
    for (int r = 0; r < 16; ++r) csv[r] = u2f(__builtin_amdgcn_raw_buffer_load_b32(rs, (tid & 127) * 4 + (r & 7) * 512, WS_CSP + cg * 8192 + (r >> 3) * 4096, SC1));
    FENCE(); SB();
    STAMP(63);
    FENCE(); SB();
    STAMP(57);
    { float cs = 0.f;
#pragma unroll
      for (int r = 0; r < 16; ++r) cs += csv[r];
      const unsigned ch = pk2(cs, 0.f) & 0xffffu, cl = pk2(cs - lo16(ch), 0.f) & 0xffffu;
      asm volatile("" :: "v"(cs));
      if (tid < 128) sts128(L, L_CS + tid * 16, v4u{ch | (cl << 16), ch, 0u, 0u}); }
    BAR();
    if (tid == 0) __builtin_amdgcn_raw_buffer_store_b32(tm.epoch, rs, 0, WS_FLAG + (256 + cg * 16 + rg) * 128, SC1);
  }

  for (int l = 0; l < 4; ++l) {
    const int m = l & 1;
    const rsrc_t rw = __builtin_amdgcn_make_buffer_rsrc(p.ws + WS_WQ + ((m ^ 1) * 16 + cg) * WQ_SZ, 0, l < 3 ? WQ_SZ : 0u, 0x00020000);
    const unsigned wb1k = w * 1024;
#define DM(i) do { SB(); dmaw<(i)>(rw, l16, wb1k); SB(); } while (0)
    unsigned wos;
#define LDWO(i) do { lda<160 + 4 * (i), ((i) & 3) * 1024, true>(rs, l16, wos); if constexpr (((i) & 3) == 3) sbump(wos); } while (0)
    v4u wo24, mfr;
    f32x16 a0, a1, a2, a3;
    float rstd, sd, o[20];
    {
      RELANE();
      STAMP(1 + 12 * l);
      const unsigned xs = WS_X + l * XB + (rg * 4 + w) * 40960;
      const unsigned sts = (l ? WS_ST1 + (l - 1) * STB : WS_ST0) + (rg * 8 * 128 + w * 32) * 8;
#define LDX(s) do { lda<4 * (s), ((s) & 3) * 1024, true>(rs, l16, xso); if constexpr (((s) & 3) == 3) sbump(xso); } while (0)
      const unsigned stv = h * 4096 + tok * 8;
      constexpr int NB = PFD + 1, HI = 23;
      constexpr int XR = 15 - XW;
      const unsigned b12 = l16, b3 = 2048 + h * 384 + tok * 16, b12h = b12 + HI * KSTEP, b3h = b3 + HI * KSTEP, bcs = L_CS + m * 2048 + tok * 16;
      unsigned f1 = 0;
      const unsigned f1v = (lane < 16 ? lane * 16 + rg : (lane < 32 ? cg : (c8 >> 2) * 8 + ((lane >> 4) & 1) * 4 + (c8 & 3)) * 16 + (lane & 15)) * 128;
      for (unsigned att = 0, fresh = 1;; fresh = 0) {
        v2u st[4];
        unsigned xso = xs;
        wos = WS_WO + ((m * 8 + c8) * 4 + w) * 25600;
        asm volatile("buffer_load_dwordx2 %0, %4, %5, %6 offen sc1\n\tbuffer_load_dwordx2 %1, %4, %5, %6 offen offset:1024 sc1\n\t"
                     "buffer_load_dwordx2 %2, %4, %5, %6 offen offset:2048 sc1\n\tbuffer_load_dwordx2 %3, %4, %5, %6 offen offset:3072 sc1"
                     : "=&v"(st[0]), "=&v"(st[1]), "=&v"(st[2]), "=&v"(st[3]) : "v"(stv), "s"(rs), "s"(sts) : "memory");
        SFOR(s, XW) SB(); LDX(s); if constexpr (s == 1 || s == 5) { SB(); if (fresh) BARE(); } SEND;
        FENCE(); SB();
        v4u wb[NB][3];
        auto rd = [&](auto s_, auto t_) __attribute__((always_inline)) {
          constexpr int s = decltype(s_)::v, t = decltype(t_)::v;
          if constexpr (s < 40) dsr<(s < HI ? s : s - HI) * KSTEP + (t == 1 ? 1024 : 0)>(wb[s % NB][t], t == 2 ? (s < HI ? b3 : b3h) : (s < HI ? b12 : b12h));
          else if constexpr (s == 40) dsr<(t + 1) * 512>(wb[s % NB][t], bcs);
        };
        SFOR(j, PFD) rd(IC<j>{}, IC<2>{}); rd(IC<j>{}, IC<1>{}); rd(IC<j>{}, IC<0>{}); SEND;
        asm volatile("s_waitcnt vmcnt(%4)" : "+v"(st[0]), "+v"(st[1]), "+v"(st[2]), "+v"(st[3]) : "i"(XW) : "memory");
        unsigned ms = max3u(max3u(st[0].x, st[0].y, st[1].x), max3u(st[1].y, st[2].x, st[2].y), umax(st[3].x, st[3].y));
        if (__builtin_expect(any_sent(ms), 0)) {
          unsigned sp = 0;
          do {
            FENCE();
#pragma unroll
            for (int i = 0; i < 4; ++i) st[i] = ldg64_sc1(rs, stv + i * 1024, sts);
            ms = max3u(max3u(st[0].x, st[0].y, st[1].x), max3u(st[1].y, st[2].x, st[2].y), umax(st[3].x, st[3].y));
          } while (any_sent(ms) && !give_up(sp, tm));
          lgkm<0>(); CNT(0);
          if (!any_sent(ms)) continue;
        }
        { const float sa = (u2f(st[0].x) + u2f(st[1].x)) + (u2f(st[2].x) + u2f(st[3].x)), sq = (u2f(st[0].y) + u2f(st[1].y)) + (u2f(st[2].y) + u2f(st[3].y));
          auto x0 = __builtin_amdgcn_permlane32_swap(f2u(sa), f2u(sa), false, false); auto x1 = __builtin_amdgcn_permlane32_swap(f2u(sq), f2u(sq), false, false);
          const float nm = (u2f(x0[0]) + u2f(x0[1])) * (-1.f / 640.f), var = __builtin_fmaxf((u2f(x1[0]) + u2f(x1[1])) * (1.f / 640.f) - nm * nm, 0.f) + 1e-5f;
          rstd = __builtin_amdgcn_rsqf(var); sd = var * rstd;
          const unsigned mh = pk2(nm, nm), ml = pk2(nm - lo16(mh), 0.f);
          mfr = v4u{h ? 0u : mh, h ? 0u : ml, 0u, 0u};
          asm volatile("" : "+v"(mfr), "+v"(rstd), "+v"(sd));
        }
        SFOR(s, 40)
          SB();
          if constexpr (s == 20) { if (l == 0) probe_issue(f1, rs, f1v, WS_FLAG + 256 * 128); SB(); }
          if constexpr (s == 25) {
            if (l == 0) {
              probe_wait<5>(f1);
              if (__builtin_amdgcn_ballot_w64(f1 != tm.epoch) != 0) wait_flags(rs, f1v, WS_FLAG + 256 * 128, tm);
            }
            if (att == 0) BARE();
            SB();
          }
          constexpr int c = s % NB;
          constexpr int issued = s <= XR ? XW + 2 * s : (15 + s < 40 ? 15 + s : 40);
          asm volatile("s_waitcnt vmcnt(%0) lgkmcnt(%1)" :: "i"(issued - 1 - s + (s > 25 ? s - 25 : 0)), "i"(3 * ((s + PFD <= 41 ? PFD : 41 - s) - 1)) : "memory");
          mf32a<s == 0, 4 * s>(a1, wb[c][0]);
          if constexpr (s == 0) STAMP(2 + 12 * l);
          rd(IC<s + PFD>{}, IC<2>{});
          if constexpr (s < XR) LDX(issued);
          SB();
          mf32a<s == 0, 4 * s>(a2, wb[c][1]);
          rd(IC<s + PFD>{}, IC<1>{});
          SB();
          mf32a<s == 0, 4 * s>(a3, wb[c][2]);
          rd(IC<s + PFD>{}, IC<0>{});
          if constexpr (s < 25) LDX(issued + (s < XR ? 1 : 0)); else LDWO(s - 25);
        SEND;
        SB();
        lgkm<0>();
        mf32v<false>(a1, wb[40 % NB][0], mfr); mf32v<false>(a3, wb[40 % NB][2], mfr); mf32v<false>(a2, wb[40 % NB][1], mfr);
        MFMA_SETTLE();
        SB();
        if (__builtin_expect(!any_nan(a1[0]), 1)) break;
        CNT(1);
        if (++att > 32) break;
      }
      STAMP(3 + 12 * l);
    }
    BARE();
    if (POIS == 3 && l < 3) { RELANE(); poison_o(rs, WS_O + (l + 1) * XB, rg, cg, w, lane); if (l > 0) poison_x(rs, WS_X + (l + 1) * XB, WS_ST1 + l * STB, rg, cg, w, lane); }
    if (l == 0) { RELANE(); poison_x(rs, WS_X + XB, WS_ST1, rg, cg, w, lane); }
#define P2VM(pp) ((pp) % 3 == 2 || (pp) == 0 || (pp) > 16 ? 0 : (((pp) - (pp) / 3 - 1) < 9 ? 1 : 0))
#define P2VI(pp) (P2VM(pp) == 1 ? 15 + ((pp) - (pp) / 3 - 1) : P2VM(pp) == 2 ? ((pp) - (pp) / 3 - 1) - 9 : 0)
#define P2PAIR(pp, nw) do { p2pair<2 * (pp), nw, P2VM(pp), P2VI(pp)>(a0, wq[(2 * (pp)) % 6], wq[(2 * (pp) + 1) % 6], bq, bcs, P2VM(pp) == 2 ? rw : rs, l16, P2VM(pp) == 2 ? wb1k : wos); \
                            if constexpr (P2VM(pp) == 1 && (P2VI(pp) & 3) == 3) sbump(wos); } while (0)
    constexpr int NDM2 = 0;
    if (m == 0) {
      RELANE();
      const unsigned bq = L_P0 + l16, bcs = L_CS + tok * 16;
      v4u wq[6], kc0, kc1, kc2, hd[4] = {};
      const unsigned hsrc = WS_HALO + (((l >> 1) * 16 + rg - 1) * 16 + cg) * 4096;
      const unsigned kwa = ad[0], vwa = ad[1];
#define VCH(i) ((i) < 4 ? v4u{f2u(a2[4 * (i)]), f2u(a2[4 * (i) + 1]), f2u(a2[4 * (i) + 2]), f2u(a2[4 * (i) + 3])} : v4u{f2u(a3[4]), f2u(a3[5]), f2u(a3[6]), f2u(a3[7])})
      SFOR(j, 6) dsr<j * 1024>(wq[j], bq); SEND;
      kc0 = v4u{pk2(a1[0], a1[1]), pk2(a1[2], a1[3]), pk2(a1[4], a1[5]), pk2(a1[6], a1[7])};
      kc1 = v4u{pk2(a1[8], a1[9]), pk2(a1[10], a1[11]), pk2(a1[12], a1[13]), pk2(a1[14], a1[15])};
      kc2 = v4u{pk2(a3[0], a3[1]), pk2(a3[2], a3[3]), f2u(rstd), 0u};
      unsigned qp[10];
      qp[8] = pk2(a3[8], a3[9]); qp[9] = pk2(a3[10], a3[11]);
      asm volatile("" : "+v"(kc0), "+v"(kc1), "+v"(kc2), "+v"(qp[8]), "+v"(qp[9]), "+v"(hd[0]), "+v"(hd[1]), "+v"(hd[2]), "+v"(hd[3]));
      SFOR(pp, 20)
        SB();
        constexpr int nwr = (pp - 1 >= 1 ? ((pp - 1 > 15 ? 15 : pp - 1) + 1) / 2 : 0) - (pp - 4 >= 1 ? ((pp - 4 > 15 ? 15 : pp - 4) + 1) / 2 : 0);
        P2PAIR(pp, (2 * pp + 6 <= 41 ? 4 : 39 - 2 * pp) + nwr);
        if constexpr (pp == 0) {
          if (w == 3 && tok >= 16) {
            const unsigned hs = hsrc + 65536, hv = ad[2];
            stg_sc1(rs, hv, hs, kc0); stg_sc1(rs, hv + 512, hs, kc1); stg_sc1(rs, hv + 1024, hs, kc2);
#pragma unroll
            for (int i = 0; i < 5; ++i) stg_sc1(rs, hv + 1536 + i * 512, hs, VCH(i));
          }
        }
        if constexpr (pp == 1) DSW(kwa, 0, kc0);
        if constexpr (pp == 3) DSW(kwa, 16, kc1);
        if constexpr (pp == 5) DSW(kwa, 32, kc2);
        if constexpr (pp >= 7 && pp < 16 && (pp & 1)) { constexpr int vi = pp >= 7 ? (pp - 7) / 2 : 0; DSW(vwa, vi * 16, VCH(vi)); }
        if constexpr (pp == 17) wo24 = ldg_sc1(rs, l16, wos);
        if constexpr (pp == 18) {
          if (w == 0 && rg > 0) {
#pragma unroll
            for (int i = 0; i < 4; ++i) hd[i] = ldg_sc1(rs, l16 + i * 1024, hsrc);
          }
        }
      SEND;
      SB();
      lgkm<0>();
      mf32v<false>(a0, wq[40 % 6], mfr);
      vmc<10>();
      BARE();
      unsigned ka = kwa - 14 * 48, va = vwa - 14 * 80;
      asm volatile("" : "+v"(ka), "+v"(va));
      const float fq = rstd * (LOG2E * 0.22360679774997896f);
      STAMP(5 + 12 * l);
      if (w == 0) {
        unsigned mm = 0;
#pragma unroll
        for (int i = 0; i < 4; ++i) mm = mx4(mm, hd[i]);
        if (__builtin_expect(any_sent(mm), 0)) {
          unsigned sp = 0;
          do {
            FENCE();
            mm = 0;
#pragma unroll
            for (int i = 0; i < 4; ++i) hd[i] = ldg_sc1(rs, l16 + i * 1024, hsrc);
#pragma unroll
            for (int i = 0; i < 4; ++i) mm = mx4(mm, hd[i]);
            CNT(2);
          } while (any_sent(mm) && !give_up(sp, tm));
        }
        sts128(L, ad[3], hd[0]);
        sts128(L, ad[4], hd[1]);
        sts128(L, ad[5], hd[2]);
        sts128(L, ad[5] + 32, hd[3]);
      }
      STAMP(4 + 12 * l);
      float lg[8], rk[8];
      v4u kb[8][3], vb[8][5];
      auto rdk = [&](auto j_) __attribute__((always_inline)) { constexpr int j = decltype(j_)::v;
#pragma unroll
        for (int i = 0; i < 3; ++i) kb[j][i] = lds128(L, ka + (7 - j) * 96 + i * 16); };
      auto rdv = [&](auto j_) __attribute__((always_inline)) { constexpr int j = decltype(j_)::v;
#pragma unroll
        for (int i = 0; i < 5; ++i) vb[j][i] = lds128(L, va + (7 - j) * 160 + i * 16); };
      SB();
      rdk(IC<0>{}); rdk(IC<1>{}); rdk(IC<2>{}); rdk(IC<3>{});
      SB();
#pragma unroll
      for (int i = 0; i < 8; ++i) qp[i] = pk2(a0[2 * i], a0[2 * i + 1]);
      SFOR(jj, 4)
        SB();
        if constexpr (jj < ADM) DM(NDM2 + jj);
        if constexpr (jj < 2) { rdk(IC<2 * jj + 4>{}); rdk(IC<2 * jj + 5>{}); } else rdv(IC<jj - 2>{});
        SB();
        float s0 = 0.f, s1 = 0.f;
#pragma unroll
        for (int i = 0; i < 10; ++i) { s0 = dot2(kb[2 * jj][i >> 2][i & 3], qp[i], s0); s1 = dot2(kb[2 * jj + 1][i >> 2][i & 3], qp[i], s1); }
        asm volatile("" :: "v"(kb[2 * jj][2]), "v"(kb[2 * jj + 1][2]));
        rk[2 * jj] = u2f(kb[2 * jj][2][2]); rk[2 * jj + 1] = u2f(kb[2 * jj + 1][2][2]);
        lg[2 * jj] = s0 * (fq * rk[2 * jj]); lg[2 * jj + 1] = s1 * (fq * rk[2 * jj + 1]);
      SEND;
      SB();
      if constexpr (4 < ADM) DM(NDM2 + 4);
      rdv(IC<2>{});
      SB();
      if (rg == 0 && w == 0) {
#pragma unroll
        for (int j = 1; j < 8; ++j) lg[j] = tok >= 2 * j ? lg[j] : -1e30f;
      }
      const float mx = __builtin_fmaxf(__builtin_fmaxf(__builtin_fmaxf(lg[0], lg[1]), __builtin_fmaxf(lg[2], lg[3])), __builtin_fmaxf(__builtin_fmaxf(lg[4], lg[5]), __builtin_fmaxf(lg[6], lg[7])));
#pragma unroll
      for (int j = 0; j < 8; ++j) lg[j] -= mx;
      SB();
#pragma unroll
      for (int j = 0; j < 8; ++j) lg[j] = __builtin_amdgcn_exp2f(lg[j]);
      SB();
      if constexpr (5 < ADM) DM(NDM2 + 5);
      rdv(IC<3>{});
      SB();
      const float inv = __builtin_amdgcn_rcpf(((lg[0] + lg[1]) + (lg[2] + lg[3])) + ((lg[4] + lg[5]) + (lg[6] + lg[7])));
#pragma unroll
      for (int j = 0; j < 8; ++j) lg[j] *= rk[j];
      f32x2 oa[10];
      SFOR(j, 8)
        SB();
        if constexpr (6 + j < ADM) DM(NDM2 + 6 + j);
        if constexpr (j < 4) rdv(IC<j + 4>{});
        SB();
        const float pj = lg[j];
        SFOR(i, 5)
          const f32x4 v = __builtin_bit_cast(f32x4, vb[j][i]);
          if constexpr (j == 0) { oa[2 * i] = pkmul(f32x2{pj, pj}, f32x2{v[0], v[1]}); oa[2 * i + 1] = pkmul(f32x2{pj, pj}, f32x2{v[2], v[3]}); }
          else { oa[2 * i] = pkfma(f32x2{pj, pj}, f32x2{v[0], v[1]}, oa[2 * i]); oa[2 * i + 1] = pkfma(f32x2{pj, pj}, f32x2{v[2], v[3]}, oa[2 * i + 1]); }
        SEND;
      SEND;
#pragma unroll
      for (int i = 0; i < 10; ++i) oa[i] = pkmul(oa[i], f32x2{inv, inv});
#pragma unroll
      for (int i = 0; i < 10; ++i) { o[2 * i] = oa[i][0]; o[2 * i + 1] = oa[i][1]; }
    } else {
      RELANE();
      const float sl = rstd * LOG2E;
      const int g = lane >> 4;
      {
        const unsigned rb = ad[0];
        float kk[20];
#pragma unroll
        for (int i = 0; i < 16; ++i) kk[i] = a1[i];
#pragma unroll
        for (int i = 0; i < 4; ++i) kk[16 + i] = a3[i];
        unsigned ph[20];
        phi_map<true>(kk, sl, rstd, ph, [&](auto i_) __attribute__((always_inline)) { constexpr int i = decltype(i_)::v; SB();
          if constexpr (i < ADM) dmaw<NDM2 + i>(rw, l16, wb1k);
          if (i == 0) sts128(L, rb ^ (5 << 4), v4u{pk2(a2[0], a2[1]), pk2(a2[2], a2[3]), pk2(a2[4], a2[5]), pk2(a2[6], a2[7])});
          if (i == 1) sts128(L, rb ^ (6 << 4), v4u{pk2(a2[8], a2[9]), pk2(a2[10], a2[11]), pk2(a2[12], a2[13]), pk2(a2[14], a2[15])});
          if (i == 2) sts128(L, rb ^ (7 << 4), v4u{pk2(a3[4], a3[5]), pk2(a3[6], a3[7]), pk2(sd, 0.f), 0u});
          if (i == 5) sts128(L, rb ^ (0 << 4), v4u{ph[0], ph[1], ph[2], ph[3]});
          if (i == 6) sts128(L, rb ^ (1 << 4), v4u{ph[4], ph[5], ph[6], ph[7]});
          SB(); });
#pragma unroll
        for (int c = 2; c < 5; ++c) sts128(L, rb ^ (c << 4), v4u{ph[4 * c], ph[4 * c + 1], ph[4 * c + 2], ph[4 * c + 3]});
      }
      const unsigned bq = L_P0 + l16, bcs = L_CS + 2048 + tok * 16;
      v4u wq[6];
      {
        const unsigned aA0 = ad[1], aA2 = ad[2], aB = ad[3], so0 = ad[4], so1 = ad[5];
        unsigned aA0x = (aA0 ^ 64) + 512, aA2x = (aA2 ^ 64) + 512, aBx = (aB ^ 64) + 512;
        const int d = 16 * (w & 1) + (lane & 15);
        asm volatile("" : "+v"(aA0x), "+v"(aA2x), "+v"(aBx));
        BAR();
        f32x4 d0, d1, d2;
        v2u t[4][8];
        auto trd = [&](auto k_) __attribute__((always_inline)) {
          constexpr int ks = decltype(k_)::v; constexpr unsigned o_ = ks * 4096;
          t[ks][0] = ldstr(L, aB + o_); t[ks][1] = ldstr(L, aBx + o_); t[ks][6] = ldstr(L, aA2 + o_); t[ks][7] = ldstr(L, aA2x + o_);
          t[ks][2] = ldstr(L, aA0 + o_); t[ks][3] = ldstr(L, aA0x + o_); t[ks][4] = ldstr(L, aA0 + 8 + o_); t[ks][5] = ldstr(L, aA0x + 8 + o_);
          SB();
        };
        trd(IC<0>{});
        SFOR(ks, 4)
          if constexpr (ks < 3) trd(IC<ks + 1>{});
          const v4u bb = {t[ks][0].x, t[ks][0].y, t[ks][1].x, t[ks][1].y};
          mf16v<ks == 0>(d2, v4u{t[ks][6].x, t[ks][6].y, t[ks][7].x, t[ks][7].y}, bb);
          mf16v<ks == 0>(d0, v4u{t[ks][2].x, t[ks][2].y, t[ks][3].x, t[ks][3].y}, bb);
          mf16v<ks == 0>(d1, v4u{t[ks][4].x, t[ks][4].y, t[ks][5].x, t[ks][5].y}, bb);
          SB();
        SEND;
        SFOR(j, 6) dsr<j * 1024>(wq[j], bq); SEND;
        asm volatile("s_nop 1");
        SB();
        const unsigned e0 = pk2(d2[0], d2[1]), e1 = pk2(d2[2], d2[3]);
        auto s0 = __builtin_amdgcn_permlane16_swap(e0, e0, false, false);
        auto s1 = __builtin_amdgcn_permlane16_swap(e1, e1, false, false);
        const unsigned ks_ = WS_KV + (((l >> 1) * 16 + cg) * 16 + rg) * 4096;
        if (d < 24) {
          stg_sc1(rs, so0, ks_, v4u{pk2(d0[0], d0[1]), pk2(d0[2], d0[3]), pk2(d1[0], d1[1]), pk2(d1[2], d1[3])});
          if (g == 0) stg_sc1(rs, so1, ks_, v4u{e0, e1, s0[1], s1[1]});
        }
      }
      STAMP(4 + 12 * l);
      const unsigned ks_ = WS_KV + ((l >> 1) * 16 + cg) * 65536 + (w < 3 ? w * 1024 : 11 * 256);
      const unsigned kvv = ad[11];
      v4u kp[16];
      unsigned kpr;
      const unsigned kps = WS_KV + ((l >> 1) * 16 + cg) * 65536, kpv = (lane >> 2) * 4096 + (120 + 96 + 16) * 16 + (lane & 3) * 4;
      SFOR(pp, 20) SB(); P2PAIR(pp, (2 * pp + 6 <= 41 ? 4 : 39 - 2 * pp)); if constexpr (pp == 16) wo24 = ldg_sc1(rs, l16, wos);
        if constexpr (pp == 17) probe_issue(kpr, rs, kpv, kps);
        if constexpr (pp >= 18 && 2 * (pp - 18) < KPS) { constexpr int r = 2 * (pp - 18); kp[r] = ldg_sc1(rs, kvv, ks_ + (r & 3) * 16384 + (r >> 2) * 256); kp[r + 1] = ldg_sc1(rs, kvv, ks_ + ((r + 1) & 3) * 16384 + (r >> 2) * 256); }
      SEND;
      SB();
      lgkm<0>();
      mf32v<false>(a0, wq[40 % 6], mfr);
#pragma unroll
      for (int r = (KPS > 4 ? 4 : KPS); r < 16; ++r) kp[r] = ldg_sc1(rs, kvv + (r >> 2) * 256, ks_ + (r & 3) * 16384);
      FENCE(); SB();
      STAMP(5 + 12 * l);
      unsigned ph[20];
      float eps;
      { float qq[20];
#pragma unroll
        for (int i = 0; i < 4; ++i) qq[16 + i] = a3[8 + i];
#pragma unroll
        for (int i = 0; i < 16; ++i) qq[i] = a0[i];
        eps = 1e-6f * phi_map<false>(qq, sl, 1.f, ph, [&](auto i_) __attribute__((always_inline)) {
          if constexpr (decltype(i_)::v == 4) {
            SB();
            probe_wait<16>(kpr);
            if (__builtin_expect(any_sent(kpr), 0)) {
              CNT(3);
              poll_probe(rs, kpv, kps, tm);
#pragma unroll
              for (int r = 0; r < 16; ++r) kp[r] = ldg_sc1(rs, kvv + (r >> 2) * 256, ks_ + (r & 3) * 16384);
            }
            FENCE(); SB();
          } });
      }
      v4u f[10], sel;
      { const int r = lane & 15;
#pragma unroll
        for (int j = 0; j < 4; ++j) sel[j] = r == 2 * j ? 0x00003F80u : r == 2 * j + 1 ? 0x3F800000u : 0u;
#pragma unroll
        for (int s = 0; s < 10; ++s) f[s] = v4u{0u, 0u, 0u, 0u};
      }
#pragma unroll
      for (int i = 0; i < 20; i += 4) asm volatile("" : "+v"(ph[i]), "+v"(ph[i + 1]), "+v"(ph[i + 2]), "+v"(ph[i + 3]));
      asm volatile("" : "+v"(sel));
      asm volatile("" : "+v"(f[0]), "+v"(f[1]), "+v"(f[2]), "+v"(f[3]), "+v"(f[4]), "+v"(f[5]), "+v"(f[6]), "+v"(f[7]), "+v"(f[8]), "+v"(f[9]));
      SB();
      f32x4 sm[4];
      for (unsigned sp = 0;;) {
        SFOR(r, 16) mf16v<(r & 3) == 0>(sm[r >> 2], sel, kp[r]); SEND;
        asm volatile("s_nop 7");
        SB();
        if (__builtin_expect(!any_nan((sm[0][0] + sm[1][0]) + (sm[2][0] + sm[3][0])), 1)) break;
        if (give_up(sp, tm)) break;
        CNT(1);
        FENCE();
#pragma unroll
        for (int r = 0; r < 16; ++r) kp[r] = ldg_sc1(rs, kvv + (r >> 2) * 256, ks_ + (r & 3) * 16384);
      }
      if (lane < 32) {
#pragma unroll
        for (int j = 0; j < 4; ++j) *(LAS v2u*)(L + ad[6 + j]) = v2u{pk2(sm[j][0], sm[j][1]), pk2(sm[j][2], sm[j][3])};
      }
      const bool match = ((tok >> 2) & 1) == h;
      const unsigned aa = ad[10];
      BAR();
      if (match) {
#pragma unroll
        for (int s = 0; s < 10; ++s) f[s ^ 1] = lds128(L, aa + (s ^ 1) * 512);
      }
      f32x16 n0, n1;
      SFOR(s, 5)
        const v4u bb = {ph[4 * s], ph[4 * s + 1], ph[4 * s + 2], ph[4 * s + 3]};
        mf32v<s == 0>(n1, f[2 * s + 1], bb); mf32v<s == 0>(n0, f[2 * s], bb);
      SEND;
      MFMA_SETTLE();
      SB();
      const float inv = __builtin_amdgcn_rcpf(n1[4] + eps);
#pragma unroll
      for (int i = 0; i < 2; ++i) { const f32x2 t = pkmul(f32x2{n1[2 * i], n1[2 * i + 1]}, f32x2{inv, inv}); o[16 + 2 * i] = t[0]; o[17 + 2 * i] = t[1]; }
#pragma unroll
      for (int i = 0; i < 8; ++i) { const f32x2 t = pkmul(f32x2{n0[2 * i], n0[2 * i + 1]}, f32x2{inv, inv}); o[2 * i] = t[0]; o[2 * i + 1] = t[1]; }
    }
    {
      RELANE();
      asm volatile("" : "+v"(wo24));
      const unsigned os = WS_O + l * XB + ((rg * 8 + 2 * w) * 4 + (cg >> 2)) * 5120 + (cg & 3) * 256;
      const unsigned ov = (tok >> 4) * 20480 + (tok & 15) * 16;
      stg_sc1(rs, ov + h * 2048, os, v4u{pk2(o[0], o[1]), pk2(o[2], o[3]), pk2(o[4], o[5]), pk2(o[6], o[7])});
      stg_sc1(rs, ov + h * 2048 + 1024, os, v4u{pk2(o[8], o[9]), pk2(o[10], o[11]), pk2(o[12], o[13]), pk2(o[14], o[15])});
      const unsigned e0 = pk2(o[16], o[17]), e1 = pk2(o[18], o[19]);
      auto s0 = __builtin_amdgcn_permlane32_swap(e0, e0, false, false);
      auto s1 = __builtin_amdgcn_permlane32_swap(e1, e1, false, false);
      if (h == 0) stg_sc1(rs, ov + 4096, os, v4u{e0, e1, s0[1], s1[1]});
      STAMP(6 + 12 * l);
      if (l < 3) {
        if (POIS == 1 || POIS == 2) poison_o(rs, WS_O + (l + 1) * XB, rg, cg, w, lane);
        if (POIS == 2 && l < 2) poison_x(rs, WS_X + (l + 2) * XB, WS_ST1 + (l + 1) * STB, rg, cg, w, lane);
        SFOR(i, ODM) DM(NDM2 + ADM + i); SEND;
        if (l == 0) {
          stg_sc1(rs, l16, WS_KV + (256 + cg * 16 + rg) * 4096 + w * 1024, v4u{SENT, SENT, SENT, SENT});
          stg_sc1(rs, l16, WS_HALO + (256 + rg * 16 + cg) * 4096 + w * 1024, v4u{SENT, SENT, SENT, SENT});
          load_cs(L, rs, 1, cg, tid);
        }
        if (l < 2) vmc<ODM + 7>(); else vmc<ODM + 3>();
      } else vmc<0>();
      if constexpr (DLY_O > 0) __builtin_amdgcn_s_sleep(DLY_O);
      BARE();
    }
    {
      RELANE();
      STAMP(7 + 12 * l);
      unsigned osb[4];
#pragma unroll
      for (int i = 0; i < 4; ++i) osb[i] = WS_O + l * XB + ((rg * 8 + half * 4 + ((w + 1 + i) & 3)) * 4 + w) * 5120;
      const unsigned l16b = l16 + 1024;
#define LDO(i, s) do { if constexpr ((s) == 0) lda<20 * (i), 0, true>(rs, l16, osb[i]); else lda<20 * (i) + 4 * (s), ((s) - 1) * 1024, true>(rs, l16b, osb[i]); } while (0)
      unsigned opr;
      const unsigned ops = WS_O + l * XB + ((rg * 8 + half * 4) * 4 + w) * 5120 + 4096, opv = (lane >> 4) * 20480 + ((lane >> 2) & 3) * 256 + (lane & 3) * 4;
      probe_issue(opr, rs, opv, ops);
      SFOR(s, 5) LDO(0, s); if constexpr (s == 1) BARE(); SEND;
      BARE();
      SFOR(s, 5) LDO(1, s); SEND;
      SB();
      f32x4 acc[2][5], part[15];
      f32x2 rsum[10];
#pragma unroll
      for (int i = 0; i < 10; ++i) rsum[i] = f32x2{lo16(xr[i]), hi16(xr[i])};
      asm volatile("" : "+v"(rsum[0]), "+v"(rsum[1]), "+v"(rsum[2]), "+v"(rsum[3]), "+v"(rsum[4]), "+v"(rsum[5]), "+v"(rsum[6]), "+v"(rsum[7]), "+v"(rsum[8]), "+v"(rsum[9]));
      const unsigned rwa = L_RED + l16;
      probe_wait<10>(opr);
      if (__builtin_expect(any_sent(opr), 0)) {
        CNT(4);
        poll_probe(rs, opv, ops, tm);
        SFOR(s, 5) LDO(0, s); SEND;
        SFOR(s, 5) LDO(1, s); SEND;
      }
      auto redo = [&](auto i_) __attribute__((always_inline)) {
        constexpr int i = decltype(i_)::v;
        unsigned sp = 0;
        do {
          CNT(1);
          SFOR(s, 5) LDO(i, s); SEND;
          vmc<0>();
          SFOR(j, 25) if constexpr (j < 24) mf16aa<(j < 5), 160 + 4 * j, 20 * i + 4 * (j / 5)>(acc[i & 1][j % 5]); else mf16va<20 * i + 16>(acc[i & 1][4], wo24); SEND;
          asm volatile("s_nop 7");
          SB();
        } while (any_nan(acc[i & 1][4][0]) && !give_up(sp, tm));
      };
      constexpr int NDO = NDM2 + ADM + ODM;
      static_assert(NDO + GDM <= 22 && NDM2 + ADM + ODM <= 17 && GDM <= 10 && ADM <= 10, "");
      SFOR(i, 4)
        SFOR(j, 25)
          constexpr int s = j / 5, ft = j % 5, WS0 = 4, wstep = 4;
          SB();
          constexpr int gi = 5 * i + s;
          if constexpr (ft == 0) vmc<(gi < 10 ? 9 : 19 - gi) + (gi > 20 - GDM ? gi - (20 - GDM) : 0)>();
          if constexpr (j < 24) mf16aa<s == 0, 160 + 4 * j, 20 * i + 4 * s>(acc[i & 1][ft]); else mf16va<20 * i + 16>(acc[i & 1][ft], wo24);
          if constexpr (i == 0 && j == 0) STAMP(8 + 12 * l);
          if constexpr (ft == 2) {
            if constexpr (i < 2) LDO(i + 2, s);
            else if constexpr (gi >= 20 - GDM) dmaw<NDO + gi - (20 - GDM)>(rw, l16, wb1k);
          }
          if constexpr (i > 0 && j == WS0 - 1) { asm volatile("s_nop 1"); SB(); if (__builtin_expect(any_nan(acc[(i - 1) & 1][4][0]), 0)) redo(IC<i - 1>{}); }
          if constexpr (i > 0 && j >= WS0 && j <= WS0 + 4 * wstep && (j - WS0) % wstep == 0)
            DSW(rwa + (((w + i) & 3) * 3 + (i - 1)) * 5120, ((j - WS0) / wstep) * 1024, acc[(i - 1) & 1][(j - WS0) / wstep]);
          if constexpr (i == 3 && j == 23) { SB(); BAR(); }
          if constexpr (i == 3 && j >= 23) {
#pragma unroll
            for (int q = (j - 23) * 8; q < (j - 23) * 8 + 8 && q < 15; ++q) part[q] = __builtin_bit_cast(f32x4, lds128(L, L_RED + w * 15360 + (q % 3) * 5120 + (q / 3) * 1024 + l16));
          }
        SEND;
      SEND;
      SB();
      STAMP(9 + 12 * l);
#pragma unroll
      for (int ft = 0; ft < 5; ++ft)
#pragma unroll
        for (int q = 0; q < 3; ++q) { rsum[2 * ft] = pkadd(rsum[2 * ft], f32x2{part[ft * 3 + q][0], part[ft * 3 + q][1]}); rsum[2 * ft + 1] = pkadd(rsum[2 * ft + 1], f32x2{part[ft * 3 + q][2], part[ft * 3 + q][3]}); }
      asm volatile("" : "+v"(rsum[8]), "+v"(rsum[9]));
      SB();
      if (__builtin_expect(any_nan(acc[1][4][0]), 0)) redo(IC<3>{});
#pragma unroll
      for (int ft = 0; ft < 5; ++ft) {
        const f32x2 lo = pkadd(rsum[2 * ft], f32x2{acc[1][ft][0], acc[1][ft][1]}), hi = pkadd(rsum[2 * ft + 1], f32x2{acc[1][ft][2], acc[1][ft][3]});
        xr[2 * ft] = pk2(lo[0], lo[1]); xr[2 * ft + 1] = pk2(hi[0], hi[1]);
      }
      if (l < 3) {
        publish_x(rs, WS_X + (l + 1) * XB, WS_ST1 + l * STB, rg, cg, w, lane, xr);
        STAMP(10 + 12 * l);
        if (POIS == 1 && l < 2) poison_x(rs, WS_X + (l + 2) * XB, WS_ST1 + (l + 1) * STB, rg, cg, w, lane);
        BARE();
        SFOR(i, 22 - NDO - GDM) DM(NDO + GDM + i); SEND;
        SFOR(i, 15) DM(22 + i); SEND;
        if (w < 2) DM(37);
        addresses(m ^ 1);
        vmc<15>();
        if constexpr (DLY_X > 0) __builtin_amdgcn_s_sleep(DLY_X);
        BARE();
      } else {
        GAS v2u* dst = (GAS v2u*)(p.out + (size_t)(rg * 128 + half * 64 + w * 16 + (lane & 15)) * 640 + c8 * 80 + (lane >> 4) * 20);
#pragma unroll
        for (int i = 0; i < 5; ++i) dst[i] = v2u{xr[2 * i], xr[2 * i + 1]};
        if (b == 0 && tid == 0) __builtin_amdgcn_raw_buffer_store_b32(tm.epoch, rs, 0, WS_CTRL + 128, SC1);
      }
      STAMP(11 + 12 * l);
    }
  }
}

extern "C" void kernel_launch(void* const* d_in, const int* in_sizes, int n_in, void* d_out, int out_size, void* d_ws, size_t ws_size, hipStream_t stream) {
  Params p;
  p.x = (const u16*)d_in[0]; p.wqA = (const u16*)d_in[1]; p.woA = (const u16*)d_in[2];
  p.wqB = (const u16*)d_in[4]; p.wphi = (const u16*)d_in[5]; p.woB = (const u16*)d_in[6];
  p.out = (u16*)d_out; p.ws = (unsigned char*)d_ws;
  (void)hipFuncSetAttribute((const void*)k_main, hipFuncAttributeMaxDynamicSharedMemorySize, LDS_ALLOC);
  k_main<<<256, 256, LDS_ALLOC, stream>>>(p);
}
```

```cpp
#include <hip/hip_runtime.h>
#include <stdint.h>

typedef unsigned short u16;
typedef unsigned v4u __attribute__((ext_vector_type(4)));
typedef unsigned v2u __attribute__((ext_vector_type(2)));
typedef float f32x2 __attribute__((ext_vector_type(2)));
typedef float f32x4 __attribute__((ext_vector_type(4)));
typedef float f32x16 __attribute__((ext_vector_type(16)));
typedef __bf16 bf16x8 __attribute__((ext_vector_type(8)));
typedef __bf16 bf16x2 __attribute__((ext_vector_type(2)));
typedef short s16x4 __attribute__((ext_vector_type(4)));
#define LAS __attribute__((address_space(3)))
#define GAS __attribute__((address_space(1)))
typedef __amdgpu_buffer_rsrc_t rsrc_t;
#define DEV __device__ __forceinline__

#define DSC1 " sc1"
#define POIS 2
constexpr unsigned WQ_SZ = 153600, WO_SZ = 102400, XB = 2621440, STB = 131072;
constexpr unsigned WS_CTRL = 0;
constexpr unsigned WS_FLAG = 4096;
constexpr unsigned WS_WQ = WS_FLAG + 2 * 256 * 128;
constexpr unsigned WS_WO = WS_WQ + 32 * WQ_SZ;
constexpr unsigned WS_CSP = WS_WO + 16 * WO_SZ;
constexpr unsigned WS_ST0 = WS_CSP + 2 * 16 * 16 * 512;
constexpr unsigned WS_X = WS_ST0 + STB;
constexpr unsigned WS_O = WS_X + 4 * XB;
constexpr unsigned WS_KV = WS_O + 4 * XB;
constexpr unsigned WS_HALO = WS_KV + 2 * 256 * 4096;
constexpr unsigned WS_ST1 = WS_HALO + 2 * 256 * 4096;
constexpr unsigned WS_END = WS_ST1 + 3 * STB;
constexpr unsigned WS_STAMP = 40u << 20;
constexpr unsigned WS_TOTAL = WS_STAMP + 256 * 4 * 64 * 8;
constexpr unsigned KSTEP = 2816, L_P0 = 112640, L_CS = 153600, L_KVA = L_CS + 4096;
constexpr unsigned L_SCR = 73216;
constexpr unsigned L_K = L_SCR, L_V = L_SCR + 2 * 144 * 48;
constexpr unsigned L_RED = 90112;
constexpr unsigned LDS_BYTES = L_KVA + 5120;
constexpr unsigned SENT = 0xFFFFFFFFu;
constexpr unsigned CODE_BYTES = 24576;
constexpr int SC1 = 16;
constexpr float LOG2E = 1.4426950408889634f;

struct Params { const u16 *x, *wqA, *woA, *wqB, *wphi, *woB; u16* out; unsigned char* ws; };

DEV unsigned pk2(float a, float b) { bf16x2 v = {(__bf16)a, (__bf16)b}; return __builtin_bit_cast(unsigned, v); }
DEV float lo16(unsigned u) { return __builtin_bit_cast(float, u << 16); }
DEV float hi16(unsigned u) { return __builtin_bit_cast(float, u & 0xffff0000u); }
DEV unsigned f2u(float f) { return __builtin_bit_cast(unsigned, f); }
DEV float u2f(unsigned u) { return __builtin_bit_cast(float, u); }
DEV unsigned umax(unsigned a, unsigned b) { return __builtin_elementwise_max(a, b); }
DEV unsigned umin(unsigned a, unsigned b) { return __builtin_elementwise_min(a, b); }
DEV float dot2(unsigned a, unsigned b, float c) { return __builtin_amdgcn_fdot2_f32_bf16(__builtin_bit_cast(bf16x2, a), __builtin_bit_cast(bf16x2, b), c, false); }
DEV f32x2 pkfma(f32x2 a, f32x2 b, f32x2 c) { return __builtin_elementwise_fma(a, b, c); }
DEV f32x2 pkmul(f32x2 a, f32x2 b) { return a * b; }
DEV f32x2 pkadd(f32x2 a, f32x2 b) { return a + b; }
DEV f32x16 mfma32(v4u a, v4u b, f32x16 c) { return __builtin_amdgcn_mfma_f32_32x32x16_bf16(__builtin_bit_cast(bf16x8, a), __builtin_bit_cast(bf16x8, b), c, 0, 0, 0); }
DEV f32x4 mfma16(v4u a, v4u b, f32x4 c) { return __builtin_amdgcn_mfma_f32_16x16x32_bf16(__builtin_bit_cast(bf16x8, a), __builtin_bit_cast(bf16x8, b), c, 0, 0, 0); }
DEV v4u lds128(LAS unsigned char* L, unsigned a) { return *(LAS v4u*)(L + a); }
DEV v2u lds64(LAS unsigned char* L, unsigned a) { return *(LAS v2u*)(L + a); }
DEV void sts128(LAS unsigned char* L, unsigned a, v4u v) { *(LAS v4u*)(L + a) = v; }
DEV v2u ldstr(LAS unsigned char* L, unsigned a) { return __builtin_bit_cast(v2u, __builtin_amdgcn_ds_read_tr16_b64_v4i16((LAS s16x4*)(L + a))); }
DEV v4u ldg(rsrc_t rs, unsigned voff, unsigned soff) { return __builtin_amdgcn_raw_buffer_load_b128(rs, voff, soff, 0); }
DEV v4u ldg_sc1(rsrc_t rs, unsigned voff, unsigned soff) { return __builtin_amdgcn_raw_buffer_load_b128(rs, voff, soff, SC1); }
DEV v2u ldg64_sc1(rsrc_t rs, unsigned voff, unsigned soff) { return __builtin_amdgcn_raw_buffer_load_b64(rs, voff, soff, SC1); }
DEV void stg_sc1(rsrc_t rs, unsigned voff, unsigned soff, v4u v) { __builtin_amdgcn_raw_buffer_store_b128(v, rs, voff, soff, SC1); }
DEV unsigned mx4(unsigned m, v4u d) { return umax(umax(umax(m, d.x), d.y), umax(d.z, d.w)); }
DEV unsigned max3u(unsigned a, unsigned b, unsigned c) { return umax(umax(a, b), c); }
DEV bool any_sent(unsigned m) { return __builtin_amdgcn_ballot_w64(m == SENT) != 0; }
#define BAR() asm volatile("s_waitcnt lgkmcnt(0)\n\ts_barrier" ::: "memory")
#define DRAIN() asm volatile("s_waitcnt vmcnt(0)" ::: "memory")
#define FENCE() asm volatile("" ::: "memory")
#define SB() __builtin_amdgcn_sched_barrier(0)
DEV void dma4(rsrc_t rs, unsigned voff, unsigned soff, unsigned ldsa) {
  asm volatile("s_mov_b32 m0, %0\n\ts_nop 0\n\tbuffer_load_dwordx4 %1, %2, %3 offen sc1 lds\n\tbuffer_load_dwordx4 %1, %2, %3 offen offset:1024 sc1 lds\n\t"
               "buffer_load_dwordx4 %1, %2, %3 offen offset:2048 sc1 lds\n\tbuffer_load_dwordx4 %1, %2, %3 offen offset:3072 sc1 lds"
               :: "s"(__builtin_amdgcn_readfirstlane(ldsa)), "v"(voff), "s"(rs), "s"(__builtin_amdgcn_readfirstlane(soff)) : "memory");
}
DEV void dma1(rsrc_t rs, unsigned voff, unsigned soff, unsigned ldsa) {
  asm volatile("s_mov_b32 m0, %0\n\ts_nop 0\n\tbuffer_load_dwordx4 %1, %2, %3 offen sc1 lds"
               :: "s"(__builtin_amdgcn_readfirstlane(ldsa)), "v"(voff), "s"(rs), "s"(__builtin_amdgcn_readfirstlane(soff)) : "memory");
}
DEV void dma_range(rsrc_t rs, unsigned l16, unsigned wsrc, int w, int k0, int k1) {
  int k = k0 + 4 * w;
  for (; k + 4 <= k1; k += 16) dma4(rs, l16, wsrc + k * 1024, k * 1024);
  for (; k < k1; ++k) dma1(rs, l16, wsrc + k * 1024, k * 1024);
}
struct Tmo { unsigned char* ws; unsigned epoch; };
DEV bool give_up(unsigned& sp, const Tmo& tm) {
  ++sp;
  if (sp > 8) __builtin_amdgcn_s_sleep(2);
  if ((sp & 63u) == 0u) {
    GAS unsigned* t = (GAS unsigned*)(tm.ws + WS_CTRL);
    if (sp >= (1u << 16)) { __hip_atomic_store(t, tm.epoch, __ATOMIC_RELAXED, __HIP_MEMORY_SCOPE_AGENT); return true; }
    if (__builtin_amdgcn_readfirstlane(__hip_atomic_load(t, __ATOMIC_RELAXED, __HIP_MEMORY_SCOPE_AGENT)) == tm.epoch) return true;
  }
  return false;
}
#define MAPV 1
#define XW 10
DEV int map_rg(int b) { return ((b & 7) >> 1) * 4 + (b >> 6); }
DEV int map_cg(int b) { return (b & 1) * 8 + ((b >> 3) & 7); }
DEV int o_half(int cg) { return (cg >> 2) & 1; }
DEV int o_c8(int cg) { return (cg >> 3) * 4 + (cg & 3); }
DEV int xfeat(int c, int j) { int c8 = c / 10, l = c % 10; return 80 * c8 + (l < 8 ? 20 * (l >> 1) + 8 * (l & 1) + j : 40 * (l - 8) + 20 * (j >> 2) + 16 + (j & 3)); }
DEV int ocol(int cgp, int s, int j) { return 40 * cgp + (s < 4 ? 20 * (s >> 1) + 8 * (s & 1) + j : 20 * (j >> 2) + 16 + (j & 3)); }
DEV int fresh_lane() { int l; asm volatile("v_mbcnt_lo_u32_b32 %0, -1, 0\n\tv_mbcnt_hi_u32_b32 %0, -1, %0" : "=v"(l)); return l; }
#define RELANE() const int lane = fresh_lane(), h = lane >> 5, tok = lane & 31, tid = w * 64 + lane; const unsigned l16 = lane * 16; (void)h; (void)tok; (void)tid; (void)l16
#define STAMP(i) do {} while (0)
#define LDS_ALLOC LDS_BYTES
#define CNT(i) do {} while (0)

DEV void publish_x(rsrc_t rs, unsigned xbuf, unsigned stbuf, int rg, int cg, int w, int lane, const unsigned (&xr)[10]) {
  const int g = lane >> 4, tok = lane & 15, half = o_half(cg), c8 = o_c8(cg);
  const unsigned sb = xbuf + ((rg * 4 + half * 2 + (w >> 1)) * 40 + 5 * c8) * 1024;
  const unsigned vb = ((w & 1) * 16 + tok) * 16;
  stg_sc1(rs, vb + g * 1024, sb, v4u{xr[0], xr[1], xr[2], xr[3]});
  stg_sc1(rs, vb + g * 1024 + 512, sb, v4u{xr[4], xr[5], xr[6], xr[7]});
  auto s0 = __builtin_amdgcn_permlane16_swap(xr[8], xr[8], false, false);
  auto s1 = __builtin_amdgcn_permlane16_swap(xr[9], xr[9], false, false);
  if (!(g & 1)) stg_sc1(rs, vb + 4096 + (g >> 1) * 512, sb, v4u{xr[8], xr[9], s0[1], s1[1]});
  SB();
  float sa = 0.f, sq = 0.f;
#pragma unroll
  for (int i = 0; i < 10; ++i) { sa = dot2(xr[i], 0x3F803F80u, sa); sq = dot2(xr[i], xr[i], sq); }
  { auto a = __builtin_amdgcn_permlane16_swap(f2u(sa), f2u(sa), false, false); sa = u2f(a[0]) + u2f(a[1]);
    auto b = __builtin_amdgcn_permlane16_swap(f2u(sq), f2u(sq), false, false); sq = u2f(b[0]) + u2f(b[1]); }
  { auto a = __builtin_amdgcn_permlane32_swap(f2u(sa), f2u(sa), false, false); sa = u2f(a[0]) + u2f(a[1]);
    auto b = __builtin_amdgcn_permlane32_swap(f2u(sq), f2u(sq), false, false); sq = u2f(b[0]) + u2f(b[1]); }
  if (g == 0) __builtin_amdgcn_raw_buffer_store_b64(v2u{f2u(sa), f2u(sq)}, rs, tok * 8, stbuf + ((rg * 8 + c8) * 128 + half * 64 + w * 16) * 8, SC1);
}
DEV void load_x_in(const u16* x, int rg, int cg, int w, int lane, unsigned (&xr)[10]) {
  const int g = lane >> 4, tok = lane & 15;
  const GAS v2u* src = (const GAS v2u*)(x + (size_t)(rg * 128 + o_half(cg) * 64 + w * 16 + tok) * 640 + o_c8(cg) * 80 + g * 20);
#pragma unroll
  for (int i = 0; i < 5; ++i) { v2u t = src[i]; xr[2 * i] = t.x; xr[2 * i + 1] = t.y; }
}

DEV void poison_x(rsrc_t rs, unsigned xbuf, unsigned stbuf, int rg, int cg, int w, int lane) {
  const int g = lane >> 4, tok = lane & 15, half = o_half(cg), c8 = o_c8(cg);
  const unsigned sb = xbuf + ((rg * 4 + half * 2 + (w >> 1)) * 40 + 5 * c8) * 1024, vb = ((w & 1) * 16 + tok) * 16;
  const v4u P = {SENT, SENT, SENT, SENT};
  stg_sc1(rs, vb + g * 1024, sb, P); stg_sc1(rs, vb + g * 1024 + 512, sb, P); stg_sc1(rs, vb + 4096 + (g >> 1) * 512, sb, P);
  __builtin_amdgcn_raw_buffer_store_b64(v2u{SENT, SENT}, rs, tok * 8, stbuf + ((rg * 8 + c8) * 128 + half * 64 + w * 16) * 8, SC1);
}
DEV void poison_o(rsrc_t rs, unsigned obuf, int rg, int cg, int w, int lane) {
  const int h = lane >> 5, tok = lane & 31;
  const unsigned os = obuf + ((rg * 8 + 2 * w) * 4 + (cg >> 2)) * 5120 + (cg & 3) * 256, ov = (tok >> 4) * 20480 + (tok & 15) * 16;
  const v4u P = {SENT, SENT, SENT, SENT};
  stg_sc1(rs, ov + h * 2048, os, P); stg_sc1(rs, ov + h * 2048 + 1024, os, P); stg_sc1(rs, ov + 4096, os, P);
}
DEV void wait_flags(rsrc_t rs, unsigned voff, unsigned soff, const Tmo& tm) {
  unsigned sp = 0, v;
  do { FENCE(); v = __builtin_amdgcn_raw_buffer_load_b32(rs, voff, soff, SC1); } while (__builtin_amdgcn_ballot_w64(v != tm.epoch) != 0 && !give_up(sp, tm));
}
DEV void build_wq_read(LAS unsigned char* L, unsigned stage, int w, int lane, v4u (&fr)[3]) {
  const int gam = lane >> 4, q = (lane >> 2) & 3, pp = lane & 3, a = 2 * (gam & 1) + (pp >> 1), h = pp & 1;
  const int eoff = (w < 3) ? 40 * w + 20 * h + 4 * a : (a == 0 ? 40 : a == 1 ? 80 : 0) + 20 * h + 16;
#pragma unroll
  for (int it = 0; it < 3; ++it) {
    const int kap = 2 * it + (gam >> 1), kc = kap < 4 ? kap : 4;
    const unsigned ad = stage + (kc * 8 + q) * 256 + eoff * 2;
    const v2u t0 = ldstr(L, ad), t1 = ldstr(L, ad + 1024);
    fr[it] = v4u{t0.x, t0.y, t1.x, t1.y};
  }
}
DEV float sum8(v4u v, float c) { c = dot2(v.x, 0x3F803F80u, c); c = dot2(v.y, 0x3F803F80u, c); c = dot2(v.z, 0x3F803F80u, c); return dot2(v.w, 0x3F803F80u, c); }
DEV void build_wq_store(rsrc_t rs, int m, int rg, int cg, int w, int lane, int r0, int r1, const v4u (&fr)[3]) {
  const int row = lane & 31, hi = lane >> 5;
  const unsigned tb = w == 0 ? L_P0 : (w - 1) * 1024, ts = w == 0 ? 1024 : KSTEP, th = w == 3 ? 384 : 512;
  const bool in = row >= r0 && row < r1;
#pragma unroll
  for (int it = 0; it < 3; ++it) {
    const int c = 5 * rg + 2 * it + hi;
    stg_sc1(rs, (in && (it < 2 || !hi)) ? (c >> 1) * ts + (c & 1) * th + row * 16 : 0x80000000u, WS_WQ + (m * 16 + cg) * WQ_SZ + tb, fr[it]);
  }
  float cs = sum8(fr[1], sum8(fr[0], 0.f));
  cs = hi ? cs : sum8(fr[2], cs);
  auto t = __builtin_amdgcn_permlane32_swap(f2u(cs), f2u(cs), false, false);
  __builtin_amdgcn_raw_buffer_store_b32(f2u(u2f(t[0]) + u2f(t[1])), rs, (in && !hi) ? row * 4 : 0x80000000u, WS_CSP + ((m * 16 + cg) * 16 + rg) * 512 + w * 128, SC1);
}
DEV void load_cs(LAS unsigned char* L, rsrc_t rs, int m, int cg, int tid) {
  if (tid < 128) {
    float cs = 0.f;
#pragma unroll
    for (int r = 0; r < 16; ++r) cs += u2f(__builtin_amdgcn_raw_buffer_load_b32(rs, tid * 4 + (r & 7) * 512, WS_CSP + (m * 16 + cg) * 8192 + (r >> 3) * 4096, SC1));
    const unsigned ch = pk2(cs, 0.f) & 0xffffu, cl = pk2(cs - lo16(ch), 0.f) & 0xffffu;
    sts128(L, L_CS + m * 2048 + tid * 16, v4u{ch | (cl << 16), ch, 0u, 0u});
  }
}

DEV void poll_probe(rsrc_t rs, unsigned voff, unsigned soff, const Tmo& tm) {
  unsigned sp = 0, v;
  do { FENCE(); v = __builtin_amdgcn_raw_buffer_load_b32(rs, voff, soff, SC1); } while (any_sent(v) && !give_up(sp, tm));
}
DEV void probe_issue(unsigned& d, rsrc_t rs, unsigned voff, unsigned soff) {
  asm volatile("buffer_load_dword %0, %1, %2, %3 offen sc1" : "=v"(d) : "v"(voff), "s"(rs), "s"(soff) : "memory");
}
template <int N> DEV void probe_wait(unsigned& d) { asm volatile("s_waitcnt vmcnt(%1)" : "+v"(d) : "i"(N) : "memory"); }
template <int I> struct IC { static constexpr int v = I; };
template <int I, int N, class F> DEV void sfor(F&& f) { if constexpr (I < N) { f(IC<I>{}); sfor<I + 1, N>(f); } }
#define SFOR(var, n) sfor<0, n>([&](auto var##_) __attribute__((always_inline)) { constexpr int var = decltype(var##_)::v;
#define SEND })
template <bool NORM, class F> DEV float phi_map(const float (&a)[20], float s, float post, unsigned (&ph)[20], F slot) {
  float mx = __builtin_fmaxf(a[0], a[1]), mn = __builtin_fminf(a[0], a[1]);
#pragma unroll
  for (int i = 2; i < 20; i += 2) { mx = __builtin_fmaxf(__builtin_fmaxf(mx, a[i]), a[i + 1]); mn = __builtin_fminf(__builtin_fminf(mn, a[i]), a[i + 1]); }
  const float bp = -s * mx, bn = s * mn, ns = -s;
  float sp = 0.f;
  SFOR(sg, 2)
    const f32x2 sc = sg ? f32x2{ns, ns} : f32x2{s, s}, bi = sg ? f32x2{bn, bn} : f32x2{bp, bp};
    f32x2 e[10];
    slot(IC<5 * sg>{});
#pragma unroll
    for (int i = 0; i < 10; ++i) e[i] = pkfma(f32x2{a[2 * i], a[2 * i + 1]}, sc, bi);
    SB();
    SFOR(i, 10)
      if constexpr (i == 3 || i == 7) slot(IC<5 * sg + 1 + (i >> 2)>{});
      e[i] = f32x2{__builtin_amdgcn_exp2f(e[i][0]), __builtin_amdgcn_exp2f(e[i][1])};
    SEND;
    f32x2 s0 = pkadd(e[0], e[1]), s1 = pkadd(e[2], e[3]), s2 = pkadd(e[4], e[5]);
    s0 = pkadd(s0, e[6]); s1 = pkadd(s1, e[7]); s2 = pkadd(s2, e[8]); s0 = pkadd(s0, e[9]);
    slot(IC<5 * sg + 3>{});
    if constexpr (!NORM && sg == 0) {
#pragma unroll
      for (int i = 0; i < 10; ++i) ph[i] = pk2(e[i][0], e[i][1]);
    }
    s0 = pkadd(pkadd(s0, s1), s2);
    const float sm = s0[0] + s0[1];
    if constexpr (NORM || sg == 1) {
      const float r = __builtin_amdgcn_rcpf(sm) * (NORM ? post : sp);
#pragma unroll
      for (int i = 0; i < 10; ++i) { f32x2 t = pkmul(e[i], f32x2{r, r}); ph[10 * sg + i] = pk2(t[0], t[1]); }
    }
    sp = sg ? sp : sm;
    slot(IC<5 * sg + 4>{});
  SEND;
  return sp;
}

#define BARE() asm volatile("s_barrier" ::: "memory")
template <int OFF> DEV void dsr(v4u& d, unsigned addr) { asm volatile("ds_read_b128 %0, %1 offset:%2" : "=v"(d) : "v"(addr), "i"(OFF)); }
template <int R, int OFF, bool COH> DEV void lda(rsrc_t rs, unsigned voff, unsigned soff) {
  if constexpr (COH) asm volatile("buffer_load_dwordx4 a[%c0:%c1], %2, %3, %4 offen offset:%5 sc1" :: "i"(R), "i"(R + 3), "v"(voff), "s"(rs), "s"(soff), "i"(OFF) : "memory");
  else asm volatile("buffer_load_dwordx4 a[%c0:%c1], %2, %3, %4 offen offset:%5" :: "i"(R), "i"(R + 3), "v"(voff), "s"(rs), "s"(soff), "i"(OFF) : "memory");
}
DEV void sbump(unsigned& so) { asm volatile("s_add_u32 %0, %0, 0x1000" : "+s"(so) :: "scc"); }
template <bool Z, int R> DEV void mf32a(f32x16& c, const v4u& a) {
  if constexpr (Z) asm volatile("v_mfma_f32_32x32x16_bf16 %0, %1, a[%c2:%c3], 0" : "=&v"(c) : "v"(a), "i"(R), "i"(R + 3));
  else asm volatile("v_mfma_f32_32x32x16_bf16 %0, %1, a[%c2:%c3], %0" : "+v"(c) : "v"(a), "i"(R), "i"(R + 3));
}
template <bool Z> DEV void mf32v(f32x16& c, const v4u& a, const v4u& b) {
  if constexpr (Z) asm volatile("v_mfma_f32_32x32x16_bf16 %0, %1, %2, 0" : "=&v"(c) : "v"(a), "v"(b));
  else asm volatile("v_mfma_f32_32x32x16_bf16 %0, %1, %2, %0" : "+v"(c) : "v"(a), "v"(b));
}
template <bool Z, int R> DEV void mf16a(f32x4& c, const v4u& b) {
  if constexpr (Z) asm volatile("v_mfma_f32_16x16x32_bf16 %0, a[%c2:%c3], %1, 0" : "=&v"(c) : "v"(b), "i"(R), "i"(R + 3));
  else asm volatile("v_mfma_f32_16x16x32_bf16 %0, a[%c2:%c3], %1, %0" : "+v"(c) : "v"(b), "i"(R), "i"(R + 3));
}
template <bool Z> DEV void mf16v(f32x4& c, const v4u& a, const v4u& b) {
  if constexpr (Z) asm volatile("v_mfma_f32_16x16x32_bf16 %0, %1, %2, 0" : "=&v"(c) : "v"(a), "v"(b));
  else asm volatile("v_mfma_f32_16x16x32_bf16 %0, %1, %2, %0" : "+v"(c) : "v"(a), "v"(b));
}
template <int N> DEV void vmc() { asm volatile("s_waitcnt vmcnt(%0)" :: "i"(N) : "memory"); }
template <int I> DEV void dmaw(rsrc_t rw, unsigned l16, unsigned wb) {
  asm volatile("s_or_b32 m0, %0, %1\n\ts_nop 0\n\tbuffer_load_dwordx4 %2, %3, m0 offen" DSC1 " lds" :: "s"(wb), "i"(I * 4096), "v"(l16), "s"(rw) : "memory", "scc");
}
template <int N> DEV void lgkm() { asm volatile("s_waitcnt lgkmcnt(%0)" :: "i"(N)); }
#define MFMA_SETTLE() asm volatile("s_nop 7\n\ts_nop 3")
DEV bool any_nan(float x) { return __builtin_amdgcn_ballot_w64(x != x) != 0; }
#define PFD 3

template <int S, int NW, int VM, int VI> DEV void p2pair(f32x16& a0, v4u& w0, v4u& w1, unsigned bq, unsigned bcs, rsrc_t r, unsigned l16, unsigned so) {
#define P2_M0 "s_waitcnt lgkmcnt(%c[nw])\n\tv_mfma_f32_32x32x16_bf16 %[c], %[w0], a[%c[r0]:%c[r1]], "
#define P2_R0 "\n\tds_read_b128 %[w0], %[a0] offset:%c[o0]\n\t"
#define P2_M1 "v_mfma_f32_32x32x16_bf16 %[c], %[w1], a[%c[r2]:%c[r3]], %[c]"
#define P2_R1 "\n\tds_read_b128 %[w1], %[bq] offset:%c[o1]"
#define P2_IN [nw] "i"(NW), [r0] "i"(4 * S), [r1] "i"(4 * S + 3), [r2] "i"(4 * S + 4), [r3] "i"(4 * S + 7), [a0] "v"(S + 6 == 40 ? bcs : bq), [bq] "v"(bq), \
              [o0] "i"(S + 6 < 40 ? (S + 6) * 1024 : 0), [o1] "i"((S + 7) * 1024), [rs] "s"(r), [l16] "v"(l16), [so] "s"(so), [d0] "i"(160 + 4 * VI), [d1] "i"(163 + 4 * VI), \
              [vo] "i"((VI & 3) * 1024), [dm] "i"(VI * 4096)
#define P2_LD "buffer_load_dwordx4 a[%c[d0]:%c[d1]], %[l16], %[rs], %[so] offen offset:%c[vo] sc1\n\t"
#define P2_DMA "s_or_b32 m0, %[so], %[dm]\n\t"
#define P2_DMB "buffer_load_dwordx4 %[l16], %[rs], m0 offen" DSC1 " lds\n\t"
  if constexpr (S == 0) {
    static_assert(S != 0 || VM == 0, "");
    asm volatile(P2_M0 "0" P2_R0 P2_M1 P2_R1 : [c] "=&v"(a0), [w0] "+v"(w0), [w1] "+v"(w1) : P2_IN : "memory");
  } else if constexpr (S + 7 < 40) {
    if constexpr (VM == 0) asm volatile(P2_M0 "%[c]" P2_R0 P2_M1 P2_R1 : [c] "+v"(a0), [w0] "+v"(w0), [w1] "+v"(w1) : P2_IN : "memory");
    else if constexpr (VM == 1) asm volatile(P2_M0 "%[c]" P2_R0 P2_LD P2_M1 P2_R1 : [c] "+v"(a0), [w0] "+v"(w0), [w1] "+v"(w1) : P2_IN : "memory");
    else asm volatile(P2_M0 "%[c]\n\t" P2_DMA "ds_read_b128 %[w0], %[a0] offset:%c[o0]\n\t" P2_DMB P2_M1 P2_R1 : [c] "+v"(a0), [w0] "+v"(w0), [w1] "+v"(w1) : P2_IN : "memory", "scc");
  } else if constexpr (S + 6 == 40) {
    static_assert(S + 6 != 40 || VM == 0, "");
    asm volatile(P2_M0 "%[c]" P2_R0 P2_M1 : [c] "+v"(a0), [w0] "+v"(w0), [w1] "+v"(w1) : P2_IN : "memory");
  } else {
    static_assert(VM == 0, "");
    asm volatile(P2_M0 "%[c]\n\t" P2_M1 : [c] "+v"(a0), [w0] "+v"(w0), [w1] "+v"(w1) : P2_IN : "memory");
  }
}
template <bool Z, int RA, int RB> DEV void mf16aa(f32x4& c) {
  if constexpr (Z) asm volatile("v_mfma_f32_16x16x32_bf16 %0, a[%c1:%c2], a[%c3:%c4], 0" : "=v"(c) : "i"(RA), "i"(RA + 3), "i"(RB), "i"(RB + 3));
  else asm volatile("v_mfma_f32_16x16x32_bf16 %0, a[%c1:%c2], a[%c3:%c4], %0" : "+v"(c) : "i"(RA), "i"(RA + 3), "i"(RB), "i"(RB + 3));
}
template <int RB> DEV void mf16va(f32x4& c, const v4u& a) { asm volatile("v_mfma_f32_16x16x32_bf16 %0, %1, a[%c2:%c3], %0" : "+v"(c) : "v"(a), "i"(RB), "i"(RB + 3)); }
#define DSW(addr, off, val) asm volatile("ds_write_b128 %0, %1 offset:%2" :: "v"(addr), "v"(val), "i"(off) : "memory")
#define ADM 10
#define ODM 5
#define GDM 7
#define DLY_O 2
#define DLY_X 2
#define KPS 8

__global__ void __launch_bounds__(256, 1) k_main(Params p) {
  extern __shared__ __attribute__((aligned(16))) unsigned char smem[];
  LAS unsigned char* L = (LAS unsigned char*)smem;
  {
    const unsigned long long pc = __builtin_amdgcn_s_getpc();
    const unsigned t16 = __builtin_amdgcn_workitem_id_x() * 16;
    asm volatile("global_load_dwordx4 a[0:3], %0, %1\n\tglobal_load_dwordx4 a[4:7], %0, %1 offset:4080" :: "v"(t16), "s"(pc) : "memory");
    asm volatile("global_load_dwordx4 a[8:11], %0, %1" :: "v"(t16 + 8192 + ((__builtin_amdgcn_workgroup_id_x() >> 3) & 3) * 4096), "s"(pc) : "memory");
    SB();
  }
  const int b = __builtin_amdgcn_workgroup_id_x(), w = __builtin_amdgcn_readfirstlane(__builtin_amdgcn_workitem_id_x() >> 6);
  const int rg = map_rg(b), cg = map_cg(b), half = o_half(cg), c8 = o_c8(cg);
  const rsrc_t rs = __builtin_amdgcn_make_buffer_rsrc(p.ws, 0, WS_TOTAL, 0x00020000);
  asm volatile("" ::: "a255");
  unsigned ad[12];
  auto addresses = [&](int m) __attribute__((always_inline)) {
    RELANE();
    if (m == 0) {
      const unsigned r_ = h * 144 + 16 + 32 * w + tok, q_ = ((lane >> 4) & 1) * 144 + (lane & 15);
      ad[0] = L_K + r_ * 48; ad[1] = L_V + r_ * 80; ad[2] = (h * 16 + (tok - 16)) * 16;
      ad[3] = L_K + q_ * 48 + h * 16; ad[4] = h ? L_V + q_ * 80 : L_K + q_ * 48 + 32; ad[5] = L_V + q_ * 80 + 16 + h * 16;
      ad[6] = ad[7] = ad[8] = ad[9] = ad[10] = ad[11] = 0;
    } else {
      const int g = lane >> 4, hh = w >> 1, dt = w & 1, q = (lane >> 2) & 3, p4 = lane & 3, r = lane & 15;
      const unsigned t_ = 32 * w + tok, key = (t_ & 7) ^ (((t_ >> 3) & 1) << 2);
      ad[0] = L_SCR + (h * 128 + t_) * 128 + (key << 4);
      const unsigned k0 = q ^ ((g & 1) << 2), rb0 = L_SCR + (hh * 128 + 8 * g + q) * 128, cB = dt ? 7 : 5 + (p4 >> 1);
      ad[1] = rb0 + ((p4 ^ k0) << 4); ad[2] = rb0 + ((4 ^ k0) << 4) + (p4 & 1) * 8; ad[3] = rb0 + ((cB ^ k0) << 4) + (p4 & 1) * 8;
      ad[4] = ((hh * 5 + g) * 24 + 16 * dt + r) * 16; ad[5] = ((hh * 5 + 4) * 24 + 16 * dt + r) * 16;
#pragma unroll
      for (int j = 0; j < 4; ++j) { const int slot = (w < 3 ? 64 * w : 176) + 16 * j + r, q24 = slot / 24, d_ = slot % 24, hk = q24 >= 5, s_ = q24 - 5 * hk;
        ad[6 + j] = L_KVA + (((s_ * 2 + (d_ >> 4)) * 2 + hk) * 16 + (d_ & 15)) * 16 + g * 8; }
      ad[10] = L_KVA + h * 256 + (4 * (tok >> 3) + (tok & 3)) * 16;
      ad[11] = g * 4096 + r * 16;
    }
    asm volatile("" : "+v"(ad[0]), "+v"(ad[1]), "+v"(ad[2]), "+v"(ad[3]), "+v"(ad[4]), "+v"(ad[5]), "+v"(ad[6]), "+v"(ad[7]), "+v"(ad[8]), "+v"(ad[9]), "+v"(ad[10]), "+v"(ad[11]));
  };
  unsigned xr[10];
  Tmo tm;
  { RELANE();
    const int l = 0; (void)l;
    STAMP(0);
    constexpr unsigned SG_B = L_P0 + 2048, SG_O = SG_B + 10240, SG_P = SG_O + 10240, OOR = 0x80000000u;
    const int j = (tid >> 4) & 7, cc = lane & 15, seg = (cc * 13) >> 6, g = lane >> 4;
    v4u ra[3], rb[3], ro[3], rp;
    const unsigned wcol = (seg * 640 + 40 * cg + 8 * (cc - 5 * seg)) * 2;
    unsigned wrow[3];
#pragma unroll
    for (int i = 0; i < 3; ++i) {
      const int kap = 2 * i + (w >> 1) < 4 ? 2 * i + (w >> 1) : 4, lc = 5 * (rg & 1) + kap;
      const int t8 = lc >> 3, rb0 = 80 * (rg >> 1) + (1 - t8) * (20 * (lc >> 1) + 8 * (lc & 1)) + t8 * (40 * (lc - 8) + 16);
      wrow[i] = (rb0 + j + t8 * (16 * (j >> 2))) * 3840 + wcol;
    }
    { const rsrc_t rA = __builtin_amdgcn_make_buffer_rsrc((void*)p.wqA, 0, 640 * 1920 * 2, 0x00020000);
#pragma unroll
      for (int i = 0; i < 3; ++i) ra[i] = ldg(rA, wrow[i], 0); }
    load_x_in(p.x, rg, cg, w, lane, xr);
    const unsigned ep = __builtin_amdgcn_raw_buffer_load_b32(rs, 0, WS_CTRL + 128, SC1);
    FENCE(); SB();
    { const rsrc_t rB = __builtin_amdgcn_make_buffer_rsrc((void*)p.wqB, 0, 640 * 1920 * 2, 0x00020000);
      const rsrc_t rO = __builtin_amdgcn_make_buffer_rsrc((void*)(half ? p.woB : p.woA), 0, 640 * 640 * 2, 0x00020000);
      const rsrc_t rP = __builtin_amdgcn_make_buffer_rsrc((void*)(p.wphi + cg * 800), 0, 1600, 0x00020000);
#pragma unroll
      for (int i = 0; i < 3; ++i) rb[i] = ldg(rB, wrow[i], 0);
#pragma unroll
      for (int i = 0; i < 3; ++i) { const int so = 2 * i + (w >> 1) < 4 ? 2 * i + (w >> 1) : 4, t4 = so >> 2;
        ro[i] = ldg(rO, (40 * rg + (1 - t4) * (20 * (so >> 1) + 8 * (so & 1)) + t4 * 16 + j + t4 * (16 * (j >> 2))) * 1280 + (80 * c8 + 8 * (cc < 10 ? cc : 0)) * 2, 0); }
      rp = ldg(rP, tid * 16, 0);
    FENCE(); SB();
    STAMP(53);
    const unsigned sa = tid * 16;
#pragma unroll
    for (int i = 0; i < 3; ++i) if (i < 2 || w < 2) sts128(L, sa + i * 4096, ra[i]);
    BAR();
    STAMP(54);
    { v4u fr[3];
      build_wq_read(L, 0, w, lane, fr);
      publish_x(rs, WS_X, WS_ST0, rg, cg, w, lane, xr);
      build_wq_store(rs, 0, rg, cg, w, lane, 0, w < 3 ? 32 : 24, fr); }
    tm.ws = p.ws; tm.epoch = __builtin_amdgcn_readfirstlane(ep) + 0x9E3779B1u;
    STAMP(55);
#pragma unroll
    for (int i = 0; i < 3; ++i) if (i < 2 || w < 2) { sts128(L, SG_B + sa + i * 4096, rb[i]); sts128(L, SG_O + sa + i * 4096, ro[i]); }
    if (w < 2) sts128(L, SG_P + sa, rp);
    }
    DRAIN();
    BAR();
    if (tid == 0) __builtin_amdgcn_raw_buffer_store_b32(tm.epoch, rs, 0, WS_FLAG + (cg * 16 + rg) * 128, SC1);
    STAMP(60);
    v4u fv[4], fb[3], fo[2];
    {
      const int sg = w >> 1, hw = w & 1;
      v4u bf[2], af[4];
#pragma unroll
      for (int ct = 0; ct < 2; ++ct) {
        const int q = (lane >> 2) & 3, pp = lane & 3, fcol = ct ? 16 : 4 * pp;
        const int d0 = 8 * g + q < 19 ? 8 * g + q : 19, d1 = 8 * g + 4 + q < 19 ? 8 * g + 4 + q : 19;
        const v2u t0 = ldstr(L, SG_P + hw * 800 + d0 * 40 + fcol * 2), t1 = ldstr(L, SG_P + hw * 800 + d1 * 40 + fcol * 2);
        bf[ct] = v4u{g < 3 ? t0.x : 0u, g < 3 ? t0.y : 0u, g < 2 ? t1.x : 0u, g < 2 ? t1.y : 0u};
      }
#pragma unroll
      for (int i = 0; i < 4; ++i) { const int r = (i < 2 ? 8 * (cc >> 2) : 32) + 4 * (i & 1) + (cc & 3); const unsigned a = SG_B + r * 256 + sg * 80 + hw * 40 + g * 16;
        const v2u t0 = lds64(L, a), t1 = lds64(L, a + 8); af[i] = v4u{t0.x, t0.y, t1.x, t1.y}; }
      build_wq_read(L, SG_B, w, lane, fb);
      { const int q = (lane >> 2) & 3, pp = lane & 3;
#pragma unroll
        for (int it = 0; it < 2; ++it) {
          const int pc = it * 16 + (tid >> 4), pcc = pc < 25 ? pc : 24, so = (pcc * 13) >> 6, ft = pcc - 5 * so;
          const unsigned a = SG_O + (so * 8 + q) * 256 + 40 * pp + 8 * ft;
          const v2u t0 = ldstr(L, a), t1 = ldstr(L, a + 1024);
          fo[it] = v4u{t0.x, t0.y, t1.x, t1.y};
        } }
      f32x4 d[8];
      asm volatile("s_nop 1" : "+v"(bf[0]), "+v"(bf[1]), "+v"(af[0]), "+v"(af[1]), "+v"(af[2]), "+v"(af[3]));
      SFOR(i, 8) mf16v<true>(d[i], af[i & 3], bf[i >> 2]); SEND;
      asm volatile("s_nop 7");
      SB();
#pragma unroll
      for (int i = 0; i < 4; ++i) fv[i] = v4u{pk2(d[2 * i][0], d[2 * i][1]), pk2(d[2 * i][2], d[2 * i][3]), pk2(d[2 * i + 1][0], d[2 * i + 1][1]), pk2(d[2 * i + 1][2], d[2 * i + 1][3])};
    }
    STAMP(58);
    { const int sg = w >> 1, hw = w & 1;
#pragma unroll
      for (int ct = 0; ct < 2; ++ct) {
        const int row = ct ? (sg ? 0 : 16) + 4 * hw + (cc & 3) : 8 * (cc >> 2) + 4 * hw + (cc & 3);
        const unsigned tb = ct ? 2048 : (sg ? 0 : L_P0), ts = (ct || sg) ? KSTEP : 1024, th = ct ? 384 : 512;
        const bool in = ct == 0 || cc < 4;
#pragma unroll
        for (int u = 0; u < 2; ++u) { const int c = 5 * rg + (u ? 4 : g);
          stg_sc1(rs, (in && (u == 0 || g == 0)) ? (c >> 1) * ts + (c & 1) * th + row * 16 : OOR, WS_WQ + (16 + cg) * WQ_SZ + tb, fv[2 * ct + u]); }
        float cs = sum8(fv[2 * ct], 0.f);
        cs = g ? cs : sum8(fv[2 * ct + 1], cs);
        { auto t = __builtin_amdgcn_permlane16_swap(f2u(cs), f2u(cs), false, false); cs = u2f(t[0]) + u2f(t[1]); }
        { auto t = __builtin_amdgcn_permlane32_swap(f2u(cs), f2u(cs), false, false); cs = u2f(t[0]) + u2f(t[1]); }
        __builtin_amdgcn_raw_buffer_store_b32(f2u(cs), rs, (in && g == 0) ? row * 4 : OOR, WS_CSP + ((16 + cg) * 16 + rg) * 512 + (ct ? 3 : sg) * 128, SC1);
      }
      if (w >= 2) build_wq_store(rs, 1, rg, cg, w, lane, w == 2 ? 0 : 8, w == 2 ? 32 : 16, fb);
#pragma unroll
      for (int it = 0; it < 2; ++it) { const int pc = it * 16 + (tid >> 4);
        stg_sc1(rs, pc < 25 ? pc * 1024 + cc * 16 : OOR, WS_WO + (half * 8 + c8) * WO_SZ + (rg >> 2) * 25600 + (rg & 3) * 256, fo[it]); }
    }
    poison_o(rs, WS_O, rg, cg, w, lane);
    stg_sc1(rs, l16, WS_KV + (cg * 16 + rg) * 4096 + w * 1024, v4u{SENT, SENT, SENT, SENT});
    stg_sc1(rs, l16, WS_HALO + (rg * 16 + cg) * 4096 + w * 1024, v4u{SENT, SENT, SENT, SENT});
    addresses(0);
    STAMP(61);
    if (w == 1) wait_flags(rs, (h == 0 ? cc * 16 + rg : cg * 16 + cc) * 128, WS_FLAG, tm);
    BAR();
    STAMP(62);
    const rsrc_t rw = __builtin_amdgcn_make_buffer_rsrc(p.ws + WS_WQ + cg * WQ_SZ, 0, WQ_SZ, 0x00020000);
    SFOR(i, 37) dmaw<i>(rw, l16, w * 1024); SEND;
    if (w < 2) dmaw<37>(rw, l16, w * 1024);
    float csv[16];
#pragma unroll
    for (int r = 0; r < 16; ++r) csv[r] = u2f(__builtin_amdgcn_raw_buffer_load_b32(rs, (tid & 127) * 4 + (r & 7) * 512, WS_CSP + cg * 8192 + (r >> 3) * 4096, SC1));
    FENCE(); SB();
    STAMP(63);
    FENCE(); SB();
    STAMP(57);
    { float cs = 0.f;
#pragma unroll
      for (int r = 0; r < 16; ++r) cs += csv[r];
      const unsigned ch = pk2(cs, 0.f) & 0xffffu, cl = pk2(cs - lo16(ch), 0.f) & 0xffffu;
      asm volatile("" :: "v"(cs));
      if (tid < 128) sts128(L, L_CS + tid * 16, v4u{ch | (cl << 16), ch, 0u, 0u}); }
    BAR();
    if (tid == 0) __builtin_amdgcn_raw_buffer_store_b32(tm.epoch, rs, 0, WS_FLAG + (256 + cg * 16 + rg) * 128, SC1);
  }

  for (int l = 0; l < 4; ++l) {
    const int m = l & 1;
    const rsrc_t rw = __builtin_amdgcn_make_buffer_rsrc(p.ws + WS_WQ + ((m ^ 1) * 16 + cg) * WQ_SZ, 0, l < 3 ? WQ_SZ : 0u, 0x00020000);
    const unsigned wb1k = w * 1024;
#define DM(i) do { SB(); dmaw<(i)>(rw, l16, wb1k); SB(); } while (0)
    unsigned wos;
#define LDWO(i) do { lda<160 + 4 * (i), ((i) & 3) * 1024, true>(rs, l16, wos); if constexpr (((i) & 3) == 3) sbump(wos); } while (0)
    v4u wo24, mfr;
    f32x16 a0, a1, a2, a3;
    float rstd, sd, o[20];
    {
      RELANE();
      STAMP(1 + 12 * l);
      const unsigned xs = WS_X + l * XB + (rg * 4 + w) * 40960;
      const unsigned sts = (l ? WS_ST1 + (l - 1) * STB : WS_ST0) + (rg * 8 * 128 + w * 32) * 8;
#define LDX(s) do { lda<4 * (s), ((s) & 3) * 1024, true>(rs, l16, xso); if constexpr (((s) & 3) == 3) sbump(xso); } while (0)
      const unsigned stv = h * 4096 + tok * 8;
      constexpr int NB = PFD + 1, HI = 23;
      constexpr int XR = 15 - XW;
      const unsigned b12 = l16, b3 = 2048 + h * 384 + tok * 16, b12h = b12 + HI * KSTEP, b3h = b3 + HI * KSTEP, bcs = L_CS + m * 2048 + tok * 16;
      unsigned f1 = 0;
      const unsigned f1v = (lane < 16 ? lane * 16 + rg : (lane < 32 ? cg : (c8 >> 2) * 8 + ((lane >> 4) & 1) * 4 + (c8 & 3)) * 16 + (lane & 15)) * 128;
      for (unsigned att = 0, fresh = 1;; fresh = 0) {
        v2u st[4];
        unsigned xso = xs;
        wos = WS_WO + ((m * 8 + c8) * 4 + w) * 25600;
        asm volatile("buffer_load_dwordx2 %0, %4, %5, %6 offen sc1\n\tbuffer_load_dwordx2 %1, %4, %5, %6 offen offset:1024 sc1\n\t"
                     "buffer_load_dwordx2 %2, %4, %5, %6 offen offset:2048 sc1\n\tbuffer_load_dwordx2 %3, %4, %5, %6 offen offset:3072 sc1"
                     : "=&v"(st[0]), "=&v"(st[1]), "=&v"(st[2]), "=&v"(st[3]) : "v"(stv), "s"(rs), "s"(sts) : "memory");
        SFOR(s, XW) SB(); LDX(s); if constexpr (s == 1 || s == 5) { SB(); if (fresh) BARE(); } SEND;
        FENCE(); SB();
        v4u wb[NB][3];
        auto rd = [&](auto s_, auto t_) __attribute__((always_inline)) {
          constexpr int s = decltype(s_)::v, t = decltype(t_)::v;
          if constexpr (s < 40) dsr<(s < HI ? s : s - HI) * KSTEP + (t == 1 ? 1024 : 0)>(wb[s % NB][t], t == 2 ? (s < HI ? b3 : b3h) : (s < HI ? b12 : b12h));
          else if constexpr (s == 40) dsr<(t + 1) * 512>(wb[s % NB][t], bcs);
        };
        SFOR(j, PFD) rd(IC<j>{}, IC<2>{}); rd(IC<j>{}, IC<1>{}); rd(IC<j>{}, IC<0>{}); SEND;
        asm volatile("s_waitcnt vmcnt(%4)" : "+v"(st[0]), "+v"(st[1]), "+v"(st[2]), "+v"(st[3]) : "i"(XW) : "memory");
        unsigned ms = max3u(max3u(st[0].x, st[0].y, st[1].x), max3u(st[1].y, st[2].x, st[2].y), umax(st[3].x, st[3].y));
        if (__builtin_expect(any_sent(ms), 0)) {
          unsigned sp = 0;
          do {
            FENCE();
#pragma unroll
            for (int i = 0; i < 4; ++i) st[i] = ldg64_sc1(rs, stv + i * 1024, sts);
            ms = max3u(max3u(st[0].x, st[0].y, st[1].x), max3u(st[1].y, st[2].x, st[2].y), umax(st[3].x, st[3].y));
          } while (any_sent(ms) && !give_up(sp, tm));
          lgkm<0>(); CNT(0);
          if (!any_sent(ms)) continue;
        }
        { const float sa = (u2f(st[0].x) + u2f(st[1].x)) + (u2f(st[2].x) + u2f(st[3].x)), sq = (u2f(st[0].y) + u2f(st[1].y)) + (u2f(st[2].y) + u2f(st[3].y));
          auto x0 = __builtin_amdgcn_permlane32_swap(f2u(sa), f2u(sa), false, false); auto x1 = __builtin_amdgcn_permlane32_swap(f2u(sq), f2u(sq), false, false);
          const float nm = (u2f(x0[0]) + u2f(x0[1])) * (-1.f / 640.f), var = __builtin_fmaxf((u2f(x1[0]) + u2f(x1[1])) * (1.f / 640.f) - nm * nm, 0.f) + 1e-5f;
          rstd = __builtin_amdgcn_rsqf(var); sd = var * rstd;
          const unsigned mh = pk2(nm, nm), ml = pk2(nm - lo16(mh), 0.f);
          mfr = v4u{h ? 0u : mh, h ? 0u : ml, 0u, 0u};
          asm volatile("" : "+v"(mfr), "+v"(rstd), "+v"(sd));
        }
        SFOR(s, 40)
          SB();
          if constexpr (s == 20) { if (l == 0) probe_issue(f1, rs, f1v, WS_FLAG + 256 * 128); SB(); }
          if constexpr (s == 25) {
            if (l == 0) {
              probe_wait<5>(f1);
              if (__builtin_amdgcn_ballot_w64(f1 != tm.epoch) != 0) wait_flags(rs, f1v, WS_FLAG + 256 * 128, tm);
            }
            if (att == 0) BARE();
            SB();
          }
          constexpr int c = s % NB;
          constexpr int issued = s <= XR ? XW + 2 * s : (15 + s < 40 ? 15 + s : 40);
          asm volatile("s_waitcnt vmcnt(%0) lgkmcnt(%1)" :: "i"(issued - 1 - s + (s > 25 ? s - 25 : 0)), "i"(3 * ((s + PFD <= 41 ? PFD : 41 - s) - 1)) : "memory");
          mf32a<s == 0, 4 * s>(a1, wb[c][0]);
          if constexpr (s == 0) STAMP(2 + 12 * l);
          rd(IC<s + PFD>{}, IC<2>{});
          if constexpr (s < XR) LDX(issued);
          SB();
          mf32a<s == 0, 4 * s>(a2, wb[c][1]);
          rd(IC<s + PFD>{}, IC<1>{});
          SB();
          mf32a<s == 0, 4 * s>(a3, wb[c][2]);
          rd(IC<s + PFD>{}, IC<0>{});
          if constexpr (s < 25) LDX(issued + (s < XR ? 1 : 0)); else LDWO(s - 25);
        SEND;
        SB();
        lgkm<0>();
        mf32v<false>(a1, wb[40 % NB][0], mfr); mf32v<false>(a3, wb[40 % NB][2], mfr); mf32v<false>(a2, wb[40 % NB][1], mfr);
        MFMA_SETTLE();
        SB();
        if (__builtin_expect(!any_nan(a1[0]), 1)) break;
        CNT(1);
        if (++att > 32) break;
      }
      STAMP(3 + 12 * l);
    }
    BARE();
    if (POIS == 3 && l < 3) { RELANE(); poison_o(rs, WS_O + (l + 1) * XB, rg, cg, w, lane); if (l > 0) poison_x(rs, WS_X + (l + 1) * XB, WS_ST1 + l * STB, rg, cg, w, lane); }
    if (l == 0) { RELANE(); poison_x(rs, WS_X + XB, WS_ST1, rg, cg, w, lane); }
#define P2VM(pp) ((pp) % 3 == 2 || (pp) == 0 || (pp) > 16 ? 0 : (((pp) - (pp) / 3 - 1) < 9 ? 1 : 0))
#define P2VI(pp) (P2VM(pp) == 1 ? 15 + ((pp) - (pp) / 3 - 1) : P2VM(pp) == 2 ? ((pp) - (pp) / 3 - 1) - 9 : 0)
#define P2PAIR(pp, nw) do { p2pair<2 * (pp), nw, P2VM(pp), P2VI(pp)>(a0, wq[(2 * (pp)) % 6], wq[(2 * (pp) + 1) % 6], bq, bcs, P2VM(pp) == 2 ? rw : rs, l16, P2VM(pp) == 2 ? wb1k : wos); \
                            if constexpr (P2VM(pp) == 1 && (P2VI(pp) & 3) == 3) sbump(wos); } while (0)
    constexpr int NDM2 = 0;
    if (m == 0) {
      RELANE();
      const unsigned bq = L_P0 + l16, bcs = L_CS + tok * 16;
      v4u wq[6], kc0, kc1, kc2, hd[4] = {};
      const unsigned hsrc = WS_HALO + (((l >> 1) * 16 + rg - 1) * 16 + cg) * 4096;
      const unsigned kwa = ad[0], vwa = ad[1];
#define VCH(i) ((i) < 4 ? v4u{f2u(a2[4 * (i)]), f2u(a2[4 * (i) + 1]), f2u(a2[4 * (i) + 2]), f2u(a2[4 * (i) + 3])} : v4u{f2u(a3[4]), f2u(a3[5]), f2u(a3[6]), f2u(a3[7])})
      SFOR(j, 6) dsr<j * 1024>(wq[j], bq); SEND;
      kc0 = v4u{pk2(a1[0], a1[1]), pk2(a1[2], a1[3]), pk2(a1[4], a1[5]), pk2(a1[6], a1[7])};
      kc1 = v4u{pk2(a1[8], a1[9]), pk2(a1[10], a1[11]), pk2(a1[12], a1[13]), pk2(a1[14], a1[15])};
      kc2 = v4u{pk2(a3[0], a3[1]), pk2(a3[2], a3[3]), f2u(rstd), 0u};
      unsigned qp[10];
      qp[8] = pk2(a3[8], a3[9]); qp[9] = pk2(a3[10], a3[11]);
      asm volatile("" : "+v"(kc0), "+v"(kc1), "+v"(kc2), "+v"(qp[8]), "+v"(qp[9]), "+v"(hd[0]), "+v"(hd[1]), "+v"(hd[2]), "+v"(hd[3]));
      SFOR(pp, 20)
        SB();
        constexpr int nwr = (pp - 1 >= 1 ? ((pp - 1 > 15 ? 15 : pp - 1) + 1) / 2 : 0) - (pp - 4 >= 1 ? ((pp - 4 > 15 ? 15 : pp - 4) + 1) / 2 : 0);
        P2PAIR(pp, (2 * pp + 6 <= 41 ? 4 : 39 - 2 * pp) + nwr);
        if constexpr (pp == 0) {
          if (w == 3 && tok >= 16) {
            const unsigned hs = hsrc + 65536, hv = ad[2];
            stg_sc1(rs, hv, hs, kc0); stg_sc1(rs, hv + 512, hs, kc1); stg_sc1(rs, hv + 1024, hs, kc2);
#pragma unroll
            for (int i = 0; i < 5; ++i) stg_sc1(rs, hv + 1536 + i * 512, hs, VCH(i));
          }
        }
        if constexpr (pp == 1) DSW(kwa, 0, kc0);
        if constexpr (pp == 3) DSW(kwa, 16, kc1);
        if constexpr (pp == 5) DSW(kwa, 32, kc2);
        if constexpr (pp >= 7 && pp < 16 && (pp & 1)) { constexpr int vi = pp >= 7 ? (pp - 7) / 2 : 0; DSW(vwa, vi * 16, VCH(vi)); }
        if constexpr (pp == 17) wo24 = ldg_sc1(rs, l16, wos);
        if constexpr (pp == 18) {
          if (w == 0 && rg > 0) {
#pragma unroll
            for (int i = 0; i < 4; ++i) hd[i] = ldg_sc1(rs, l16 + i * 1024, hsrc);
          }
        }
      SEND;
      SB();
      lgkm<0>();
      mf32v<false>(a0, wq[40 % 6], mfr);
      vmc<10>();
      BARE();
      unsigned ka = kwa - 14 * 48, va = vwa - 14 * 80;
      asm volatile("" : "+v"(ka), "+v"(va));
      const float fq = rstd * (LOG2E * 0.22360679774997896f);
      STAMP(5 + 12 * l);
      if (w == 0) {
        unsigned mm = 0;
#pragma unroll
        for (int i = 0; i < 4; ++i) mm = mx4(mm, hd[i]);
        if (__builtin_expect(any_sent(mm), 0)) {
          unsigned sp = 0;
          do {
            FENCE();
            mm = 0;
#pragma unroll
            for (int i = 0; i < 4; ++i) hd[i] = ldg_sc1(rs, l16 + i * 1024, hsrc);
#pragma unroll
            for (int i = 0; i < 4; ++i) mm = mx4(mm, hd[i]);
            CNT(2);
          } while (any_sent(mm) && !give_up(sp, tm));
        }
        sts128(L, ad[3], hd[0]);
        sts128(L, ad[4], hd[1]);
        sts128(L, ad[5], hd[2]);
        sts128(L, ad[5] + 32, hd[3]);
      }
      STAMP(4 + 12 * l);
      float lg[8], rk[8];
      v4u kb[8][3], vb[8][5];
      auto rdk = [&](auto j_) __attribute__((always_inline)) { constexpr int j = decltype(j_)::v;
#pragma unroll
        for (int i = 0; i < 3; ++i) kb[j][i] = lds128(L, ka + (7 - j) * 96 + i * 16); };
      auto rdv = [&](auto j_) __attribute__((always_inline)) { constexpr int j = decltype(j_)::v;
#pragma unroll
        for (int i = 0; i < 5; ++i) vb[j][i] = lds128(L, va + (7 - j) * 160 + i * 16); };
      SB();
      rdk(IC<0>{}); rdk(IC<1>{}); rdk(IC<2>{}); rdk(IC<3>{});
      SB();
#pragma unroll
      for (int i = 0; i < 8; ++i) qp[i] = pk2(a0[2 * i], a0[2 * i + 1]);
      SFOR(jj, 4)
        SB();
        if constexpr (jj < ADM) DM(NDM2 + jj);
        if constexpr (jj < 2) { rdk(IC<2 * jj + 4>{}); rdk(IC<2 * jj + 5>{}); } else rdv(IC<jj - 2>{});
        SB();
        float s0 = 0.f, s1 = 0.f;
#pragma unroll
        for (int i = 0; i < 10; ++i) { s0 = dot2(kb[2 * jj][i >> 2][i & 3], qp[i], s0); s1 = dot2(kb[2 * jj + 1][i >> 2][i & 3], qp[i], s1); }
        asm volatile("" :: "v"(kb[2 * jj][2]), "v"(kb[2 * jj + 1][2]));
        rk[2 * jj] = u2f(kb[2 * jj][2][2]); rk[2 * jj + 1] = u2f(kb[2 * jj + 1][2][2]);
        lg[2 * jj] = s0 * (fq * rk[2 * jj]); lg[2 * jj + 1] = s1 * (fq * rk[2 * jj + 1]);
      SEND;
      SB();
      if constexpr (4 < ADM) DM(NDM2 + 4);
      rdv(IC<2>{});
      SB();
      if (rg == 0 && w == 0) {
#pragma unroll
        for (int j = 1; j < 8; ++j) lg[j] = tok >= 2 * j ? lg[j] : -1e30f;
      }
      const float mx = __builtin_fmaxf(__builtin_fmaxf(__builtin_fmaxf(lg[0], lg[1]), __builtin_fmaxf(lg[2], lg[3])), __builtin_fmaxf(__builtin_fmaxf(lg[4], lg[5]), __builtin_fmaxf(lg[6], lg[7])));
#pragma unroll
      for (int j = 0; j < 8; ++j) lg[j] -= mx;
      SB();
#pragma unroll
      for (int j = 0; j < 8; ++j) lg[j] = __builtin_amdgcn_exp2f(lg[j]);
      SB();
      if constexpr (5 < ADM) DM(NDM2 + 5);
      rdv(IC<3>{});
      SB();
      const float inv = __builtin_amdgcn_rcpf(((lg[0] + lg[1]) + (lg[2] + lg[3])) + ((lg[4] + lg[5]) + (lg[6] + lg[7])));
#pragma unroll
      for (int j = 0; j < 8; ++j) lg[j] *= rk[j];
      f32x2 oa[10];
      SFOR(j, 8)
        SB();
        if constexpr (6 + j < ADM) DM(NDM2 + 6 + j);
        if constexpr (j < 4) rdv(IC<j + 4>{});
        SB();
        const float pj = lg[j];
        SFOR(i, 5)
          const f32x4 v = __builtin_bit_cast(f32x4, vb[j][i]);
          if constexpr (j == 0) { oa[2 * i] = pkmul(f32x2{pj, pj}, f32x2{v[0], v[1]}); oa[2 * i + 1] = pkmul(f32x2{pj, pj}, f32x2{v[2], v[3]}); }
          else { oa[2 * i] = pkfma(f32x2{pj, pj}, f32x2{v[0], v[1]}, oa[2 * i]); oa[2 * i + 1] = pkfma(f32x2{pj, pj}, f32x2{v[2], v[3]}, oa[2 * i + 1]); }
        SEND;
      SEND;
#pragma unroll
      for (int i = 0; i < 10; ++i) oa[i] = pkmul(oa[i], f32x2{inv, inv});
#pragma unroll
      for (int i = 0; i < 10; ++i) { o[2 * i] = oa[i][0]; o[2 * i + 1] = oa[i][1]; }
    } else {
      RELANE();
      const float sl = rstd * LOG2E;
      const int g = lane >> 4;
      {
        const unsigned rb = ad[0];
        float kk[20];
#pragma unroll
        for (int i = 0; i < 16; ++i) kk[i] = a1[i];
#pragma unroll
        for (int i = 0; i < 4; ++i) kk[16 + i] = a3[i];
        unsigned ph[20];
        phi_map<true>(kk, sl, rstd, ph, [&](auto i_) __attribute__((always_inline)) { constexpr int i = decltype(i_)::v; SB();
          if constexpr (i < ADM) dmaw<NDM2 + i>(rw, l16, wb1k);
          if (i == 0) sts128(L, rb ^ (5 << 4), v4u{pk2(a2[0], a2[1]), pk2(a2[2], a2[3]), pk2(a2[4], a2[5]), pk2(a2[6], a2[7])});
          if (i == 1) sts128(L, rb ^ (6 << 4), v4u{pk2(a2[8], a2[9]), pk2(a2[10], a2[11]), pk2(a2[12], a2[13]), pk2(a2[14], a2[15])});
          if (i == 2) sts128(L, rb ^ (7 << 4), v4u{pk2(a3[4], a3[5]), pk2(a3[6], a3[7]), pk2(sd, 0.f), 0u});
          if (i == 5) sts128(L, rb ^ (0 << 4), v4u{ph[0], ph[1], ph[2], ph[3]});
          if (i == 6) sts128(L, rb ^ (1 << 4), v4u{ph[4], ph[5], ph[6], ph[7]});
          SB(); });
#pragma unroll
        for (int c = 2; c < 5; ++c) sts128(L, rb ^ (c << 4), v4u{ph[4 * c], ph[4 * c + 1], ph[4 * c + 2], ph[4 * c + 3]});
      }
      const unsigned bq = L_P0 + l16, bcs = L_CS + 2048 + tok * 16;
      v4u wq[6];
      {
        const unsigned aA0 = ad[1], aA2 = ad[2], aB = ad[3], so0 = ad[4], so1 = ad[5];
        unsigned aA0x = (aA0 ^ 64) + 512, aA2x = (aA2 ^ 64) + 512, aBx = (aB ^ 64) + 512;
        const int d = 16 * (w & 1) + (lane & 15);
        asm volatile("" : "+v"(aA0x), "+v"(aA2x), "+v"(aBx));
        BAR();
        f32x4 d0, d1, d2;
        v2u t[4][8];
        auto trd = [&](auto k_) __attribute__((always_inline)) {
          constexpr int ks = decltype(k_)::v; constexpr unsigned o_ = ks * 4096;
          t[ks][0] = ldstr(L, aB + o_); t[ks][1] = ldstr(L, aBx + o_); t[ks][6] = ldstr(L, aA2 + o_); t[ks][7] = ldstr(L, aA2x + o_);
          t[ks][2] = ldstr(L, aA0 + o_); t[ks][3] = ldstr(L, aA0x + o_); t[ks][4] = ldstr(L, aA0 + 8 + o_); t[ks][5] = ldstr(L, aA0x + 8 + o_);
          SB();
        };
        trd(IC<0>{});
        SFOR(ks, 4)
          if constexpr (ks < 3) trd(IC<ks + 1>{});
          const v4u bb = {t[ks][0].x, t[ks][0].y, t[ks][1].x, t[ks][1].y};
          mf16v<ks == 0>(d2, v4u{t[ks][6].x, t[ks][6].y, t[ks][7].x, t[ks][7].y}, bb);
          mf16v<ks == 0>(d0, v4u{t[ks][2].x, t[ks][2].y, t[ks][3].x, t[ks][3].y}, bb);
          mf16v<ks == 0>(d1, v4u{t[ks][4].x, t[ks][4].y, t[ks][5].x, t[ks][5].y}, bb);
          SB();
        SEND;
        SFOR(j, 6) dsr<j * 1024>(wq[j], bq); SEND;
        asm volatile("s_nop 1");
        SB();
        const unsigned e0 = pk2(d2[0], d2[1]), e1 = pk2(d2[2], d2[3]);
        auto s0 = __builtin_amdgcn_permlane16_swap(e0, e0, false, false);
        auto s1 = __builtin_amdgcn_permlane16_swap(e1, e1, false, false);
        const unsigned ks_ = WS_KV + (((l >> 1) * 16 + cg) * 16 + rg) * 4096;
        if (d < 24) {
          stg_sc1(rs, so0, ks_, v4u{pk2(d0[0], d0[1]), pk2(d0[2], d0[3]), pk2(d1[0], d1[1]), pk2(d1[2], d1[3])});
          if (g == 0) stg_sc1(rs, so1, ks_, v4u{e0, e1, s0[1], s1[1]});
        }
      }
      STAMP(4 + 12 * l);
      const unsigned ks_ = WS_KV + ((l >> 1) * 16 + cg) * 65536 + (w < 3 ? w * 1024 : 11 * 256);
      const unsigned kvv = ad[11];
      v4u kp[16];
      unsigned kpr;
      const unsigned kps = WS_KV + ((l >> 1) * 16 + cg) * 65536, kpv = (lane >> 2) * 4096 + (120 + 96 + 16) * 16 + (lane & 3) * 4;
      SFOR(pp, 20) SB(); P2PAIR(pp, (2 * pp + 6 <= 41 ? 4 : 39 - 2 * pp)); if constexpr (pp == 16) wo24 = ldg_sc1(rs, l16, wos);
        if constexpr (pp == 17) probe_issue(kpr, rs, kpv, kps);
        if constexpr (pp >= 18 && 2 * (pp - 18) < KPS) { constexpr int r = 2 * (pp - 18); kp[r] = ldg_sc1(rs, kvv, ks_ + (r & 3) * 16384 + (r >> 2) * 256); kp[r + 1] = ldg_sc1(rs, kvv, ks_ + ((r + 1) & 3) * 16384 + (r >> 2) * 256); }
      SEND;
      SB();
      lgkm<0>();
      mf32v<false>(a0, wq[40 % 6], mfr);
#pragma unroll
      for (int r = (KPS > 4 ? 4 : KPS); r < 16; ++r) kp[r] = ldg_sc1(rs, kvv + (r >> 2) * 256, ks_ + (r & 3) * 16384);
      FENCE(); SB();
      STAMP(5 + 12 * l);
      unsigned ph[20];
      float eps;
      { float qq[20];
#pragma unroll
        for (int i = 0; i < 4; ++i) qq[16 + i] = a3[8 + i];
#pragma unroll
        for (int i = 0; i < 16; ++i) qq[i] = a0[i];
        eps = 1e-6f * phi_map<false>(qq, sl, 1.f, ph, [&](auto i_) __attribute__((always_inline)) {
          if constexpr (decltype(i_)::v == 4) {
            SB();
            probe_wait<16>(kpr);
            if (__builtin_expect(any_sent(kpr), 0)) {
              CNT(3);
              poll_probe(rs, kpv, kps, tm);
#pragma unroll
              for (int r = 0; r < 16; ++r) kp[r] = ldg_sc1(rs, kvv + (r >> 2) * 256, ks_ + (r & 3) * 16384);
            }
            FENCE(); SB();
          } });
      }
      v4u f[10], sel;
      { const int r = lane & 15;
#pragma unroll
        for (int j = 0; j < 4; ++j) sel[j] = r == 2 * j ? 0x00003F80u : r == 2 * j + 1 ? 0x3F800000u : 0u;
#pragma unroll
        for (int s = 0; s < 10; ++s) f[s] = v4u{0u, 0u, 0u, 0u};
      }
#pragma unroll
      for (int i = 0; i < 20; i += 4) asm volatile("" : "+v"(ph[i]), "+v"(ph[i + 1]), "+v"(ph[i + 2]), "+v"(ph[i + 3]));
      asm volatile("" : "+v"(sel));
      asm volatile("" : "+v"(f[0]), "+v"(f[1]), "+v"(f[2]), "+v"(f[3]), "+v"(f[4]), "+v"(f[5]), "+v"(f[6]), "+v"(f[7]), "+v"(f[8]), "+v"(f[9]));
      SB();
      f32x4 sm[4];
      for (unsigned sp = 0;;) {
        SFOR(r, 16) mf16v<(r & 3) == 0>(sm[r >> 2], sel, kp[r]); SEND;
        asm volatile("s_nop 7");
        SB();
        if (__builtin_expect(!any_nan((sm[0][0] + sm[1][0]) + (sm[2][0] + sm[3][0])), 1)) break;
        if (give_up(sp, tm)) break;
        CNT(1);
        FENCE();
#pragma unroll
        for (int r = 0; r < 16; ++r) kp[r] = ldg_sc1(rs, kvv + (r >> 2) * 256, ks_ + (r & 3) * 16384);
      }
      if (lane < 32) {
#pragma unroll
        for (int j = 0; j < 4; ++j) *(LAS v2u*)(L + ad[6 + j]) = v2u{pk2(sm[j][0], sm[j][1]), pk2(sm[j][2], sm[j][3])};
      }
      const bool match = ((tok >> 2) & 1) == h;
      const unsigned aa = ad[10];
      BAR();
      if (match) {
#pragma unroll
        for (int s = 0; s < 10; ++s) f[s ^ 1] = lds128(L, aa + (s ^ 1) * 512);
      }
      f32x16 n0, n1;
      SFOR(s, 5)
        const v4u bb = {ph[4 * s], ph[4 * s + 1], ph[4 * s + 2], ph[4 * s + 3]};
        mf32v<s == 0>(n1, f[2 * s + 1], bb); mf32v<s == 0>(n0, f[2 * s], bb);
      SEND;
      MFMA_SETTLE();
      SB();
      const float inv = __builtin_amdgcn_rcpf(n1[4] + eps);
#pragma unroll
      for (int i = 0; i < 2; ++i) { const f32x2 t = pkmul(f32x2{n1[2 * i], n1[2 * i + 1]}, f32x2{inv, inv}); o[16 + 2 * i] = t[0]; o[17 + 2 * i] = t[1]; }
#pragma unroll
      for (int i = 0; i < 8; ++i) { const f32x2 t = pkmul(f32x2{n0[2 * i], n0[2 * i + 1]}, f32x2{inv, inv}); o[2 * i] = t[0]; o[2 * i + 1] = t[1]; }
    }
    {
      RELANE();
      asm volatile("" : "+v"(wo24));
      const unsigned os = WS_O + l * XB + ((rg * 8 + 2 * w) * 4 + (cg >> 2)) * 5120 + (cg & 3) * 256;
      const unsigned ov = (tok >> 4) * 20480 + (tok & 15) * 16;
      stg_sc1(rs, ov + h * 2048, os, v4u{pk2(o[0], o[1]), pk2(o[2], o[3]), pk2(o[4], o[5]), pk2(o[6], o[7])});
      stg_sc1(rs, ov + h * 2048 + 1024, os, v4u{pk2(o[8], o[9]), pk2(o[10], o[11]), pk2(o[12], o[13]), pk2(o[14], o[15])});
      const unsigned e0 = pk2(o[16], o[17]), e1 = pk2(o[18], o[19]);
      auto s0 = __builtin_amdgcn_permlane32_swap(e0, e0, false, false);
      auto s1 = __builtin_amdgcn_permlane32_swap(e1, e1, false, false);
      if (h == 0) stg_sc1(rs, ov + 4096, os, v4u{e0, e1, s0[1], s1[1]});
      STAMP(6 + 12 * l);
      if (l < 3) {
        if (POIS == 1 || POIS == 2) poison_o(rs, WS_O + (l + 1) * XB, rg, cg, w, lane);
        if (POIS == 2 && l < 2) poison_x(rs, WS_X + (l + 2) * XB, WS_ST1 + (l + 1) * STB, rg, cg, w, lane);
        SFOR(i, ODM) DM(NDM2 + ADM + i); SEND;
        if (l == 0) {
          stg_sc1(rs, l16, WS_KV + (256 + cg * 16 + rg) * 4096 + w * 1024, v4u{SENT, SENT, SENT, SENT});
          stg_sc1(rs, l16, WS_HALO + (256 + rg * 16 + cg) * 4096 + w * 1024, v4u{SENT, SENT, SENT, SENT});
          load_cs(L, rs, 1, cg, tid);
        }
        if (l < 2) vmc<ODM + 7>(); else vmc<ODM + 3>();
      } else vmc<0>();
      if constexpr (DLY_O > 0) __builtin_amdgcn_s_sleep(DLY_O);
      BARE();
    }
    {
      RELANE();
      STAMP(7 + 12 * l);
      unsigned osb[4];
#pragma unroll
      for (int i = 0; i < 4; ++i) osb[i] = WS_O + l * XB + ((rg * 8 + half * 4 + ((w + 1 + i) & 3)) * 4 + w) * 5120;
      const unsigned l16b = l16 + 1024;
#define LDO(i, s) do { if constexpr ((s) == 0) lda<20 * (i), 0, true>(rs, l16, osb[i]); else lda<20 * (i) + 4 * (s), ((s) - 1) * 1024, true>(rs, l16b, osb[i]); } while (0)
      unsigned opr;
      const unsigned ops = WS_O + l * XB + ((rg * 8 + half * 4) * 4 + w) * 5120 + 4096, opv = (lane >> 4) * 20480 + ((lane >> 2) & 3) * 256 + (lane & 3) * 4;
      probe_issue(opr, rs, opv, ops);
      SFOR(s, 5) LDO(0, s); if constexpr (s == 1) BARE(); SEND;
      BARE();
      SFOR(s, 5) LDO(1, s); SEND;
      SB();
      f32x4 acc[2][5], part[15];
      f32x2 rsum[10];
#pragma unroll
      for (int i = 0; i < 10; ++i) rsum[i] = f32x2{lo16(xr[i]), hi16(xr[i])};
      asm volatile("" : "+v"(rsum[0]), "+v"(rsum[1]), "+v"(rsum[2]), "+v"(rsum[3]), "+v"(rsum[4]), "+v"(rsum[5]), "+v"(rsum[6]), "+v"(rsum[7]), "+v"(rsum[8]), "+v"(rsum[9]));
      const unsigned rwa = L_RED + l16;
      probe_wait<10>(opr);
      if (__builtin_expect(any_sent(opr), 0)) {
        CNT(4);
        poll_probe(rs, opv, ops, tm);
        SFOR(s, 5) LDO(0, s); SEND;
        SFOR(s, 5) LDO(1, s); SEND;
      }
      auto redo = [&](auto i_) __attribute__((always_inline)) {
        constexpr int i = decltype(i_)::v;
        unsigned sp = 0;
        do {
          CNT(1);
          SFOR(s, 5) LDO(i, s); SEND;
          vmc<0>();
          SFOR(j, 25) if constexpr (j < 24) mf16aa<(j < 5), 160 + 4 * j, 20 * i + 4 * (j / 5)>(acc[i & 1][j % 5]); else mf16va<20 * i + 16>(acc[i & 1][4], wo24); SEND;
          asm volatile("s_nop 7");
          SB();
        } while (any_nan(acc[i & 1][4][0]) && !give_up(sp, tm));
      };
      constexpr int NDO = NDM2 + ADM + ODM;
      static_assert(NDO + GDM <= 22 && NDM2 + ADM + ODM <= 17 && GDM <= 10 && ADM <= 10, "");
      SFOR(i, 4)
        SFOR(j, 25)
          constexpr int s = j / 5, ft = j % 5, WS0 = 4, wstep = 4;
          SB();
          constexpr int gi = 5 * i + s;
          if constexpr (ft == 0) vmc<(gi < 10 ? 9 : 19 - gi) + (gi > 20 - GDM ? gi - (20 - GDM) : 0)>();
          if constexpr (j < 24) mf16aa<s == 0, 160 + 4 * j, 20 * i + 4 * s>(acc[i & 1][ft]); else mf16va<20 * i + 16>(acc[i & 1][ft], wo24);
          if constexpr (i == 0 && j == 0) STAMP(8 + 12 * l);
          if constexpr (ft == 2) {
            if constexpr (i < 2) LDO(i + 2, s);
            else if constexpr (gi >= 20 - GDM) dmaw<NDO + gi - (20 - GDM)>(rw, l16, wb1k);
          }
          if constexpr (i > 0 && j == WS0 - 1) { asm volatile("s_nop 1"); SB(); if (__builtin_expect(any_nan(acc[(i - 1) & 1][4][0]), 0)) redo(IC<i - 1>{}); }
          if constexpr (i > 0 && j >= WS0 && j <= WS0 + 4 * wstep && (j - WS0) % wstep == 0)
            DSW(rwa + (((w + i) & 3) * 3 + (i - 1)) * 5120, ((j - WS0) / wstep) * 1024, acc[(i - 1) & 1][(j - WS0) / wstep]);
          if constexpr (i == 3 && j == 23) { SB(); BAR(); }
          if constexpr (i == 3 && j >= 23) {
#pragma unroll
            for (int q = (j - 23) * 8; q < (j - 23) * 8 + 8 && q < 15; ++q) part[q] = __builtin_bit_cast(f32x4, lds128(L, L_RED + w * 15360 + (q % 3) * 5120 + (q / 3) * 1024 + l16));
          }
        SEND;
      SEND;
      SB();
      STAMP(9 + 12 * l);
#pragma unroll
      for (int ft = 0; ft < 5; ++ft)
#pragma unroll
        for (int q = 0; q < 3; ++q) { rsum[2 * ft] = pkadd(rsum[2 * ft], f32x2{part[ft * 3 + q][0], part[ft * 3 + q][1]}); rsum[2 * ft + 1] = pkadd(rsum[2 * ft + 1], f32x2{part[ft * 3 + q][2], part[ft * 3 + q][3]}); }
      asm volatile("" : "+v"(rsum[8]), "+v"(rsum[9]));
      SB();
      if (__builtin_expect(any_nan(acc[1][4][0]), 0)) redo(IC<3>{});
#pragma unroll
      for (int ft = 0; ft < 5; ++ft) {
        const f32x2 lo = pkadd(rsum[2 * ft], f32x2{acc[1][ft][0], acc[1][ft][1]}), hi = pkadd(rsum[2 * ft + 1], f32x2{acc[1][ft][2], acc[1][ft][3]});
        xr[2 * ft] = pk2(lo[0], lo[1]); xr[2 * ft + 1] = pk2(hi[0], hi[1]);
      }
      if (l < 3) {
        publish_x(rs, WS_X + (l + 1) * XB, WS_ST1 + l * STB, rg, cg, w, lane, xr);
        STAMP(10 + 12 * l);
        if (POIS == 1 && l < 2) poison_x(rs, WS_X + (l + 2) * XB, WS_ST1 + (l + 1) * STB, rg, cg, w, lane);
        BARE();
        SFOR(i, 22 - NDO - GDM) DM(NDO + GDM + i); SEND;
        SFOR(i, 15) DM(22 + i); SEND;
        if (w < 2) DM(37);
        addresses(m ^ 1);
        vmc<15>();
        if constexpr (DLY_X > 0) __builtin_amdgcn_s_sleep(DLY_X);
        BARE();
      } else {
        GAS v2u* dst = (GAS v2u*)(p.out + (size_t)(rg * 128 + half * 64 + w * 16 + (lane & 15)) * 640 + c8 * 80 + (lane >> 4) * 20);
#pragma unroll
        for (int i = 0; i < 5; ++i) dst[i] = v2u{xr[2 * i], xr[2 * i + 1]};
        if (b == 0 && tid == 0) __builtin_amdgcn_raw_buffer_store_b32(tm.epoch, rs, 0, WS_CTRL + 128, SC1);
      }
      STAMP(11 + 12 * l);
    }
  }
}

extern "C" void kernel_launch(void* const* d_in, const int* in_sizes, int n_in, void* d_out, int out_size, void* d_ws, size_t ws_size, hipStream_t stream) {
  Params p;
  p.x = (const u16*)d_in[0]; p.wqA = (const u16*)d_in[1]; p.woA = (const u16*)d_in[2];
  p.wqB = (const u16*)d_in[4]; p.wphi = (const u16*)d_in[5]; p.woB = (const u16*)d_in[6];
  p.out = (u16*)d_out; p.ws = (unsigned char*)d_ws;
  (void)hipFuncSetAttribute((const void*)k_main, hipFuncAttributeMaxDynamicSharedMemorySize, LDS_ALLOC);
  k_main<<<256, 256, LDS_ALLOC, stream>>>(p);
}
```

```cpp
#include <hip/hip_runtime.h>
#include <stdint.h>

typedef unsigned short u16;
typedef unsigned v4u __attribute__((ext_vector_type(4)));
typedef unsigned v2u __attribute__((ext_vector_type(2)));
typedef float f32x2 __attribute__((ext_vector_type(2)));
typedef float f32x4 __attribute__((ext_vector_type(4)));
typedef float f32x16 __attribute__((ext_vector_type(16)));
typedef __bf16 bf16x8 __attribute__((ext_vector_type(8)));
typedef __bf16 bf16x2 __attribute__((ext_vector_type(2)));
typedef short s16x4 __attribute__((ext_vector_type(4)));
#define LAS __attribute__((address_space(3)))
#define GAS __attribute__((address_space(1)))
typedef __amdgpu_buffer_rsrc_t rsrc_t;
#define DEV __device__ __forceinline__

#define DSC1 " sc1"
constexpr unsigned WQ_SZ = 153600, WO_SZ = 102400, XB = 2621440, STB = 131072;
constexpr unsigned WS_CTRL = 0;
constexpr unsigned WS_FLAG = 4096;
constexpr unsigned WS_WQ = WS_FLAG + 2 * 256 * 128;
constexpr unsigned WS_WO = WS_WQ + 32 * WQ_SZ;
constexpr unsigned WS_CSP = WS_WO + 16 * WO_SZ;
constexpr unsigned WS_ST0 = WS_CSP + 2 * 16 * 16 * 512;
constexpr unsigned WS_ST1 = WS_ST0 + STB;
constexpr unsigned WS_X = WS_ST0 + 4 * STB;
constexpr unsigned WS_O = WS_X + 4 * XB;
constexpr unsigned WS_KV = WS_O + 4 * XB;
constexpr unsigned WS_HALO = WS_KV + 2 * 256 * 4096;
constexpr unsigned WS_END = WS_HALO + 2 * 256 * 4096;
constexpr unsigned WS_STAMP = 40u << 20;
constexpr unsigned WS_TOTAL = WS_STAMP + 256 * 4 * 64 * 8;
constexpr unsigned KSTEP = 2816, L_P0 = 112640, L_CS = 153600, L_KVA = L_CS + 4096;
constexpr unsigned L_SCR = 73216;
constexpr unsigned L_K = L_SCR, L_V = L_SCR + 2 * 144 * 48;
constexpr unsigned L_RED = 90112;
constexpr unsigned LDS_BYTES = L_KVA + 4096;
constexpr unsigned SENT = 0xFFFFFFFFu;
constexpr unsigned CODE_BYTES = 24576;
constexpr int SC1 = 16;
constexpr float LOG2E = 1.4426950408889634f;

struct Params { const u16 *x, *wqA, *woA, *wqB, *wphi, *woB; u16* out; unsigned char* ws; };

DEV unsigned pk2(float a, float b) { bf16x2 v = {(__bf16)a, (__bf16)b}; return __builtin_bit_cast(unsigned, v); }
DEV float lo16(unsigned u) { return __builtin_bit_cast(float, u << 16); }
DEV float hi16(unsigned u) { return __builtin_bit_cast(float, u & 0xffff0000u); }
DEV unsigned f2u(float f) { return __builtin_bit_cast(unsigned, f); }
DEV float u2f(unsigned u) { return __builtin_bit_cast(float, u); }
DEV unsigned umax(unsigned a, unsigned b) { return __builtin_elementwise_max(a, b); }
DEV unsigned umin(unsigned a, unsigned b) { return __builtin_elementwise_min(a, b); }
DEV float max3f(float a, float b, float c) { float d; asm("v_max3_f32 %0, %1, %2, %3" : "=v"(d) : "v"(a), "v"(b), "v"(c)); return d; }
DEV float min3f(float a, float b, float c) { float d; asm("v_min3_f32 %0, %1, %2, %3" : "=v"(d) : "v"(a), "v"(b), "v"(c)); return d; }
DEV float dot2(unsigned a, unsigned b, float c) { return __builtin_amdgcn_fdot2_f32_bf16(__builtin_bit_cast(bf16x2, a), __builtin_bit_cast(bf16x2, b), c, false); }
DEV f32x2 pkfma(f32x2 a, f32x2 b, f32x2 c) { return __builtin_elementwise_fma(a, b, c); }
DEV f32x2 pkmul(f32x2 a, f32x2 b) { return a * b; }
DEV f32x2 pkadd(f32x2 a, f32x2 b) { return a + b; }
DEV f32x16 mfma32(v4u a, v4u b, f32x16 c) { return __builtin_amdgcn_mfma_f32_32x32x16_bf16(__builtin_bit_cast(bf16x8, a), __builtin_bit_cast(bf16x8, b), c, 0, 0, 0); }
DEV f32x4 mfma16(v4u a, v4u b, f32x4 c) { return __builtin_amdgcn_mfma_f32_16x16x32_bf16(__builtin_bit_cast(bf16x8, a), __builtin_bit_cast(bf16x8, b), c, 0, 0, 0); }
DEV v4u lds128(LAS unsigned char* L, unsigned a) { return *(LAS v4u*)(L + a); }
DEV v2u lds64(LAS unsigned char* L, unsigned a) { return *(LAS v2u*)(L + a); }
DEV void sts128(LAS unsigned char* L, unsigned a, v4u v) { *(LAS v4u*)(L + a) = v; }
DEV v2u ldstr(LAS unsigned char* L, unsigned a) { return __builtin_bit_cast(v2u, __builtin_amdgcn_ds_read_tr16_b64_v4i16((LAS s16x4*)(L + a))); }
DEV v4u ldg(rsrc_t rs, unsigned voff, unsigned soff) { return __builtin_amdgcn_raw_buffer_load_b128(rs, voff, soff, 0); }
DEV v4u ldg_sc1(rsrc_t rs, unsigned voff, unsigned soff) { return __builtin_amdgcn_raw_buffer_load_b128(rs, voff, soff, SC1); }
DEV v2u ldg64_sc1(rsrc_t rs, unsigned voff, unsigned soff) { return __builtin_amdgcn_raw_buffer_load_b64(rs, voff, soff, SC1); }
DEV void stg_sc1(rsrc_t rs, unsigned voff, unsigned soff, v4u v) { __builtin_amdgcn_raw_buffer_store_b128(v, rs, voff, soff, SC1); }
DEV unsigned mx4(unsigned m, v4u d) { return umax(umax(umax(m, d.x), d.y), umax(d.z, d.w)); }
DEV unsigned max3u(unsigned a, unsigned b, unsigned c) { return umax(umax(a, b), c); }
DEV bool any_sent(unsigned m) { return __builtin_amdgcn_ballot_w64(m == SENT) != 0; }
#define BAR() asm volatile("s_waitcnt lgkmcnt(0)\n\ts_barrier" ::: "memory")
#define DRAIN() asm volatile("s_waitcnt vmcnt(0)" ::: "memory")
#define FENCE() asm volatile("" ::: "memory")
#define SB() __builtin_amdgcn_sched_barrier(0)
DEV void dma4(rsrc_t rs, unsigned voff, unsigned soff, unsigned ldsa) {
  asm volatile("s_mov_b32 m0, %0\n\ts_nop 0\n\tbuffer_load_dwordx4 %1, %2, %3 offen sc1 lds\n\tbuffer_load_dwordx4 %1, %2, %3 offen offset:1024 sc1 lds\n\t"
               "buffer_load_dwordx4 %1, %2, %3 offen offset:2048 sc1 lds\n\tbuffer_load_dwordx4 %1, %2, %3 offen offset:3072 sc1 lds"
               :: "s"(__builtin_amdgcn_readfirstlane(ldsa)), "v"(voff), "s"(rs), "s"(__builtin_amdgcn_readfirstlane(soff)) : "memory");
}
DEV void dma1(rsrc_t rs, unsigned voff, unsigned soff, unsigned ldsa) {
  asm volatile("s_mov_b32 m0, %0\n\ts_nop 0\n\tbuffer_load_dwordx4 %1, %2, %3 offen sc1 lds"
               :: "s"(__builtin_amdgcn_readfirstlane(ldsa)), "v"(voff), "s"(rs), "s"(__builtin_amdgcn_readfirstlane(soff)) : "memory");
}
DEV void dma_range(rsrc_t rs, unsigned l16, unsigned wsrc, int w, int k0, int k1) {
  int k = k0 + 4 * w;
  for (; k + 4 <= k1; k += 16) dma4(rs, l16, wsrc + k * 1024, k * 1024);
  for (; k < k1; ++k) dma1(rs, l16, wsrc + k * 1024, k * 1024);
}
struct Tmo { unsigned char* ws; unsigned epoch; };
DEV bool give_up(unsigned& sp, const Tmo& tm) {
  ++sp;
  if (sp > 32) __builtin_amdgcn_s_sleep(8);
  if ((sp & 63u) == 0u) {
    GAS unsigned* t = (GAS unsigned*)(tm.ws + WS_CTRL);
    if (sp >= (1u << 16)) { __hip_atomic_store(t, tm.epoch, __ATOMIC_RELAXED, __HIP_MEMORY_SCOPE_AGENT); return true; }
    if (__builtin_amdgcn_readfirstlane(__hip_atomic_load(t, __ATOMIC_RELAXED, __HIP_MEMORY_SCOPE_AGENT)) == tm.epoch) return true;
  }
  return false;
}
#define MAPV 1
#define XW 10
DEV int map_rg(int b) { return ((b & 7) >> 1) * 4 + (b >> 6); }
DEV int map_cg(int b) { return (b & 1) * 8 + ((b >> 3) & 7); }
DEV int o_half(int cg) { return (cg >> 2) & 1; }
DEV int o_c8(int cg) { return (cg >> 3) * 4 + (cg & 3); }
DEV int xfeat(int c, int j) { int c8 = c / 10, l = c % 10; return 80 * c8 + (l < 8 ? 20 * (l >> 1) + 8 * (l & 1) + j : 40 * (l - 8) + 20 * (j >> 2) + 16 + (j & 3)); }
DEV int ocol(int cgp, int s, int j) { return 40 * cgp + (s < 4 ? 20 * (s >> 1) + 8 * (s & 1) + j : 20 * (j >> 2) + 16 + (j & 3)); }
DEV void pin_s(unsigned& v) { asm volatile("" : "+s"(v)); }
DEV int fresh_lane() { int l; asm volatile("v_mbcnt_lo_u32_b32 %0, -1, 0\n\tv_mbcnt_hi_u32_b32 %0, -1, %0" : "=v"(l)); return l; }
#define RELANE() const int lane = fresh_lane(), h = lane >> 5, tok = lane & 31, tid = w * 64 + lane; const unsigned l16 = lane * 16; (void)h; (void)tok; (void)tid; (void)l16
#define STAMP(i) do {} while (0)
#define LDS_ALLOC LDS_BYTES
#define CNT(i) do {} while (0)

DEV void publish_x(rsrc_t rs, unsigned xbuf, unsigned stbuf, int rg, int cg, int w, int lane, const unsigned (&xr)[10]) {
  const int g = lane >> 4, tok = lane & 15, half = o_half(cg), c8 = o_c8(cg);
  const unsigned sb = xbuf + ((rg * 4 + half * 2 + (w >> 1)) * 40 + 5 * c8) * 1024;
  const unsigned vb = ((w & 1) * 16 + tok) * 16;
  stg_sc1(rs, vb + g * 1024, sb, v4u{xr[0], xr[1], xr[2], xr[3]});
  stg_sc1(rs, vb + g * 1024 + 512, sb, v4u{xr[4], xr[5], xr[6], xr[7]});
  auto s0 = __builtin_amdgcn_permlane16_swap(xr[8], xr[8], false, false);
  auto s1 = __builtin_amdgcn_permlane16_swap(xr[9], xr[9], false, false);
  if (!(g & 1)) stg_sc1(rs, vb + 4096 + (g >> 1) * 512, sb, v4u{xr[8], xr[9], s0[1], s1[1]});
  SB();
  float sa = 0.f, sq = 0.f;
#pragma unroll
  for (int i = 0; i < 10; ++i) { sa = dot2(xr[i], 0x3F803F80u, sa); sq = dot2(xr[i], xr[i], sq); }
  { auto a = __builtin_amdgcn_permlane16_swap(f2u(sa), f2u(sa), false, false); sa = u2f(a[0]) + u2f(a[1]);
    auto b = __builtin_amdgcn_permlane16_swap(f2u(sq), f2u(sq), false, false); sq = u2f(b[0]) + u2f(b[1]); }
  { auto a = __builtin_amdgcn_permlane32_swap(f2u(sa), f2u(sa), false, false); sa = u2f(a[0]) + u2f(a[1]);
    auto b = __builtin_amdgcn_permlane32_swap(f2u(sq), f2u(sq), false, false); sq = u2f(b[0]) + u2f(b[1]); }
  if (g == 0) __builtin_amdgcn_raw_buffer_store_b64(v2u{f2u(sa), f2u(sq)}, rs, tok * 8, stbuf + ((rg * 8 + c8) * 128 + half * 64 + w * 16) * 8, SC1);
}
DEV void load_x_in(const u16* x, int rg, int cg, int w, int lane, unsigned (&xr)[10]) {
  const int g = lane >> 4, tok = lane & 15;
  const GAS v2u* src = (const GAS v2u*)(x + (size_t)(rg * 128 + o_half(cg) * 64 + w * 16 + tok) * 640 + o_c8(cg) * 80 + g * 20);
#pragma unroll
  for (int i = 0; i < 5; ++i) { v2u t = src[i]; xr[2 * i] = t.x; xr[2 * i + 1] = t.y; }
}

DEV void poison_x(rsrc_t rs, unsigned xbuf, unsigned stbuf, int rg, int cg, int w, int lane) {
  const int g = lane >> 4, tok = lane & 15, half = o_half(cg), c8 = o_c8(cg);
  const unsigned sb = xbuf + ((rg * 4 + half * 2 + (w >> 1)) * 40 + 5 * c8) * 1024, vb = ((w & 1) * 16 + tok) * 16;
  const v4u P = {SENT, SENT, SENT, SENT};
  stg_sc1(rs, vb + g * 1024, sb, P); stg_sc1(rs, vb + g * 1024 + 512, sb, P); stg_sc1(rs, vb + 4096 + (g >> 1) * 512, sb, P);
  __builtin_amdgcn_raw_buffer_store_b64(v2u{SENT, SENT}, rs, tok * 8, stbuf + ((rg * 8 + c8) * 128 + half * 64 + w * 16) * 8, SC1);
}
DEV void poison_o(rsrc_t rs, unsigned obuf, int rg, int cg, int w, int lane) {
  const int h = lane >> 5, tok = lane & 31;
  const unsigned os = obuf + ((rg * 8 + 2 * w) * 4 + (cg >> 2)) * 5120 + (cg & 3) * 256, ov = (tok >> 4) * 20480 + (tok & 15) * 16;
  const v4u P = {SENT, SENT, SENT, SENT};
  stg_sc1(rs, ov + h * 2048, os, P); stg_sc1(rs, ov + h * 2048 + 1024, os, P); stg_sc1(rs, ov + 4096, os, P);
}
DEV void wait_flags(rsrc_t rs, unsigned voff, unsigned soff, const Tmo& tm) {
  unsigned sp = 0, v;
  do { FENCE(); v = __builtin_amdgcn_raw_buffer_load_b32(rs, voff, soff, SC1); } while (__builtin_amdgcn_ballot_w64(v != tm.epoch) != 0 && !give_up(sp, tm));
}
DEV void build_wq_read(LAS unsigned char* L, unsigned stage, int w, int lane, v4u (&fr)[3]) {
  const int gam = lane >> 4, q = (lane >> 2) & 3, pp = lane & 3, a = 2 * (gam & 1) + (pp >> 1), h = pp & 1;
  const int eoff = (w < 3) ? 40 * w + 20 * h + 4 * a : (a == 0 ? 40 : a == 1 ? 80 : 0) + 20 * h + 16;
#pragma unroll
  for (int it = 0; it < 3; ++it) {
    const int kap = 2 * it + (gam >> 1), kc = kap < 4 ? kap : 4;
    const unsigned ad = stage + (kc * 8 + q) * 256 + eoff * 2;
    const v2u t0 = ldstr(L, ad), t1 = ldstr(L, ad + 1024);
    fr[it] = v4u{t0.x, t0.y, t1.x, t1.y};
  }
}
DEV float sum8(v4u v, float c) { c = dot2(v.x, 0x3F803F80u, c); c = dot2(v.y, 0x3F803F80u, c); c = dot2(v.z, 0x3F803F80u, c); return dot2(v.w, 0x3F803F80u, c); }
DEV void build_wq_store(rsrc_t rs, int m, int rg, int cg, int w, int lane, int r0, int r1, const v4u (&fr)[3]) {
  const int row = lane & 31, hi = lane >> 5;
  const unsigned tb = w == 0 ? L_P0 : (w - 1) * 1024, ts = w == 0 ? 1024 : KSTEP, th = w == 3 ? 384 : 512;
  const bool in = row >= r0 && row < r1;
#pragma unroll
  for (int it = 0; it < 3; ++it) {
    const int c = 5 * rg + 2 * it + hi;
    stg_sc1(rs, (in && (it < 2 || !hi)) ? (c >> 1) * ts + (c & 1) * th + row * 16 : 0x80000000u, WS_WQ + (m * 16 + cg) * WQ_SZ + tb, fr[it]);
  }
  float cs = sum8(fr[1], sum8(fr[0], 0.f));
  cs = hi ? cs : sum8(fr[2], cs);
  auto t = __builtin_amdgcn_permlane32_swap(f2u(cs), f2u(cs), false, false);
  __builtin_amdgcn_raw_buffer_store_b32(f2u(u2f(t[0]) + u2f(t[1])), rs, (in && !hi) ? row * 4 : 0x80000000u, WS_CSP + ((m * 16 + cg) * 16 + rg) * 512 + w * 128, SC1);
}
DEV void load_cs(LAS unsigned char* L, rsrc_t rs, int m, int cg, int tid) {
  if (tid < 128) {
    float cs = 0.f;
#pragma unroll
    for (int r = 0; r < 16; ++r) cs += u2f(__builtin_amdgcn_raw_buffer_load_b32(rs, tid * 4 + (r & 7) * 512, WS_CSP + (m * 16 + cg) * 8192 + (r >> 3) * 4096, SC1));
    const unsigned ch = pk2(cs, 0.f) & 0xffffu, cl = pk2(cs - lo16(ch), 0.f) & 0xffffu;
    sts128(L, L_CS + m * 2048 + tid * 16, v4u{ch | (cl << 16), ch, 0u, 0u});
  }
}

DEV void probe_issue(unsigned& d, rsrc_t rs, unsigned voff, unsigned soff) {
  asm volatile("buffer_load_dword %0, %1, %2, %3 offen sc1" : "=v"(d) : "v"(voff), "s"(rs), "s"(soff) : "memory");
}
template <int N> DEV void probe_wait(unsigned& d) { asm volatile("s_waitcnt vmcnt(%1)" : "+v"(d) : "i"(N) : "memory"); }
#define PSL 2
template <int R> DEV unsigned probe_turn(rsrc_t rs, unsigned voff, unsigned soff) {
  unsigned v;
  asm volatile("buffer_load_dword a%c4, %1, %2, %3 offen sc1\n\ts_waitcnt vmcnt(1)\n\tv_accvgpr_read_b32 %0, a%c5" : "=v"(v) : "v"(voff), "s"(rs), "s"(soff), "i"(R), "i"(R ^ 1) : "memory");
  return v;
}
template <int R> DEV void poll_probe(rsrc_t rs, unsigned voff, unsigned soff, const Tmo& tm) {
  unsigned sp = 0;
  asm volatile("buffer_load_dword a%c3, %0, %1, %2 offen sc1" :: "v"(voff), "s"(rs), "s"(soff), "i"(R) : "memory");
  for (;;) {
    __builtin_amdgcn_s_sleep(PSL);
    if (!any_sent(probe_turn<R + 1>(rs, voff, soff))) break;
    __builtin_amdgcn_s_sleep(PSL);
    if (!any_sent(probe_turn<R>(rs, voff, soff))) break;
    if (give_up(sp, tm)) break;
  }
}
template <int I> struct IC { static constexpr int v = I; };
template <int I, int N, class F> DEV void sfor(F&& f) { if constexpr (I < N) { f(IC<I>{}); sfor<I + 1, N>(f); } }
#define SFOR(var, n) sfor<0, n>([&](auto var##_) __attribute__((always_inline)) { constexpr int var = decltype(var##_)::v;
#define SEND })
template <bool NORM, class F> DEV float phi_map(const float (&a)[20], float s, float post, unsigned (&ph)[20], F slot) {
  float mx = max3f(a[0], a[1], a[2]), mn = min3f(a[0], a[1], a[2]), mx2 = max3f(a[3], a[4], a[5]), mn2 = min3f(a[3], a[4], a[5]);
#pragma unroll
  for (int i = 6; i < 18; i += 4) { mx = max3f(mx, a[i], a[i + 1]); mn = min3f(mn, a[i], a[i + 1]); mx2 = max3f(mx2, a[i + 2], a[i + 3]); mn2 = min3f(mn2, a[i + 2], a[i + 3]); }
  mx = max3f(mx, a[18], a[19]); mn = min3f(mn, a[18], a[19]); mx = max3f(mx, mx2, mx2); mn = min3f(mn, mn2, mn2);
  const float bp = -s * mx, bn = s * mn, ns = -s;
  float sp = 0.f;
  SFOR(sg, 2)
    const f32x2 sc = sg ? f32x2{ns, ns} : f32x2{s, s}, bi = sg ? f32x2{bn, bn} : f32x2{bp, bp};
    f32x2 e[10];
    slot(IC<5 * sg>{});
#pragma unroll
    for (int i = 0; i < 10; ++i) e[i] = pkfma(f32x2{a[2 * i], a[2 * i + 1]}, sc, bi);
    SB();
    SFOR(i, 10)
      if constexpr (i == 3 || i == 7) slot(IC<5 * sg + 1 + (i >> 2)>{});
      e[i] = f32x2{__builtin_amdgcn_exp2f(e[i][0]), __builtin_amdgcn_exp2f(e[i][1])};
    SEND;
    f32x2 s0 = pkadd(e[0], e[1]), s1 = pkadd(e[2], e[3]), s2 = pkadd(e[4], e[5]);
    s0 = pkadd(s0, e[6]); s1 = pkadd(s1, e[7]); s2 = pkadd(s2, e[8]); s0 = pkadd(s0, e[9]);
    slot(IC<5 * sg + 3>{});
    if constexpr (!NORM && sg == 0) {
#pragma unroll
      for (int i = 0; i < 10; ++i) ph[i] = pk2(e[i][0], e[i][1]);
    }
    s0 = pkadd(pkadd(s0, s1), s2);
    const float sm = s0[0] + s0[1];
    if constexpr (NORM || sg == 1) {
      const float r = __builtin_amdgcn_rcpf(sm) * (NORM ? post : sp);
#pragma unroll
      for (int i = 0; i < 10; ++i) { f32x2 t = pkmul(e[i], f32x2{r, r}); ph[10 * sg + i] = pk2(t[0], t[1]); }
    }
    sp = sg ? sp : sm;
    slot(IC<5 * sg + 4>{});
  SEND;
  return sp;
}

#define BARE() asm volatile("s_barrier" ::: "memory")
template <int OFF> DEV void dsr(v4u& d, unsigned addr) { asm volatile("ds_read_b128 %0, %1 offset:%2" : "=v"(d) : "v"(addr), "i"(OFF)); }
template <int R, int OFF, bool COH> DEV void lda(rsrc_t rs, unsigned voff, unsigned soff) {
  if constexpr (COH) asm volatile("buffer_load_dwordx4 a[%c0:%c1], %2, %3, %4 offen offset:%5 sc1" :: "i"(R), "i"(R + 3), "v"(voff), "s"(rs), "s"(soff), "i"(OFF) : "memory");
  else asm volatile("buffer_load_dwordx4 a[%c0:%c1], %2, %3, %4 offen offset:%5" :: "i"(R), "i"(R + 3), "v"(voff), "s"(rs), "s"(soff), "i"(OFF) : "memory");
}
DEV void sbump(unsigned& so) { asm volatile("s_add_u32 %0, %0, 0x1000" : "+s"(so) :: "scc"); }
template <bool Z, int R> DEV void mf32a(f32x16& c, const v4u& a) {
  if constexpr (Z) asm volatile("v_mfma_f32_32x32x16_bf16 %0, %1, a[%c2:%c3], 0" : "=&v"(c) : "v"(a), "i"(R), "i"(R + 3));
  else asm volatile("v_mfma_f32_32x32x16_bf16 %0, %1, a[%c2:%c3], %0" : "+v"(c) : "v"(a), "i"(R), "i"(R + 3));
}
template <bool Z> DEV void mf32v(f32x16& c, const v4u& a, const v4u& b) {
  if constexpr (Z) asm volatile("v_mfma_f32_32x32x16_bf16 %0, %1, %2, 0" : "=&v"(c) : "v"(a), "v"(b));
  else asm volatile("v_mfma_f32_32x32x16_bf16 %0, %1, %2, %0" : "+v"(c) : "v"(a), "v"(b));
}
template <bool Z, int R> DEV void mf16a(f32x4& c, const v4u& b) {
  if constexpr (Z) asm volatile("v_mfma_f32_16x16x32_bf16 %0, a[%c2:%c3], %1, 0" : "=&v"(c) : "v"(b), "i"(R), "i"(R + 3));
  else asm volatile("v_mfma_f32_16x16x32_bf16 %0, a[%c2:%c3], %1, %0" : "+v"(c) : "v"(b), "i"(R), "i"(R + 3));
}
template <bool Z> DEV void mf16v(f32x4& c, const v4u& a, const v4u& b) {
  if constexpr (Z) asm volatile("v_mfma_f32_16x16x32_bf16 %0, %1, %2, 0" : "=&v"(c) : "v"(a), "v"(b));
  else asm volatile("v_mfma_f32_16x16x32_bf16 %0, %1, %2, %0" : "+v"(c) : "v"(a), "v"(b));
}
template <int N> DEV void vmc() { asm volatile("s_waitcnt vmcnt(%0)" :: "i"(N) : "memory"); }
template <int I> DEV void dmaw(rsrc_t rw, unsigned l16, unsigned wb) {
  asm volatile("s_or_b32 m0, %0, %1\n\ts_nop 0\n\tbuffer_load_dwordx4 %2, %3, m0 offen" DSC1 " lds" :: "s"(wb), "i"(I * 4096), "v"(l16), "s"(rw) : "memory", "scc");
}
template <int N> DEV void lgkm() { asm volatile("s_waitcnt lgkmcnt(%0)" :: "i"(N)); }
#define MFMA_SETTLE() asm volatile("s_nop 7\n\ts_nop 3")
DEV bool any_nan(float x) { return __builtin_amdgcn_ballot_w64(x != x) != 0; }
#define PFD 3

template <int S, int NW, int VM, int VI> DEV void p2pair(f32x16& a0, v4u& w0, v4u& w1, unsigned bq, unsigned bcs, rsrc_t r, unsigned l16, unsigned so) {
#define P2_M0 "s_waitcnt lgkmcnt(%c[nw])\n\tv_mfma_f32_32x32x16_bf16 %[c], %[w0], a[%c[r0]:%c[r1]], "
#define P2_R0 "\n\tds_read_b128 %[w0], %[a0] offset:%c[o0]\n\t"
#define P2_M1 "v_mfma_f32_32x32x16_bf16 %[c], %[w1], a[%c[r2]:%c[r3]], %[c]"
#define P2_R1 "\n\tds_read_b128 %[w1], %[bq] offset:%c[o1]"
#define P2_IN [nw] "i"(NW), [r0] "i"(4 * S), [r1] "i"(4 * S + 3), [r2] "i"(4 * S + 4), [r3] "i"(4 * S + 7), [a0] "v"(S + 6 == 40 ? bcs : bq), [bq] "v"(bq), \
              [o0] "i"(S + 6 < 40 ? (S + 6) * 1024 : 0), [o1] "i"((S + 7) * 1024), [rs] "s"(r), [l16] "v"(l16), [so] "s"(so), [d0] "i"(160 + 4 * VI), [d1] "i"(163 + 4 * VI), \
              [vo] "i"((VI & 3) * 1024), [dm] "i"(VI * 4096)
#define P2_LD "buffer_load_dwordx4 a[%c[d0]:%c[d1]], %[l16], %[rs], %[so] offen offset:%c[vo] sc1\n\t"
#define P2_DMA "s_or_b32 m0, %[so], %[dm]\n\t"
#define P2_DMB "buffer_load_dwordx4 %[l16], %[rs], m0 offen" DSC1 " lds\n\t"
  if constexpr (S == 0) {
    static_assert(S != 0 || VM == 0, "");
    asm volatile(P2_M0 "0" P2_R0 P2_M1 P2_R1 : [c] "=&v"(a0), [w0] "+v"(w0), [w1] "+v"(w1) : P2_IN : "memory");
  } else if constexpr (S + 7 < 40) {
    if constexpr (VM == 0) asm volatile(P2_M0 "%[c]" P2_R0 P2_M1 P2_R1 : [c] "+v"(a0), [w0] "+v"(w0), [w1] "+v"(w1) : P2_IN : "memory");
    else if constexpr (VM == 1) asm volatile(P2_M0 "%[c]" P2_R0 P2_LD P2_M1 P2_R1 : [c] "+v"(a0), [w0] "+v"(w0), [w1] "+v"(w1) : P2_IN : "memory");
    else asm volatile(P2_M0 "%[c]\n\t" P2_DMA "ds_read_b128 %[w0], %[a0] offset:%c[o0]\n\t" P2_DMB P2_M1 P2_R1 : [c] "+v"(a0), [w0] "+v"(w0), [w1] "+v"(w1) : P2_IN : "memory", "scc");
  } else if constexpr (S + 6 == 40) {
    static_assert(S + 6 != 40 || VM == 0, "");
    asm volatile(P2_M0 "%[c]" P2_R0 P2_M1 : [c] "+v"(a0), [w0] "+v"(w0), [w1] "+v"(w1) : P2_IN : "memory");
  } else {
    static_assert(VM == 0, "");
    asm volatile(P2_M0 "%[c]\n\t" P2_M1 : [c] "+v"(a0), [w0] "+v"(w0), [w1] "+v"(w1) : P2_IN : "memory");
  }
}
template <bool Z, int RA, int RB> DEV void mf16aa(f32x4& c) {
  if constexpr (Z) asm volatile("v_mfma_f32_16x16x32_bf16 %0, a[%c1:%c2], a[%c3:%c4], 0" : "=v"(c) : "i"(RA), "i"(RA + 3), "i"(RB), "i"(RB + 3));
  else asm volatile("v_mfma_f32_16x16x32_bf16 %0, a[%c1:%c2], a[%c3:%c4], %0" : "+v"(c) : "i"(RA), "i"(RA + 3), "i"(RB), "i"(RB + 3));
}
template <int RB> DEV void mf16va(f32x4& c, const v4u& a) { asm volatile("v_mfma_f32_16x16x32_bf16 %0, %1, a[%c2:%c3], %0" : "+v"(c) : "v"(a), "i"(RB), "i"(RB + 3)); }
#define DSW(addr, off, val) asm volatile("ds_write_b128 %0, %1 offset:%2" :: "v"(addr), "v"(val), "i"(off) : "memory")
#define ADM 10
#define ODM 5
#define GDM 7
#define DLY_O 3
#define DLY_X 4
#define HLP 17
#define KPS 8

__global__ void __launch_bounds__(256, 1) k_main(Params p) {
  extern __shared__ __attribute__((aligned(16))) unsigned char smem[];
  LAS unsigned char* L = (LAS unsigned char*)smem;
  {
    const unsigned long long pc = __builtin_amdgcn_s_getpc();
    const unsigned t16 = __builtin_amdgcn_workitem_id_x() * 16;
    asm volatile("global_load_dwordx4 a[0:3], %0, %1\n\tglobal_load_dwordx4 a[4:7], %0, %1 offset:4080" :: "v"(t16), "s"(pc) : "memory");
    asm volatile("global_load_dwordx4 a[8:11], %0, %1" :: "v"(t16 + 8192 + ((__builtin_amdgcn_workgroup_id_x() >> 3) & 3) * 4096), "s"(pc) : "memory");
    SB();
  }
  const int b = __builtin_amdgcn_workgroup_id_x(), w = __builtin_amdgcn_readfirstlane(__builtin_amdgcn_workitem_id_x() >> 6);
  const int rg = map_rg(b), cg = map_cg(b), half = o_half(cg), c8 = o_c8(cg);
  const rsrc_t rs = __builtin_amdgcn_make_buffer_rsrc(p.ws, 0, WS_TOTAL, 0x00020000);
  asm volatile("" ::: "a255");
  unsigned vKa, vVa, vHv, vImg, vA0, vA2, vB, vS0, vS1, vK0, vK1, vF0, vF1, vKv, vKp, vOv, vOp, vXv, vSt, vB3;
  auto addresses = [&]() __attribute__((always_inline)) {
    RELANE();
    const int g = lane >> 4, hh = w >> 1, dt = w & 1, q = (lane >> 2) & 3, p4 = lane & 3, r = lane & 15;
    const unsigned r_ = h * 144 + 2 + 32 * w + tok;
    vKa = L_K + r_ * 48; vVa = L_V + r_ * 80; vHv = (w == 3 && tok >= 16) ? (h * 16 + (tok - 16)) * 16 : 0x80000000u;
    const unsigned t_ = 32 * w + tok, key = (t_ & 7) ^ (((t_ >> 3) & 1) << 2);
    vImg = L_SCR + (h * 128 + t_) * 128 + (key << 4);
    const unsigned k0 = q ^ ((g & 1) << 2), rb0 = L_SCR + (hh * 128 + 8 * g + q) * 128, cB = dt ? 7 : 5 + (p4 >> 1);
    vA0 = rb0 + ((p4 ^ k0) << 4); vA2 = rb0 + ((4 ^ k0) << 4) + (p4 & 1) * 8; vB = rb0 + ((cB ^ k0) << 4) + (p4 & 1) * 8;
    vS0 = dt ? (r < 8 ? 2560 + ((hh * 5 + g) * 8 + r) * 16 : 0x80000000u) : ((hh * 5 + g) * 16 + r) * 16;
    vS1 = g ? 0x80000000u : dt ? (r < 8 ? 2560 + ((hh * 5 + 4) * 8 + r) * 16 : 0x80000000u) : ((hh * 5 + 4) * 16 + r) * 16;
    const unsigned ty0 = L_KVA + r * 16 + g * 8, ty1 = L_KVA + (r >> 3) * 400 + 256 + (r & 7) * 16 + g * 8;
    vK0 = w < 3 ? ty0 + w * 1600 : ty1 + 800;
    vK1 = w < 2 ? ty0 + w * 1600 + 800 : ty1 + (w == 2 ? 0 : 2400);
    const bool match = ((tok >> 2) & 1) == h;
    const unsigned row = 4 * (tok >> 3) + (tok & 3);
    vF0 = L_KVA + h * 2000 + (match ? row * 16 : 384); vF1 = L_KVA + h * 2000 + (match && row < 8 ? 256 + row * 16 : 384);
    vKv = g * 4096 + r * 16; vKp = (lane >> 2) * 4096 + 3712 + (lane & 3) * 4;
    vOv = (tok >> 4) * 20480 + (tok & 15) * 16 + h * 2048; vOp = g * 20480 + q * 256 + p4 * 4;
    vXv = ((w & 1) * 16 + r) * 16 + g * 1024; vSt = h * 4096 + tok * 8; vB3 = 2048 + h * 384 + tok * 16;
    asm volatile("" : "+v"(vKa), "+v"(vVa), "+v"(vHv), "+v"(vImg), "+v"(vA0), "+v"(vA2), "+v"(vB), "+v"(vS0), "+v"(vS1), "+v"(vK0));
    asm volatile("" : "+v"(vK1), "+v"(vF0), "+v"(vF1), "+v"(vKv), "+v"(vKp), "+v"(vOv), "+v"(vOp), "+v"(vXv), "+v"(vSt), "+v"(vB3));
    if (tid < 10) sts128(L, L_KVA + tid * 400 + 384, v4u{0u, 0u, 0u, 0u});
  };
  unsigned xr[10];
  Tmo tm;
  { RELANE();
    const int l = 0; (void)l;
    STAMP(0);
    constexpr unsigned SG_B = L_P0 + 2048, SG_O = SG_B + 10240, SG_P = SG_O + 10240, OOR = 0x80000000u;
    const int j = (tid >> 4) & 7, cc = lane & 15, seg = (cc * 13) >> 6, g = lane >> 4;
    v4u ra[3], rb[3], ro[3], rp;
    const unsigned wcol = (seg * 640 + 40 * cg + 8 * (cc - 5 * seg)) * 2;
    unsigned wrow[3];
#pragma unroll
    for (int i = 0; i < 3; ++i) {
      const int kap = 2 * i + (w >> 1) < 4 ? 2 * i + (w >> 1) : 4, lc = 5 * (rg & 1) + kap;
      const int t8 = lc >> 3, rb0 = 80 * (rg >> 1) + (1 - t8) * (20 * (lc >> 1) + 8 * (lc & 1)) + t8 * (40 * (lc - 8) + 16);
      wrow[i] = (rb0 + j + t8 * (16 * (j >> 2))) * 3840 + wcol;
    }
    { const rsrc_t rA = __builtin_amdgcn_make_buffer_rsrc((void*)p.wqA, 0, 640 * 1920 * 2, 0x00020000);
#pragma unroll
      for (int i = 0; i < 3; ++i) ra[i] = ldg(rA, wrow[i], 0); }
    load_x_in(p.x, rg, cg, w, lane, xr);
    const unsigned ep = __builtin_amdgcn_raw_buffer_load_b32(rs, 0, WS_CTRL + 128, SC1);
    FENCE(); SB();
    { const rsrc_t rB = __builtin_amdgcn_make_buffer_rsrc((void*)p.wqB, 0, 640 * 1920 * 2, 0x00020000);
      const rsrc_t rO = __builtin_amdgcn_make_buffer_rsrc((void*)(half ? p.woB : p.woA), 0, 640 * 640 * 2, 0x00020000);
      const rsrc_t rP = __builtin_amdgcn_make_buffer_rsrc((void*)(p.wphi + cg * 800), 0, 1600, 0x00020000);
#pragma unroll
      for (int i = 0; i < 3; ++i) rb[i] = ldg(rB, wrow[i], 0);
#pragma unroll
      for (int i = 0; i < 3; ++i) { const int so = 2 * i + (w >> 1) < 4 ? 2 * i + (w >> 1) : 4, t4 = so >> 2;
        ro[i] = ldg(rO, (40 * rg + (1 - t4) * (20 * (so >> 1) + 8 * (so & 1)) + t4 * 16 + j + t4 * (16 * (j >> 2))) * 1280 + (80 * c8 + 8 * (cc < 10 ? cc : 0)) * 2, 0); }
      rp = ldg(rP, tid * 16, 0);
    FENCE(); SB();
    STAMP(53);
    const unsigned sa = tid * 16;
#pragma unroll
    for (int i = 0; i < 3; ++i) if (i < 2 || w < 2) sts128(L, sa + i * 4096, ra[i]);
    BAR();
    STAMP(54);
    { v4u fr[3];
      build_wq_read(L, 0, w, lane, fr);
      publish_x(rs, WS_X, WS_ST0, rg, cg, w, lane, xr);
      build_wq_store(rs, 0, rg, cg, w, lane, 0, w < 3 ? 32 : 24, fr); }
    tm.ws = p.ws; tm.epoch = __builtin_amdgcn_readfirstlane(ep) + 0x9E3779B1u;
    STAMP(55);
#pragma unroll
    for (int i = 0; i < 3; ++i) if (i < 2 || w < 2) { sts128(L, SG_B + sa + i * 4096, rb[i]); sts128(L, SG_O + sa + i * 4096, ro[i]); }
    if (w < 2) sts128(L, SG_P + sa, rp);
    }
    DRAIN();
    BAR();
    if (tid == 0) __builtin_amdgcn_raw_buffer_store_b32(tm.epoch, rs, 0, WS_FLAG + (cg * 16 + rg) * 128, SC1);
    STAMP(60);
    v4u fv[4], fb[3], fo[2];
    {
      const int sg = w >> 1, hw = w & 1;
      v4u bf[2], af[4];
#pragma unroll
      for (int ct = 0; ct < 2; ++ct) {
        const int q = (lane >> 2) & 3, pp = lane & 3, fcol = ct ? 16 : 4 * pp;
        const int d0 = 8 * g + q < 19 ? 8 * g + q : 19, d1 = 8 * g + 4 + q < 19 ? 8 * g + 4 + q : 19;
        const v2u t0 = ldstr(L, SG_P + hw * 800 + d0 * 40 + fcol * 2), t1 = ldstr(L, SG_P + hw * 800 + d1 * 40 + fcol * 2);
        bf[ct] = v4u{g < 3 ? t0.x : 0u, g < 3 ? t0.y : 0u, g < 2 ? t1.x : 0u, g < 2 ? t1.y : 0u};
      }
#pragma unroll
      for (int i = 0; i < 4; ++i) { const int r = (i < 2 ? 8 * (cc >> 2) : 32) + 4 * (i & 1) + (cc & 3); const unsigned a = SG_B + r * 256 + sg * 80 + hw * 40 + g * 16;
        const v2u t0 = lds64(L, a), t1 = lds64(L, a + 8); af[i] = v4u{t0.x, t0.y, t1.x, t1.y}; }
      build_wq_read(L, SG_B, w, lane, fb);
      { const int q = (lane >> 2) & 3, pp = lane & 3;
#pragma unroll
        for (int it = 0; it < 2; ++it) {
          const int pc = it * 16 + (tid >> 4), pcc = pc < 25 ? pc : 24, so = (pcc * 13) >> 6, ft = pcc - 5 * so;
          const unsigned a = SG_O + (so * 8 + q) * 256 + 40 * pp + 8 * ft;
          const v2u t0 = ldstr(L, a), t1 = ldstr(L, a + 1024);
          fo[it] = v4u{t0.x, t0.y, t1.x, t1.y};
        } }
      f32x4 d[8];
      asm volatile("s_nop 1" : "+v"(bf[0]), "+v"(bf[1]), "+v"(af[0]), "+v"(af[1]), "+v"(af[2]), "+v"(af[3]));
      SFOR(i, 8) mf16v<true>(d[i], af[i & 3], bf[i >> 2]); SEND;
      asm volatile("s_nop 7");
      SB();
#pragma unroll
      for (int i = 0; i < 4; ++i) fv[i] = v4u{pk2(d[2 * i][0], d[2 * i][1]), pk2(d[2 * i][2], d[2 * i][3]), pk2(d[2 * i + 1][0], d[2 * i + 1][1]), pk2(d[2 * i + 1][2], d[2 * i + 1][3])};
    }
    STAMP(58);
    { const int sg = w >> 1, hw = w & 1;
#pragma unroll
      for (int ct = 0; ct < 2; ++ct) {
        const int row = ct ? (sg ? 0 : 16) + 4 * hw + (cc & 3) : 8 * (cc >> 2) + 4 * hw + (cc & 3);
        const unsigned tb = ct ? 2048 : (sg ? 0 : L_P0), ts = (ct || sg) ? KSTEP : 1024, th = ct ? 384 : 512;
        const bool in = ct == 0 || cc < 4;
#pragma unroll
        for (int u = 0; u < 2; ++u) { const int c = 5 * rg + (u ? 4 : g);
          stg_sc1(rs, (in && (u == 0 || g == 0)) ? (c >> 1) * ts + (c & 1) * th + row * 16 : OOR, WS_WQ + (16 + cg) * WQ_SZ + tb, fv[2 * ct + u]); }
        float cs = sum8(fv[2 * ct], 0.f);
        cs = g ? cs : sum8(fv[2 * ct + 1], cs);
        { auto t = __builtin_amdgcn_permlane16_swap(f2u(cs), f2u(cs), false, false); cs = u2f(t[0]) + u2f(t[1]); }
        { auto t = __builtin_amdgcn_permlane32_swap(f2u(cs), f2u(cs), false, false); cs = u2f(t[0]) + u2f(t[1]); }
        __builtin_amdgcn_raw_buffer_store_b32(f2u(cs), rs, (in && g == 0) ? row * 4 : OOR, WS_CSP + ((16 + cg) * 16 + rg) * 512 + (ct ? 3 : sg) * 128, SC1);
      }
      if (w >= 2) build_wq_store(rs, 1, rg, cg, w, lane, w == 2 ? 0 : 8, w == 2 ? 32 : 16, fb);
#pragma unroll
      for (int it = 0; it < 2; ++it) { const int pc = it * 16 + (tid >> 4);
        stg_sc1(rs, pc < 25 ? pc * 1024 + cc * 16 : OOR, WS_WO + (half * 8 + c8) * WO_SZ + (rg >> 2) * 25600 + (rg & 3) * 256, fo[it]); }
    }
    poison_o(rs, WS_O, rg, cg, w, lane);
    stg_sc1(rs, l16, WS_KV + (cg * 16 + rg) * 4096 + w * 1024, v4u{SENT, SENT, SENT, SENT});
    stg_sc1(rs, l16, WS_HALO + (rg * 16 + cg) * 4096 + w * 1024, v4u{SENT, SENT, SENT, SENT});
    addresses();
    STAMP(61);
    if (w == 1) wait_flags(rs, (h == 0 ? cc * 16 + rg : cg * 16 + cc) * 128, WS_FLAG, tm);
    BAR();
    STAMP(62);
    const rsrc_t rw = __builtin_amdgcn_make_buffer_rsrc(p.ws + WS_WQ + cg * WQ_SZ, 0, WQ_SZ, 0x00020000);
    SFOR(i, 37) dmaw<i>(rw, l16, w * 1024); SEND;
    if (w < 2) dmaw<37>(rw, l16, w * 1024);
    float csv[16];
#pragma unroll
    for (int r = 0; r < 16; ++r) csv[r] = u2f(__builtin_amdgcn_raw_buffer_load_b32(rs, (tid & 127) * 4 + (r & 7) * 512, WS_CSP + cg * 8192 + (r >> 3) * 4096, SC1));
    FENCE(); SB();
    STAMP(63);
    FENCE(); SB();
    STAMP(57);
    { float cs = 0.f;
#pragma unroll
      for (int r = 0; r < 16; ++r) cs += csv[r];
      const unsigned ch = pk2(cs, 0.f) & 0xffffu, cl = pk2(cs - lo16(ch), 0.f) & 0xffffu;
      asm volatile("" :: "v"(cs));
      if (tid < 128) sts128(L, L_CS + tid * 16, v4u{ch | (cl << 16), ch, 0u, 0u}); }
    BAR();
    if (tid == 0) __builtin_amdgcn_raw_buffer_store_b32(tm.epoch, rs, 0, WS_FLAG + (256 + cg * 16 + rg) * 128, SC1);
  }

  unsigned sX = WS_X + (rg * 4 + w) * 40960, sSt = WS_ST0 + (rg * 1024 + w * 32) * 8, sWo = WS_WO + (c8 * 4 + w) * 25600;
  unsigned sOp = WS_O + ((rg * 8 + 2 * w) * 4 + (cg >> 2)) * 5120 + (cg & 3) * 256, sOr = WS_O + ((rg * 8 + half * 4) * 4 + w) * 5120;
  unsigned sXp = WS_X + XB + ((rg * 4 + half * 2 + (w >> 1)) * 40 + 5 * c8) * 1024, sSp = WS_ST1 + ((rg * 8 + c8) * 128 + half * 64 + w * 16) * 8;
  pin_s(sX); pin_s(sSt); pin_s(sWo); pin_s(sOp); pin_s(sOr); pin_s(sXp); pin_s(sSp);
  for (int l = 0; l < 4; ++l) {
    const int m = l & 1;
    const rsrc_t rw = __builtin_amdgcn_make_buffer_rsrc(p.ws + WS_WQ + ((m ^ 1) * 16 + cg) * WQ_SZ, 0, l < 3 ? WQ_SZ : 0u, 0x00020000);
    const unsigned wb1k = w * 1024;
#define DM(i) do { SB(); dmaw<(i)>(rw, l16, wb1k); SB(); } while (0)
    unsigned wos;
#define LDWO(i) do { lda<160 + 4 * (i), ((i) & 3) * 1024, true>(rs, l16, wos); if constexpr (((i) & 3) == 3) sbump(wos); } while (0)
    v4u wo24, mfr;
    f32x16 a0, a1, a2, a3;
    float rstd, sd, o[20];
    {
      RELANE();
      STAMP(1 + 12 * l);
      asm volatile("" : "+v"(vSt), "+v"(vB3));
      const unsigned xs = sX, sts = sSt;
#define LDX(s) do { lda<4 * (s), ((s) & 3) * 1024, true>(rs, l16, xso); if constexpr (((s) & 3) == 3) sbump(xso); } while (0)
      const unsigned stv = vSt;
      constexpr int NB = PFD + 1, HI = 23;
      constexpr int XR = 15 - XW;
      const unsigned b12 = l16, b3 = vB3;
      unsigned b12h, b3h, bcs;
      unsigned f1 = 0, f1v = 0;
      for (unsigned att = 0, att2 = 0, fresh = 1;; fresh = 0) {
        v2u st[4];
        unsigned xso = xs;
        wos = sWo;
        asm volatile("buffer_load_dwordx2 %0, %4, %5, %6 offen sc1\n\tbuffer_load_dwordx2 %1, %4, %5, %6 offen offset:1024 sc1\n\t"
                     "buffer_load_dwordx2 %2, %4, %5, %6 offen offset:2048 sc1\n\tbuffer_load_dwordx2 %3, %4, %5, %6 offen offset:3072 sc1"
                     : "=&v"(st[0]), "=&v"(st[1]), "=&v"(st[2]), "=&v"(st[3]) : "v"(stv), "s"(rs), "s"(sts) : "memory");
        SFOR(s, XW) SB(); LDX(s); if constexpr (s == 1 || s == 5) { SB(); if (fresh) BARE(); } SEND;
        FENCE(); SB();
        v4u wb[NB][3];
        auto rd = [&](auto s_, auto t_) __attribute__((always_inline)) {
          constexpr int s = decltype(s_)::v, t = decltype(t_)::v;
          if constexpr (s < 40) dsr<(s < HI ? s : s - HI) * KSTEP + (t == 1 ? 1024 : 0)>(wb[s % NB][t], t == 2 ? (s < HI ? b3 : b3h) : (s < HI ? b12 : b12h));
          else if constexpr (s == 40) dsr<(t + 1) * 512>(wb[s % NB][t], bcs);
        };
        SFOR(j, PFD) rd(IC<j>{}, IC<2>{}); rd(IC<j>{}, IC<1>{}); rd(IC<j>{}, IC<0>{}); SEND;
        asm volatile("s_waitcnt vmcnt(%4)" : "+v"(st[0]), "+v"(st[1]), "+v"(st[2]), "+v"(st[3]) : "i"(XW) : "memory");
        unsigned ms = max3u(max3u(st[0].x, st[0].y, st[1].x), max3u(st[1].y, st[2].x, st[2].y), umax(st[3].x, st[3].y));
        if (__builtin_expect(any_sent(ms), 0)) {
          CNT(0);
          if (++att2 < 64) {
            poll_probe<200>(rs, (lane >> 3) * 1024 + (lane & 4) * 32 + 4, sts, tm);
            lgkm<0>();
            continue;
          }
        }
        float sa, sq, nm;
        auto stat = [&](auto s_) __attribute__((always_inline)) {
          constexpr int s = decltype(s_)::v;
          if constexpr (s == 1) { sa = u2f(st[0].x) + u2f(st[1].x); asm volatile("" : "+v"(sa)); sq = u2f(st[0].y) + u2f(st[1].y); asm volatile("" : "+v"(sq)); }
          if constexpr (s == 2) { sa += u2f(st[2].x); asm volatile("" : "+v"(sa)); sq += u2f(st[2].y); asm volatile("" : "+v"(sq)); }
          if constexpr (s == 3) { sa += u2f(st[3].x); asm volatile("" : "+v"(sa)); sq += u2f(st[3].y); asm volatile("" : "+v"(sq)); }
          if constexpr (s == 5) { auto x0 = __builtin_amdgcn_permlane32_swap(f2u(sa), f2u(sa), false, false); nm = u2f(x0[0]) + u2f(x0[1]); asm volatile("" : "+v"(nm)); }
          if constexpr (s == 6) { auto x1 = __builtin_amdgcn_permlane32_swap(f2u(sq), f2u(sq), false, false); sq = u2f(x1[0]) + u2f(x1[1]); asm volatile("" : "+v"(sq)); }
          if constexpr (s == 7) { nm *= -1.f / 640.f; sq = sq * (1.f / 640.f) - nm * nm; asm volatile("" : "+v"(nm), "+v"(sq)); }
          if constexpr (s == 8) { sq = __builtin_fmaxf(sq, 0.f) + 1e-5f; rstd = __builtin_amdgcn_rsqf(sq); asm volatile("" : "+v"(rstd), "+v"(sq)); }
          if constexpr (s == 9) { sd = sq * rstd; mfr.x = pk2(nm, nm); asm volatile("" : "+v"(sd), "+v"(mfr.x)); }
          if constexpr (s == 10) { mfr.y = pk2(nm - lo16(mfr.x), 0.f); asm volatile("" : "+v"(mfr.y)); }
          if constexpr (s == 11) { mfr = v4u{h ? 0u : mfr.x, h ? 0u : mfr.y, 0u, 0u}; asm volatile("" : "+v"(mfr)); }
          if constexpr (s == 12) { b12h = b12 + HI * KSTEP; b3h = b3 + HI * KSTEP; asm volatile("" : "+v"(b12h), "+v"(b3h)); }
          if constexpr (s == 13) { bcs = L_CS + m * 2048 + (l16 & 0x1f0); asm volatile("" : "+v"(bcs)); }
        };
        SFOR(s, 40)
          SB();
          if constexpr (s == 20) {
            if (l == 0) { const int ln = fresh_lane(); f1v = (ln < 16 ? ln * 16 + rg : (ln < 32 ? cg : (c8 >> 2) * 8 + ((ln >> 4) & 1) * 4 + (c8 & 3)) * 16 + (ln & 15)) * 128; probe_issue(f1, rs, f1v, WS_FLAG + 256 * 128); }
            SB();
          }
          if constexpr (s == 25) {
            if (l == 0) {
              probe_wait<5>(f1);
              if (__builtin_amdgcn_ballot_w64(f1 != tm.epoch) != 0) wait_flags(rs, f1v, WS_FLAG + 256 * 128, tm);
            }
            if (att == 0) BARE();
            SB();
          }
          constexpr int c = s % NB;
          constexpr int issued = s <= XR ? XW + 2 * s : (15 + s < 40 ? 15 + s : 40);
          asm volatile("s_waitcnt vmcnt(%0) lgkmcnt(%1)" :: "i"(issued - 1 - s + (s > 25 ? s - 25 : 0)), "i"(3 * ((s + PFD <= 41 ? PFD : 41 - s) - 1)) : "memory");
          mf32a<s == 0, 4 * s>(a1, wb[c][0]);
          if constexpr (s == 0) STAMP(2 + 12 * l);
          rd(IC<s + PFD>{}, IC<2>{});
          if constexpr (s < XR) LDX(issued);
          SB();
          mf32a<s == 0, 4 * s>(a2, wb[c][1]);
          rd(IC<s + PFD>{}, IC<1>{});
          stat(IC<s>{});
          SB();
          mf32a<s == 0, 4 * s>(a3, wb[c][2]);
          rd(IC<s + PFD>{}, IC<0>{});
                    if constexpr (s < 25) LDX(issued + (s < XR ? 1 : 0)); else LDWO(s - 25);
        SEND;
        SB();
        lgkm<0>();
        mf32v<false>(a1, wb[40 % NB][0], mfr); mf32v<false>(a3, wb[40 % NB][2], mfr); mf32v<false>(a2, wb[40 % NB][1], mfr);
        MFMA_SETTLE();
        SB();
        if (__builtin_expect(!any_nan(a1[0]), 1)) break;
        CNT(1);
        if (++att > 32) break;
      }
      STAMP(3 + 12 * l);
    }
    BARE();
    if (l == 0) { RELANE(); poison_x(rs, WS_X + XB, WS_ST1, rg, cg, w, lane); }
#define P2VM(pp) ((pp) % 3 == 2 || (pp) == 0 || (pp) > 16 ? 0 : (((pp) - (pp) / 3 - 1) < 9 ? 1 : 0))
#define P2VI(pp) (P2VM(pp) == 1 ? 15 + ((pp) - (pp) / 3 - 1) : P2VM(pp) == 2 ? ((pp) - (pp) / 3 - 1) - 9 : 0)
#define P2PAIR(pp, nw) do { p2pair<2 * (pp), nw, P2VM(pp), P2VI(pp)>(a0, wq[(2 * (pp)) % 6], wq[(2 * (pp) + 1) % 6], bq, bcs, P2VM(pp) == 2 ? rw : rs, l16, P2VM(pp) == 2 ? wb1k : wos); \
                            if constexpr (P2VM(pp) == 1 && (P2VI(pp) & 3) == 3) sbump(wos); } while (0)
    constexpr int NDM2 = 0;
    if (m == 0) {
      RELANE();
      const unsigned bq = L_P0 + l16, bcs = L_CS + tok * 16;
      asm volatile("" : "+v"(vKa), "+v"(vVa), "+v"(vHv));
      v4u wq[6], kc0, kc1, kc2, hd[4];
      const unsigned hsrc = WS_HALO + (((l >> 1) * 16 + rg - 1) * 16 + cg) * 4096;
#define VCH(i) ((i) < 4 ? v4u{f2u(a2[4 * (i)]), f2u(a2[4 * (i) + 1]), f2u(a2[4 * (i) + 2]), f2u(a2[4 * (i) + 3])} : v4u{f2u(a3[4]), f2u(a3[5]), f2u(a3[6]), f2u(a3[7])})
      SFOR(j, 6) dsr<j * 1024>(wq[j], bq); SEND;
      {
        f32x2 t[10];
#pragma unroll
        for (int i = 0; i < 8; ++i) t[i] = pkmul(f32x2{a1[2 * i], a1[2 * i + 1]}, f32x2{rstd, rstd});
#pragma unroll
        for (int i = 0; i < 2; ++i) t[8 + i] = pkmul(f32x2{a3[2 * i], a3[2 * i + 1]}, f32x2{rstd, rstd});
        kc0 = v4u{pk2(t[0][0], t[0][1]), pk2(t[1][0], t[1][1]), pk2(t[2][0], t[2][1]), pk2(t[3][0], t[3][1])};
        kc1 = v4u{pk2(t[4][0], t[4][1]), pk2(t[5][0], t[5][1]), pk2(t[6][0], t[6][1]), pk2(t[7][0], t[7][1])};
        kc2 = v4u{pk2(t[8][0], t[8][1]), pk2(t[9][0], t[9][1]), 0u, 0u};
#pragma unroll
        for (int i = 0; i < 8; ++i) { const f32x2 u = pkmul(f32x2{a2[2 * i], a2[2 * i + 1]}, f32x2{rstd, rstd}); a2[2 * i] = u[0]; a2[2 * i + 1] = u[1]; }
#pragma unroll
        for (int i = 2; i < 4; ++i) { const f32x2 u = pkmul(f32x2{a3[2 * i], a3[2 * i + 1]}, f32x2{rstd, rstd}); a3[2 * i] = u[0]; a3[2 * i + 1] = u[1]; }
        asm volatile("" : "+v"(kc0), "+v"(kc1), "+v"(kc2), "+v"(a2), "+v"(a3)); }
      unsigned qp[10];
      const rsrc_t rh = __builtin_amdgcn_make_buffer_rsrc(p.ws + hsrc, 0, rg > 0 ? 4096u : 0u, 0x00020000);
      SFOR(pp, 20)
        SB();
        constexpr int nwr = (pp - 1 >= 1 ? ((pp - 1 > 15 ? 15 : pp - 1) + 1) / 2 : 0) - (pp - 4 >= 1 ? ((pp - 4 > 15 ? 15 : pp - 4) + 1) / 2 : 0);
        P2PAIR(pp, (2 * pp + 6 <= 41 ? 4 : 39 - 2 * pp) + nwr);
        if constexpr (pp == 0) {
          if (w == 3) {
            const unsigned hs = hsrc + 65536;
            stg_sc1(rs, vHv, hs, kc0); stg_sc1(rs, vHv + 512, hs, kc1); stg_sc1(rs, vHv + 1024, hs, kc2);
#pragma unroll
            for (int i = 0; i < 5; ++i) stg_sc1(rs, vHv + 1536 + i * 512, hs, VCH(i));
          }
        }
        if constexpr (pp == 1) DSW(vKa, 14 * 48, kc0);
        if constexpr (pp == 3) DSW(vKa, 14 * 48 + 16, kc1);
        if constexpr (pp == 5) DSW(vKa, 14 * 48 + 32, kc2);
        if constexpr (pp >= 7 && pp < 16 && (pp & 1)) { constexpr int vi = pp >= 7 ? (pp - 7) / 2 : 0; DSW(vVa, 14 * 80 + vi * 16, VCH(vi)); }
        if constexpr (pp == 12) { qp[8] = pk2(a3[8], a3[9]); qp[9] = pk2(a3[10], a3[11]); asm volatile("" : "+v"(qp[8]), "+v"(qp[9])); }
        if constexpr (pp == 17) wo24 = ldg_sc1(rs, l16, wos);
        if constexpr (pp == HLP) {
          if (w == 0) {
#pragma unroll
            for (int i = 0; i < 4; ++i) hd[i] = ldg_sc1(rh, l16 + i * 1024, 0);
          }
        }
      SEND;
      SB();
      lgkm<0>();
      mf32v<false>(a0, wq[40 % 6], mfr);
      vmc<10>();
      BARE();
      const unsigned ka = vKa, va = vVa;
      const float fq = rstd * (LOG2E * 0.22360679774997896f);
      STAMP(5 + 12 * l);
      if (w == 0) {
        const unsigned q_ = ((lane >> 4) & 1) * 144 + (lane & 15);
        unsigned h0 = L_K + q_ * 48 + h * 16, h1 = h ? L_V + q_ * 80 : L_K + q_ * 48 + 32, h2 = L_V + q_ * 80 + 16 + h * 16;
        asm volatile("" : "+v"(h0), "+v"(h1), "+v"(h2));
        unsigned mm = 0;
#pragma unroll
        for (int i = 0; i < 4; ++i) mm = mx4(mm, hd[i]);
        if (__builtin_expect(any_sent(mm), 0)) {
          unsigned sp = 0;
          do {
            FENCE();
            mm = 0;
#pragma unroll
            for (int i = 0; i < 4; ++i) hd[i] = ldg_sc1(rh, l16 + i * 1024, 0);
#pragma unroll
            for (int i = 0; i < 4; ++i) mm = mx4(mm, hd[i]);
            CNT(2);
          } while (any_sent(mm) && !give_up(sp, tm));
        }
        sts128(L, h0, hd[0]);
        sts128(L, h1, hd[1]);
        sts128(L, h2, hd[2]);
        sts128(L, h2 + 32, hd[3]);
      }
      STAMP(4 + 12 * l);
      float lg[8];
      v4u kb[8][2], vb[8][5];
      v2u kt[8];
      auto rdk = [&](auto j_) __attribute__((always_inline)) { constexpr int j = decltype(j_)::v;
        kb[j][0] = lds128(L, ka + (7 - j) * 96); kb[j][1] = lds128(L, ka + (7 - j) * 96 + 16); kt[j] = lds64(L, ka + (7 - j) * 96 + 32); };
      auto rdv = [&](auto j_) __attribute__((always_inline)) { constexpr int j = decltype(j_)::v;
#pragma unroll
        for (int i = 0; i < 5; ++i) vb[j][i] = lds128(L, va + (7 - j) * 160 + i * 16); };
      SB();
      rdk(IC<0>{}); rdk(IC<1>{}); rdk(IC<2>{}); rdk(IC<3>{});
      SB();
#pragma unroll
      for (int i = 0; i < 8; ++i) qp[i] = pk2(a0[2 * i], a0[2 * i + 1]);
      SFOR(jj, 4)
        SB();
        if constexpr (jj < ADM) DM(NDM2 + jj);
        if constexpr (jj < 2) { rdk(IC<2 * jj + 4>{}); rdk(IC<2 * jj + 5>{}); } else rdv(IC<jj - 2>{});
        SB();
        float s0 = 0.f, s1 = 0.f;
#pragma unroll
        for (int i = 0; i < 8; ++i) { s0 = dot2(kb[2 * jj][i >> 2][i & 3], qp[i], s0); s1 = dot2(kb[2 * jj + 1][i >> 2][i & 3], qp[i], s1); }
#pragma unroll
        for (int i = 0; i < 2; ++i) { s0 = dot2(kt[2 * jj][i], qp[8 + i], s0); s1 = dot2(kt[2 * jj + 1][i], qp[8 + i], s1); }
        asm volatile("" : "+v"(s0), "+v"(s1));
        lg[2 * jj] = s0; lg[2 * jj + 1] = s1;
      SEND;
      SB();
      if constexpr (4 < ADM) DM(NDM2 + 4);
      rdv(IC<2>{});
      SB();
      if (rg == 0 && w == 0) {
#pragma unroll
        for (int j = 1; j < 8; ++j) lg[j] = tok >= 2 * j ? lg[j] : -__builtin_inff();
      }
      const float mx = max3f(max3f(lg[0], lg[1], lg[2]), max3f(lg[3], lg[4], lg[5]), max3f(lg[6], lg[7], lg[7]));
      const float nb = -fq * mx;
#pragma unroll
      for (int j = 0; j < 8; j += 2) { const f32x2 t = pkfma(f32x2{lg[j], lg[j + 1]}, f32x2{fq, fq}, f32x2{nb, nb}); lg[j] = t[0]; lg[j + 1] = t[1]; }
      SB();
#pragma unroll
      for (int j = 0; j < 8; ++j) lg[j] = __builtin_amdgcn_exp2f(lg[j]);
      SB();
      if constexpr (5 < ADM) DM(NDM2 + 5);
      SB();
      const float inv = __builtin_amdgcn_rcpf(((lg[0] + lg[1]) + (lg[2] + lg[3])) + ((lg[4] + lg[5]) + (lg[6] + lg[7])));
      f32x2 oa[10];
      SFOR(j, 8)
        SB();
        if constexpr (6 + j < ADM) DM(NDM2 + 6 + j);
        if constexpr (j < 5) rdv(IC<j + 3>{});
        SB();
        const float pj = lg[j];
        SFOR(i, 5)
          const f32x4 v = __builtin_bit_cast(f32x4, vb[j][i]);
          if constexpr (j == 0) { oa[2 * i] = pkmul(f32x2{pj, pj}, f32x2{v[0], v[1]}); oa[2 * i + 1] = pkmul(f32x2{pj, pj}, f32x2{v[2], v[3]}); }
          else { oa[2 * i] = pkfma(f32x2{pj, pj}, f32x2{v[0], v[1]}, oa[2 * i]); oa[2 * i + 1] = pkfma(f32x2{pj, pj}, f32x2{v[2], v[3]}, oa[2 * i + 1]); }
        SEND;
      SEND;
#pragma unroll
      for (int i = 0; i < 10; ++i) oa[i] = pkmul(oa[i], f32x2{inv, inv});
#pragma unroll
      for (int i = 0; i < 10; ++i) { o[2 * i] = oa[i][0]; o[2 * i + 1] = oa[i][1]; }
    } else {
      RELANE();
      asm volatile("" : "+v"(vImg), "+v"(vA0), "+v"(vA2), "+v"(vB), "+v"(vS0), "+v"(vS1), "+v"(vK0), "+v"(vK1), "+v"(vF0), "+v"(vF1), "+v"(vKv), "+v"(vKp));
      const float sl = rstd * LOG2E;
      {
        const unsigned rb = vImg;
        float kk[20];
#pragma unroll
        for (int i = 0; i < 16; ++i) kk[i] = a1[i];
#pragma unroll
        for (int i = 0; i < 4; ++i) kk[16 + i] = a3[i];
        unsigned ph[20];
        phi_map<true>(kk, sl, rstd, ph, [&](auto i_) __attribute__((always_inline)) { constexpr int i = decltype(i_)::v; SB();
          if constexpr (i < ADM) dmaw<NDM2 + i>(rw, l16, wb1k);
          if (i == 0) sts128(L, rb ^ (5 << 4), v4u{pk2(a2[0], a2[1]), pk2(a2[2], a2[3]), pk2(a2[4], a2[5]), pk2(a2[6], a2[7])});
          if (i == 1) sts128(L, rb ^ (6 << 4), v4u{pk2(a2[8], a2[9]), pk2(a2[10], a2[11]), pk2(a2[12], a2[13]), pk2(a2[14], a2[15])});
          if (i == 2) sts128(L, rb ^ (7 << 4), v4u{pk2(a3[4], a3[5]), pk2(a3[6], a3[7]), pk2(sd, 0.f), 0u});
          if (i == 5) sts128(L, rb ^ (0 << 4), v4u{ph[0], ph[1], ph[2], ph[3]});
          if (i == 6) sts128(L, rb ^ (1 << 4), v4u{ph[4], ph[5], ph[6], ph[7]});
          SB(); });
#pragma unroll
        for (int c = 2; c < 5; ++c) sts128(L, rb ^ (c << 4), v4u{ph[4 * c], ph[4 * c + 1], ph[4 * c + 2], ph[4 * c + 3]});
      }
      const unsigned bq = L_P0 + l16, bcs = L_CS + 2048 + tok * 16;
      v4u wq[6];
      {
        const unsigned aA0 = vA0, aA2 = vA2, aB = vB;
        unsigned aA0x = (aA0 ^ 64) + 512, aA2x = (aA2 ^ 64) + 512, aBx = (aB ^ 64) + 512;
        asm volatile("" : "+v"(aA0x), "+v"(aA2x), "+v"(aBx));
        BAR();
        f32x4 d0, d1, d2;
        v2u t[4][8];
        auto trd = [&](auto k_) __attribute__((always_inline)) {
          constexpr int ks = decltype(k_)::v; constexpr unsigned o_ = ks * 4096;
          t[ks][0] = ldstr(L, aB + o_); t[ks][1] = ldstr(L, aBx + o_); t[ks][6] = ldstr(L, aA2 + o_); t[ks][7] = ldstr(L, aA2x + o_);
          t[ks][2] = ldstr(L, aA0 + o_); t[ks][3] = ldstr(L, aA0x + o_); t[ks][4] = ldstr(L, aA0 + 8 + o_); t[ks][5] = ldstr(L, aA0x + 8 + o_);
          SB();
        };
        trd(IC<0>{});
        SFOR(ks, 4)
          if constexpr (ks < 3) trd(IC<ks + 1>{});
          const v4u bb = {t[ks][0].x, t[ks][0].y, t[ks][1].x, t[ks][1].y};
          mf16v<ks == 0>(d2, v4u{t[ks][6].x, t[ks][6].y, t[ks][7].x, t[ks][7].y}, bb);
          mf16v<ks == 0>(d0, v4u{t[ks][2].x, t[ks][2].y, t[ks][3].x, t[ks][3].y}, bb);
          mf16v<ks == 0>(d1, v4u{t[ks][4].x, t[ks][4].y, t[ks][5].x, t[ks][5].y}, bb);
          SB();
        SEND;
        SFOR(j, 6) dsr<j * 1024>(wq[j], bq); SEND;
        asm volatile("s_nop 1");
        SB();
        const unsigned e0 = pk2(d2[0], d2[1]), e1 = pk2(d2[2], d2[3]);
        auto s0 = __builtin_amdgcn_permlane16_swap(e0, e0, false, false);
        auto s1 = __builtin_amdgcn_permlane16_swap(e1, e1, false, false);
        const unsigned ks_ = WS_KV + (((l >> 1) * 16 + cg) * 16 + rg) * 4096;
        stg_sc1(rs, vS0, ks_, v4u{pk2(d0[0], d0[1]), pk2(d0[2], d0[3]), pk2(d1[0], d1[1]), pk2(d1[2], d1[3])});
        stg_sc1(rs, vS1, ks_, v4u{e0, e1, s0[1], s1[1]});
      }
      STAMP(4 + 12 * l);
      const unsigned ks_ = WS_KV + ((l >> 1) * 16 + cg) * 65536 + (w < 3 ? w * 1024 : 11 * 256);
      const unsigned kvv = vKv;
      v4u kp[16];
      unsigned kpr;
      const unsigned kps = WS_KV + ((l >> 1) * 16 + cg) * 65536, kpv = vKp;
      SFOR(pp, 20) SB(); P2PAIR(pp, (2 * pp + 6 <= 41 ? 4 : 39 - 2 * pp)); if constexpr (pp == 16) wo24 = ldg_sc1(rs, l16, wos);
        if constexpr (pp == 17) probe_issue(kpr, rs, kpv, kps);
        if constexpr (pp >= 18 && 2 * (pp - 18) < KPS) { constexpr int r = 2 * (pp - 18); kp[r] = ldg_sc1(rs, kvv, ks_ + (r & 3) * 16384 + (r >> 2) * 256); kp[r + 1] = ldg_sc1(rs, kvv, ks_ + ((r + 1) & 3) * 16384 + (r >> 2) * 256); }
      SEND;
      SB();
      lgkm<0>();
      mf32v<false>(a0, wq[40 % 6], mfr);
#pragma unroll
      for (int r = (KPS > 4 ? 4 : KPS); r < 16; ++r) kp[r] = ldg_sc1(rs, kvv + (r >> 2) * 256, ks_ + (r & 3) * 16384);
      FENCE(); SB();
      STAMP(5 + 12 * l);
      unsigned ph[20];
      float eps;
      { float qq[20];
#pragma unroll
        for (int i = 0; i < 4; ++i) qq[16 + i] = a3[8 + i];
#pragma unroll
        for (int i = 0; i < 16; ++i) qq[i] = a0[i];
        eps = 1e-6f * phi_map<false>(qq, sl, 1.f, ph, [&](auto i_) __attribute__((always_inline)) {
          if constexpr (decltype(i_)::v == 4) {
            SB();
            probe_wait<16>(kpr);
            if (__builtin_expect(any_sent(kpr), 0)) {
              CNT(3);
              poll_probe<100>(rs, kpv, kps, tm);
#pragma unroll
              for (int r = 0; r < 16; ++r) kp[r] = ldg_sc1(rs, kvv + (r >> 2) * 256, ks_ + (r & 3) * 16384);
            }
            FENCE(); SB();
          } });
      }
      v4u f[10], sel;
      { const int r = lane & 15;
#pragma unroll
        for (int j = 0; j < 4; ++j) sel[j] = r == 2 * j ? 0x00003F80u : r == 2 * j + 1 ? 0x3F800000u : 0u;
      }
#pragma unroll
      for (int i = 0; i < 20; i += 4) asm volatile("" : "+v"(ph[i]), "+v"(ph[i + 1]), "+v"(ph[i + 2]), "+v"(ph[i + 3]));
      asm volatile("s_nop 1" : "+v"(sel));
      SB();
      f32x4 sm[4];
      for (unsigned sp = 0;;) {
        SFOR(r, 16) mf16v<(r & 3) == 0>(sm[r >> 2], sel, kp[r]); SEND;
        asm volatile("s_nop 7");
        SB();
        if (__builtin_expect(!any_nan((sm[0][0] + sm[1][0]) + (sm[2][0] + sm[3][0])), 1)) break;
        if (give_up(sp, tm)) break;
        CNT(1);
        FENCE();
#pragma unroll
        for (int r = 0; r < 16; ++r) kp[r] = ldg_sc1(rs, kvv + (r >> 2) * 256, ks_ + (r & 3) * 16384);
      }
      if (lane < 32) {
        const unsigned st0 = w < 3 ? 400 : 800, st1 = w < 2 ? 400 : 800;
#pragma unroll
        for (int j = 0; j < 4; ++j) *(LAS v2u*)(L + (j < 2 ? vK0 + j * st0 : vK1 + (j - 2) * st1)) = v2u{pk2(sm[j][0], sm[j][1]), pk2(sm[j][2], sm[j][3])};
      }
      BAR();
#pragma unroll
      for (int s = 0; s < 5; ++s) { f[2 * s + 1] = lds128(L, vF1 + s * 400); f[2 * s] = lds128(L, vF0 + s * 400); }
      f32x16 n0, n1;
      SFOR(s, 5)
        const v4u bb = {ph[4 * s], ph[4 * s + 1], ph[4 * s + 2], ph[4 * s + 3]};
        mf32v<s == 0>(n1, f[2 * s + 1], bb); mf32v<s == 0>(n0, f[2 * s], bb);
      SEND;
      MFMA_SETTLE();
      SB();
      const float inv = __builtin_amdgcn_rcpf(n1[4] + eps);
#pragma unroll
      for (int i = 0; i < 2; ++i) { const f32x2 t = pkmul(f32x2{n1[2 * i], n1[2 * i + 1]}, f32x2{inv, inv}); o[16 + 2 * i] = t[0]; o[17 + 2 * i] = t[1]; }
#pragma unroll
      for (int i = 0; i < 8; ++i) { const f32x2 t = pkmul(f32x2{n0[2 * i], n0[2 * i + 1]}, f32x2{inv, inv}); o[2 * i] = t[0]; o[2 * i + 1] = t[1]; }
    }
    {
      RELANE();
      asm volatile("" : "+v"(vOv), "+v"(vOp), "+v"(vXv));
      asm volatile("" : "+v"(wo24));
      const unsigned os = sOp;
      stg_sc1(rs, vOv, os, v4u{pk2(o[0], o[1]), pk2(o[2], o[3]), pk2(o[4], o[5]), pk2(o[6], o[7])});
      stg_sc1(rs, vOv + 1024, os, v4u{pk2(o[8], o[9]), pk2(o[10], o[11]), pk2(o[12], o[13]), pk2(o[14], o[15])});
      const unsigned e0 = pk2(o[16], o[17]), e1 = pk2(o[18], o[19]);
      auto s0 = __builtin_amdgcn_permlane32_swap(e0, e0, false, false);
      auto s1 = __builtin_amdgcn_permlane32_swap(e1, e1, false, false);
      if (h == 0) stg_sc1(rs, vOv + 2048, os + 2048, v4u{e0, e1, s0[1], s1[1]});
      STAMP(6 + 12 * l);
      unsigned osb[4], ops = sOr + 4096, l16b = l16 + 1024, rwa[3], rra = L_RED + w * 15360 + l16;
      f32x2 rsum[10];
#pragma unroll
      for (int i = 0; i < 4; ++i) osb[i] = sOr + ((w + 1 + i) & 3) * 20480;
#pragma unroll
      for (int i = 0; i < 3; ++i) rwa[i] = L_RED + l16 + (((w + 1 + i) & 3) * 3 + i) * 5120;
#pragma unroll
      for (int i = 0; i < 10; ++i) rsum[i] = f32x2{lo16(xr[i]), hi16(xr[i])};
      asm volatile("" : "+v"(rsum[0]), "+v"(rsum[1]), "+v"(rsum[2]), "+v"(rsum[3]), "+v"(rsum[4]), "+v"(rsum[5]), "+v"(rsum[6]), "+v"(rsum[7]), "+v"(rsum[8]), "+v"(rsum[9]));
      pin_s(osb[0]); pin_s(osb[1]); pin_s(osb[2]); pin_s(osb[3]); pin_s(ops);
      const unsigned cXp = sXp, cSp = sSp;
      sX += XB; sSt += STB; sWo += m ? -8 * WO_SZ : 8 * WO_SZ; sOp += XB; sOr += XB; sXp += XB; sSp += STB;
      pin_s(sX); pin_s(sSt); pin_s(sWo); pin_s(sOp); pin_s(sOr); pin_s(sXp); pin_s(sSp);
      asm volatile("" : "+v"(l16b), "+v"(rwa[0]), "+v"(rwa[1]), "+v"(rwa[2]), "+v"(rra));
      if (l < 3) {
        const v4u P = {SENT, SENT, SENT, SENT};
        stg_sc1(rs, vOv, sOp, P); stg_sc1(rs, vOv + 1024, sOp, P); stg_sc1(rs, (vOv & ~2048u) + 2048, sOp + 2048, P);
        if (l < 2) {
          stg_sc1(rs, vXv, sXp, P); stg_sc1(rs, vXv + 512, sXp, P); stg_sc1(rs, (vXv & 0x1f0) + (h << 9), sXp + 4096, P);
          __builtin_amdgcn_raw_buffer_store_b64(v2u{SENT, SENT}, rs, (lane & 15) * 8, sSp, SC1);
        }
        SFOR(i, ODM) DM(NDM2 + ADM + i); SEND;
        if (l == 0) {
          stg_sc1(rs, l16, WS_KV + (256 + cg * 16 + rg) * 4096 + w * 1024, v4u{SENT, SENT, SENT, SENT});
          stg_sc1(rs, l16, WS_HALO + (256 + rg * 16 + cg) * 4096 + w * 1024, v4u{SENT, SENT, SENT, SENT});
          load_cs(L, rs, 1, cg, tid);
        }
        if (l < 2) vmc<ODM + 7>(); else vmc<ODM + 3>();
      } else vmc<0>();
      if constexpr (DLY_O > 0) __builtin_amdgcn_s_sleep(DLY_O);
      BARE();
      STAMP(7 + 12 * l);
#define LDO(i, s) do { if constexpr ((s) == 0) lda<20 * (i), 0, true>(rs, l16, osb[i]); else lda<20 * (i) + 4 * (s), ((s) - 1) * 1024, true>(rs, l16b, osb[i]); } while (0)
      unsigned opr;
      const unsigned opv = vOp;
      probe_issue(opr, rs, opv, ops);
      SFOR(s, 5) LDO(0, s); if constexpr (s == 1) BARE(); SEND;
      BARE();
      SFOR(s, 5) LDO(1, s); SEND;
      SB();
      f32x4 acc[2][5], part[15];
      probe_wait<10>(opr);
      if (__builtin_expect(any_sent(opr), 0)) {
        CNT(4);
        poll_probe<100>(rs, opv, ops, tm);
        SFOR(s, 5) LDO(0, s); SEND;
        SFOR(s, 5) LDO(1, s); SEND;
      }
      auto redo = [&](auto i_) __attribute__((always_inline)) {
        constexpr int i = decltype(i_)::v;
        unsigned sp = 0;
        do {
          CNT(1);
          SFOR(s, 5) LDO(i, s); SEND;
          vmc<0>();
          SFOR(j, 25) if constexpr (j < 24) mf16aa<(j < 5), 160 + 4 * j, 20 * i + 4 * (j / 5)>(acc[i & 1][j % 5]); else mf16va<20 * i + 16>(acc[i & 1][4], wo24); SEND;
          asm volatile("s_nop 7");
          SB();
        } while (any_nan(acc[i & 1][4][0]) && !give_up(sp, tm));
      };
      constexpr int NDO = NDM2 + ADM + ODM;
      static_assert(NDO + GDM <= 22 && NDM2 + ADM + ODM <= 17 && GDM <= 10 && ADM <= 10, "");
      SFOR(i, 4)
        SFOR(j, 25)
          constexpr int s = j / 5, ft = j % 5, WS0 = 4, wstep = 4;
          SB();
          constexpr int gi = 5 * i + s;
          if constexpr (ft == 0) vmc<(gi < 10 ? 9 : 19 - gi) + (gi > 20 - GDM ? gi - (20 - GDM) : 0)>();
          if constexpr (j < 24) mf16aa<s == 0, 160 + 4 * j, 20 * i + 4 * s>(acc[i & 1][ft]); else mf16va<20 * i + 16>(acc[i & 1][ft], wo24);
          if constexpr (i == 0 && j == 0) STAMP(8 + 12 * l);
          if constexpr (ft == 2) {
            if constexpr (i < 2) LDO(i + 2, s);
            else if constexpr (gi >= 20 - GDM) dmaw<NDO + gi - (20 - GDM)>(rw, l16, wb1k);
          }
          if constexpr (i > 0 && j == WS0 - 1) { asm volatile("s_nop 1"); SB(); if (__builtin_expect(any_nan(acc[(i - 1) & 1][4][0]), 0)) redo(IC<i - 1>{}); }
          if constexpr (i > 0 && j >= WS0 && j <= WS0 + 4 * wstep && (j - WS0) % wstep == 0)
            DSW(rwa[i > 0 ? i - 1 : 0], ((j - WS0) / wstep) * 1024, acc[(i - 1) & 1][(j - WS0) / wstep]);
          if constexpr (i == 3 && j == 23) { SB(); BAR(); }
          if constexpr (i == 3 && j >= 23) {
#pragma unroll
            for (int q = (j - 23) * 8; q < (j - 23) * 8 + 8 && q < 15; ++q) part[q] = __builtin_bit_cast(f32x4, lds128(L, rra + (q % 3) * 5120 + (q / 3) * 1024));
          }
        SEND;
      SEND;
      SB();
      STAMP(9 + 12 * l);
#pragma unroll
      for (int ft = 0; ft < 5; ++ft)
#pragma unroll
        for (int q = 0; q < 3; ++q) { rsum[2 * ft] = pkadd(rsum[2 * ft], f32x2{part[ft * 3 + q][0], part[ft * 3 + q][1]}); rsum[2 * ft + 1] = pkadd(rsum[2 * ft + 1], f32x2{part[ft * 3 + q][2], part[ft * 3 + q][3]}); }
      asm volatile("" : "+v"(rsum[8]), "+v"(rsum[9]));
      SB();
      if (__builtin_expect(any_nan(acc[1][4][0]), 0)) redo(IC<3>{});
      auto fin = [&](int ft) __attribute__((always_inline)) {
        const f32x2 lo = pkadd(rsum[2 * ft], f32x2{acc[1][ft][0], acc[1][ft][1]}), hi = pkadd(rsum[2 * ft + 1], f32x2{acc[1][ft][2], acc[1][ft][3]});
        xr[2 * ft] = pk2(lo[0], lo[1]); xr[2 * ft + 1] = pk2(hi[0], hi[1]);
      };
      fin(0); fin(1);
      if (l < 3) {
        stg_sc1(rs, vXv, cXp, v4u{xr[0], xr[1], xr[2], xr[3]});
        SB();
        fin(2); fin(3);
        stg_sc1(rs, vXv + 512, cXp, v4u{xr[4], xr[5], xr[6], xr[7]});
        SB();
        fin(4);
        { auto s0 = __builtin_amdgcn_permlane16_swap(xr[8], xr[8], false, false);
          auto s1 = __builtin_amdgcn_permlane16_swap(xr[9], xr[9], false, false);
          stg_sc1(rs, (lane & 16) ? 0x80000000u : (vXv & 0x1f0) + (h << 9), cXp + 4096, v4u{xr[8], xr[9], s0[1], s1[1]}); }
        SB();
        { float sa = 0.f, sq = 0.f;
#pragma unroll
          for (int i = 0; i < 10; ++i) { sa = dot2(xr[i], 0x3F803F80u, sa); sq = dot2(xr[i], xr[i], sq); }
          { auto a = __builtin_amdgcn_permlane16_swap(f2u(sa), f2u(sa), false, false); sa = u2f(a[0]) + u2f(a[1]);
            auto c = __builtin_amdgcn_permlane16_swap(f2u(sq), f2u(sq), false, false); sq = u2f(c[0]) + u2f(c[1]); }
          { auto a = __builtin_amdgcn_permlane32_swap(f2u(sa), f2u(sa), false, false); sa = u2f(a[0]) + u2f(a[1]);
            auto c = __builtin_amdgcn_permlane32_swap(f2u(sq), f2u(sq), false, false); sq = u2f(c[0]) + u2f(c[1]); }
          __builtin_amdgcn_raw_buffer_store_b64(v2u{f2u(sa), f2u(sq)}, rs, lane < 16 ? lane * 8 : 0x80000000u, cSp, SC1); }
        STAMP(10 + 12 * l);
        BARE();
        SFOR(i, 22 - NDO - GDM) DM(NDO + GDM + i); SEND;
        SFOR(i, 15) DM(22 + i); SEND;
        if (w < 2) DM(37);
        vmc<15>();
        if constexpr (DLY_X > 0) __builtin_amdgcn_s_sleep(DLY_X);
        BARE();
      } else {
        fin(2); fin(3); fin(4);
        GAS v2u* dst = (GAS v2u*)(p.out + (size_t)(rg * 128 + half * 64 + w * 16 + (lane & 15)) * 640 + c8 * 80 + (lane >> 4) * 20);
#pragma unroll
        for (int i = 0; i < 5; ++i) dst[i] = v2u{xr[2 * i], xr[2 * i + 1]};
        if (b == 0 && tid == 0) __builtin_amdgcn_raw_buffer_store_b32(tm.epoch, rs, 0, WS_CTRL + 128, SC1);
      }
      STAMP(11 + 12 * l);
    }
  }
}

extern "C" void kernel_launch(void* const* d_in, const int* in_sizes, int n_in, void* d_out, int out_size, void* d_ws, size_t ws_size, hipStream_t stream) {
  Params p;
  p.x = (const u16*)d_in[0]; p.wqA = (const u16*)d_in[1]; p.woA = (const u16*)d_in[2];
  p.wqB = (const u16*)d_in[4]; p.wphi = (const u16*)d_in[5]; p.woB = (const u16*)d_in[6];
  p.out = (u16*)d_out; p.ws = (unsigned char*)d_ws;
  (void)hipFuncSetAttribute((const void*)k_main, hipFuncAttributeMaxDynamicSharedMemorySize, LDS_ALLOC);
  k_main<<<256, 256, LDS_ALLOC, stream>>>(p);
}
```

```cpp
#include <hip/hip_runtime.h>
#include <stdint.h>

typedef unsigned short u16;
typedef unsigned v4u __attribute__((ext_vector_type(4)));
typedef unsigned v2u __attribute__((ext_vector_type(2)));
typedef float f32x2 __attribute__((ext_vector_type(2)));
typedef float f32x4 __attribute__((ext_vector_type(4)));
typedef float f32x16 __attribute__((ext_vector_type(16)));
typedef __bf16 bf16x8 __attribute__((ext_vector_type(8)));
typedef __bf16 bf16x2 __attribute__((ext_vector_type(2)));
typedef short s16x4 __attribute__((ext_vector_type(4)));
#define LAS __attribute__((address_space(3)))
#define GAS __attribute__((address_space(1)))
typedef __amdgpu_buffer_rsrc_t rsrc_t;
#define DEV __device__ __forceinline__

#define DSC1 " sc1"
constexpr unsigned WQ_SZ = 153600, WO_SZ = 102400, XB = 2621440, STB = 131072;
constexpr unsigned WS_CTRL = 0;
constexpr unsigned WS_FLAG = 4096;
constexpr unsigned WS_WQ = WS_FLAG + 2 * 256 * 128;
constexpr unsigned WS_WO = WS_WQ + 32 * WQ_SZ;
constexpr unsigned WS_CSP = WS_WO + 16 * WO_SZ;
constexpr unsigned WS_ST0 = WS_CSP + 2 * 16 * 16 * 512;
constexpr unsigned WS_ST1 = WS_ST0 + STB;
constexpr unsigned WS_X = WS_ST0 + 4 * STB;
constexpr unsigned WS_O = WS_X + 4 * XB;
constexpr unsigned WS_KV = WS_O + 4 * XB;
constexpr unsigned WS_HALO = WS_KV + 2 * 256 * 4096;
constexpr unsigned WS_END = WS_HALO + 2 * 256 * 4096;
constexpr unsigned WS_STAMP = 40u << 20;
constexpr unsigned WS_TOTAL = WS_STAMP + 256 * 4 * 64 * 8;
constexpr unsigned KSTEP = 2816, L_P0 = 112640, L_KVA = 153600, L_CS = L_KVA + 4096;
constexpr unsigned L_SCR = 73216;
constexpr unsigned L_K = L_SCR, L_V = L_SCR + 2 * 144 * 48;
constexpr unsigned L_RED = 90112;
constexpr unsigned LDS_BYTES = L_CS + 4096;
constexpr unsigned SENT = 0xFFFFFFFFu;
constexpr unsigned CODE_BYTES = 24576;
constexpr int SC1 = 16;
constexpr float LOG2E = 1.4426950408889634f;

struct Params { const u16 *x, *wqA, *woA, *wqB, *wphi, *woB; u16* out; unsigned char* ws; };

DEV unsigned pk2(float a, float b) { bf16x2 v = {(__bf16)a, (__bf16)b}; return __builtin_bit_cast(unsigned, v); }
DEV float lo16(unsigned u) { return __builtin_bit_cast(float, u << 16); }
DEV float hi16(unsigned u) { return __builtin_bit_cast(float, u & 0xffff0000u); }
DEV unsigned f2u(float f) { return __builtin_bit_cast(unsigned, f); }
DEV float u2f(unsigned u) { return __builtin_bit_cast(float, u); }
DEV unsigned umax(unsigned a, unsigned b) { return __builtin_elementwise_max(a, b); }
DEV unsigned umin(unsigned a, unsigned b) { return __builtin_elementwise_min(a, b); }
DEV float max3f(float a, float b, float c) { float d; asm("v_max3_f32 %0, %1, %2, %3" : "=v"(d) : "v"(a), "v"(b), "v"(c)); return d; }
DEV float min3f(float a, float b, float c) { float d; asm("v_min3_f32 %0, %1, %2, %3" : "=v"(d) : "v"(a), "v"(b), "v"(c)); return d; }
DEV float dot2(unsigned a, unsigned b, float c) { return __builtin_amdgcn_fdot2_f32_bf16(__builtin_bit_cast(bf16x2, a), __builtin_bit_cast(bf16x2, b), c, false); }
DEV f32x2 pkfma(f32x2 a, f32x2 b, f32x2 c) { return __builtin_elementwise_fma(a, b, c); }
DEV f32x2 pkmul(f32x2 a, f32x2 b) { return a * b; }
DEV f32x2 pkadd(f32x2 a, f32x2 b) { return a + b; }
DEV f32x16 mfma32(v4u a, v4u b, f32x16 c) { return __builtin_amdgcn_mfma_f32_32x32x16_bf16(__builtin_bit_cast(bf16x8, a), __builtin_bit_cast(bf16x8, b), c, 0, 0, 0); }
DEV f32x4 mfma16(v4u a, v4u b, f32x4 c) { return __builtin_amdgcn_mfma_f32_16x16x32_bf16(__builtin_bit_cast(bf16x8, a), __builtin_bit_cast(bf16x8, b), c, 0, 0, 0); }
DEV v4u lds128(LAS unsigned char* L, unsigned a) { return *(LAS v4u*)(L + a); }
DEV v2u lds64(LAS unsigned char* L, unsigned a) { return *(LAS v2u*)(L + a); }
DEV void sts128(LAS unsigned char* L, unsigned a, v4u v) { *(LAS v4u*)(L + a) = v; }
DEV v2u ldstr(LAS unsigned char* L, unsigned a) { return __builtin_bit_cast(v2u, __builtin_amdgcn_ds_read_tr16_b64_v4i16((LAS s16x4*)(L + a))); }
DEV v4u ldg(rsrc_t rs, unsigned voff, unsigned soff) { return __builtin_amdgcn_raw_buffer_load_b128(rs, voff, soff, 0); }
DEV v4u ldg_sc1(rsrc_t rs, unsigned voff, unsigned soff) { return __builtin_amdgcn_raw_buffer_load_b128(rs, voff, soff, SC1); }
DEV v2u ldg64_sc1(rsrc_t rs, unsigned voff, unsigned soff) { return __builtin_amdgcn_raw_buffer_load_b64(rs, voff, soff, SC1); }
DEV void stg_sc1(rsrc_t rs, unsigned voff, unsigned soff, v4u v) { __builtin_amdgcn_raw_buffer_store_b128(v, rs, voff, soff, SC1); }
DEV unsigned mx4(unsigned m, v4u d) { return umax(umax(umax(m, d.x), d.y), umax(d.z, d.w)); }
DEV unsigned max3u(unsigned a, unsigned b, unsigned c) { return umax(umax(a, b), c); }
DEV bool any_sent(unsigned m) { return __builtin_amdgcn_ballot_w64(m == SENT) != 0; }
#define BAR() asm volatile("s_waitcnt lgkmcnt(0)\n\ts_barrier" ::: "memory")
#define DRAIN() asm volatile("s_waitcnt vmcnt(0)" ::: "memory")
#define FENCE() asm volatile("" ::: "memory")
#define SB() __builtin_amdgcn_sched_barrier(0)
DEV void dma4(rsrc_t rs, unsigned voff, unsigned soff, unsigned ldsa) {
  asm volatile("s_mov_b32 m0, %0\n\ts_nop 0\n\tbuffer_load_dwordx4 %1, %2, %3 offen sc1 lds\n\tbuffer_load_dwordx4 %1, %2, %3 offen offset:1024 sc1 lds\n\t"
               "buffer_load_dwordx4 %1, %2, %3 offen offset:2048 sc1 lds\n\tbuffer_load_dwordx4 %1, %2, %3 offen offset:3072 sc1 lds"
               :: "s"(__builtin_amdgcn_readfirstlane(ldsa)), "v"(voff), "s"(rs), "s"(__builtin_amdgcn_readfirstlane(soff)) : "memory");
}
DEV void dma1(rsrc_t rs, unsigned voff, unsigned soff, unsigned ldsa) {
  asm volatile("s_mov_b32 m0, %0\n\ts_nop 0\n\tbuffer_load_dwordx4 %1, %2, %3 offen sc1 lds"
               :: "s"(__builtin_amdgcn_readfirstlane(ldsa)), "v"(voff), "s"(rs), "s"(__builtin_amdgcn_readfirstlane(soff)) : "memory");
}
DEV void dma_range(rsrc_t rs, unsigned l16, unsigned wsrc, int w, int k0, int k1) {
  int k = k0 + 4 * w;
  for (; k + 4 <= k1; k += 16) dma4(rs, l16, wsrc + k * 1024, k * 1024);
  for (; k < k1; ++k) dma1(rs, l16, wsrc + k * 1024, k * 1024);
}
struct Tmo { unsigned char* ws; unsigned epoch; };
DEV bool give_up(unsigned& sp, const Tmo& tm) {
  ++sp;
  if (sp > 32) __builtin_amdgcn_s_sleep(8);
  if ((sp & 63u) == 0u) {
    GAS unsigned* t = (GAS unsigned*)(tm.ws + WS_CTRL);
    if (sp >= (1u << 16)) { __hip_atomic_store(t, tm.epoch, __ATOMIC_RELAXED, __HIP_MEMORY_SCOPE_AGENT); return true; }
    if (__builtin_amdgcn_readfirstlane(__hip_atomic_load(t, __ATOMIC_RELAXED, __HIP_MEMORY_SCOPE_AGENT)) == tm.epoch) return true;
  }
  return false;
}
#define MAPV 1
#define XW 10
DEV int map_rg(int b) { return ((b & 7) >> 1) * 4 + (b >> 6); }
DEV int map_cg(int b) { return (b & 1) * 8 + ((b >> 3) & 7); }
DEV int o_half(int cg) { return (cg >> 2) & 1; }
DEV int o_c8(int cg) { return (cg >> 3) * 4 + (cg & 3); }
DEV int xfeat(int c, int j) { int c8 = c / 10, l = c % 10; return 80 * c8 + (l < 8 ? 20 * (l >> 1) + 8 * (l & 1) + j : 40 * (l - 8) + 20 * (j >> 2) + 16 + (j & 3)); }
DEV int ocol(int cgp, int s, int j) { return 40 * cgp + (s < 4 ? 20 * (s >> 1) + 8 * (s & 1) + j : 20 * (j >> 2) + 16 + (j & 3)); }
DEV void pin_s(unsigned& v) { asm volatile("" : "+s"(v)); }
DEV int fresh_lane() { int l; asm volatile("v_mbcnt_lo_u32_b32 %0, -1, 0\n\tv_mbcnt_hi_u32_b32 %0, -1, %0" : "=v"(l)); return l; }
#define RELANE() const int lane = fresh_lane(), h = lane >> 5, tok = lane & 31, tid = w * 64 + lane; const unsigned l16 = lane * 16; (void)h; (void)tok; (void)tid; (void)l16
#define STAMP(i) do {} while (0)
#define LDS_ALLOC LDS_BYTES
#define CNT(i) do {} while (0)

DEV void publish_x(rsrc_t rs, unsigned xbuf, unsigned stbuf, int rg, int cg, int w, int lane, const unsigned (&xr)[10]) {
  const int g = lane >> 4, tok = lane & 15, half = o_half(cg), c8 = o_c8(cg);
  const unsigned sb = xbuf + ((rg * 4 + half * 2 + (w >> 1)) * 40 + 5 * c8) * 1024;
  const unsigned vb = ((w & 1) * 16 + tok) * 16;
  stg_sc1(rs, vb + g * 1024, sb, v4u{xr[0], xr[1], xr[2], xr[3]});
  stg_sc1(rs, vb + g * 1024 + 512, sb, v4u{xr[4], xr[5], xr[6], xr[7]});
  auto s0 = __builtin_amdgcn_permlane16_swap(xr[8], xr[8], false, false);
  auto s1 = __builtin_amdgcn_permlane16_swap(xr[9], xr[9], false, false);
  if (!(g & 1)) stg_sc1(rs, vb + 4096 + (g >> 1) * 512, sb, v4u{xr[8], xr[9], s0[1], s1[1]});
  SB();
  float sa = 0.f, sq = 0.f;
#pragma unroll
  for (int i = 0; i < 10; ++i) { sa = dot2(xr[i], 0x3F803F80u, sa); sq = dot2(xr[i], xr[i], sq); }
  { auto a = __builtin_amdgcn_permlane16_swap(f2u(sa), f2u(sa), false, false); sa = u2f(a[0]) + u2f(a[1]);
    auto b = __builtin_amdgcn_permlane16_swap(f2u(sq), f2u(sq), false, false); sq = u2f(b[0]) + u2f(b[1]); }
  { auto a = __builtin_amdgcn_permlane32_swap(f2u(sa), f2u(sa), false, false); sa = u2f(a[0]) + u2f(a[1]);
    auto b = __builtin_amdgcn_permlane32_swap(f2u(sq), f2u(sq), false, false); sq = u2f(b[0]) + u2f(b[1]); }
  if (g == 0) __builtin_amdgcn_raw_buffer_store_b64(v2u{f2u(sa), f2u(sq)}, rs, tok * 8, stbuf + ((rg * 8 + c8) * 128 + half * 64 + w * 16) * 8, SC1);
}
DEV void load_x_in(const u16* x, int rg, int cg, int w, int lane, unsigned (&xr)[10]) {
  const int g = lane >> 4, tok = lane & 15;
  const GAS v2u* src = (const GAS v2u*)(x + (size_t)(rg * 128 + o_half(cg) * 64 + w * 16 + tok) * 640 + o_c8(cg) * 80 + g * 20);
#pragma unroll
  for (int i = 0; i < 5; ++i) { v2u t = src[i]; xr[2 * i] = t.x; xr[2 * i + 1] = t.y; }
}

DEV void poison_x(rsrc_t rs, unsigned xbuf, unsigned stbuf, int rg, int cg, int w, int lane) {
  const int g = lane >> 4, tok = lane & 15, half = o_half(cg), c8 = o_c8(cg);
  const unsigned sb = xbuf + ((rg * 4 + half * 2 + (w >> 1)) * 40 + 5 * c8) * 1024, vb = ((w & 1) * 16 + tok) * 16;
  const v4u P = {SENT, SENT, SENT, SENT};
  stg_sc1(rs, vb + g * 1024, sb, P); stg_sc1(rs, vb + g * 1024 + 512, sb, P); stg_sc1(rs, vb + 4096 + (g >> 1) * 512, sb, P);
  __builtin_amdgcn_raw_buffer_store_b64(v2u{SENT, SENT}, rs, tok * 8, stbuf + ((rg * 8 + c8) * 128 + half * 64 + w * 16) * 8, SC1);
}
DEV void poison_o(rsrc_t rs, unsigned obuf, int rg, int cg, int w, int lane) {
  const int h = lane >> 5, tok = lane & 31;
  const unsigned os = obuf + ((rg * 8 + 2 * w) * 4 + (cg >> 2)) * 5120 + (cg & 3) * 256, ov = (tok >> 4) * 20480 + (tok & 15) * 16;
  const v4u P = {SENT, SENT, SENT, SENT};
  stg_sc1(rs, ov + h * 2048, os, P); stg_sc1(rs, ov + h * 2048 + 1024, os, P); stg_sc1(rs, ov + 4096, os, P);
}
DEV void wait_flags(rsrc_t rs, unsigned voff, unsigned soff, const Tmo& tm) {
  unsigned sp = 0, v;
  do { FENCE(); v = __builtin_amdgcn_raw_buffer_load_b32(rs, voff, soff, SC1); } while (__builtin_amdgcn_ballot_w64(v != tm.epoch) != 0 && !give_up(sp, tm));
}
DEV void build_wq_read(LAS unsigned char* L, unsigned stage, int w, int lane, v4u (&fr)[3]) {
  const int gam = lane >> 4, q = (lane >> 2) & 3, pp = lane & 3, a = 2 * (gam & 1) + (pp >> 1), h = pp & 1;
  const int eoff = (w < 3) ? 40 * w + 20 * h + 4 * a : (a == 0 ? 40 : a == 1 ? 80 : 0) + 20 * h + 16;
#pragma unroll
  for (int it = 0; it < 3; ++it) {
    const int kap = 2 * it + (gam >> 1), kc = kap < 4 ? kap : 4;
    const unsigned ad = stage + (kc * 8 + q) * 256 + eoff * 2;
    const v2u t0 = ldstr(L, ad), t1 = ldstr(L, ad + 1024);
    fr[it] = v4u{t0.x, t0.y, t1.x, t1.y};
  }
}
DEV float sum8(v4u v, float c) { c = dot2(v.x, 0x3F803F80u, c); c = dot2(v.y, 0x3F803F80u, c); c = dot2(v.z, 0x3F803F80u, c); return dot2(v.w, 0x3F803F80u, c); }
DEV void build_wq_store(rsrc_t rs, int m, int rg, int cg, int w, int lane, int r0, int r1, const v4u (&fr)[3]) {
  const int row = lane & 31, hi = lane >> 5;
  const unsigned tb = w == 0 ? L_P0 : (w - 1) * 1024, ts = w == 0 ? 1024 : KSTEP, th = w == 3 ? 384 : 512;
  const bool in = row >= r0 && row < r1;
#pragma unroll
  for (int it = 0; it < 3; ++it) {
    const int c = 5 * rg + 2 * it + hi;
    stg_sc1(rs, (in && (it < 2 || !hi)) ? (c >> 1) * ts + (c & 1) * th + row * 16 : 0x80000000u, WS_WQ + (m * 16 + cg) * WQ_SZ + tb, fr[it]);
  }
  float cs = sum8(fr[1], sum8(fr[0], 0.f));
  cs = hi ? cs : sum8(fr[2], cs);
  auto t = __builtin_amdgcn_permlane32_swap(f2u(cs), f2u(cs), false, false);
  __builtin_amdgcn_raw_buffer_store_b32(f2u(u2f(t[0]) + u2f(t[1])), rs, (in && !hi) ? row * 4 : 0x80000000u, WS_CSP + ((m * 16 + cg) * 16 + rg) * 512 + w * 128, SC1);
}
DEV void load_cs(LAS unsigned char* L, rsrc_t rs, int m, int cg, int tid) {
  if (tid < 128) {
    float cs = 0.f;
#pragma unroll
    for (int r = 0; r < 16; ++r) cs += u2f(__builtin_amdgcn_raw_buffer_load_b32(rs, tid * 4 + (r & 7) * 512, WS_CSP + (m * 16 + cg) * 8192 + (r >> 3) * 4096, SC1));
    const unsigned ch = pk2(cs, 0.f) & 0xffffu, cl = pk2(cs - lo16(ch), 0.f) & 0xffffu;
    sts128(L, L_CS + m * 2048 + tid * 16, v4u{ch | (cl << 16), ch, 0u, 0u});
  }
}

DEV void probe_issue(unsigned& d, rsrc_t rs, unsigned voff, unsigned soff) {
  asm volatile("buffer_load_dword %0, %1, %2, %3 offen sc1" : "=v"(d) : "v"(voff), "s"(rs), "s"(soff) : "memory");
}
template <int N> DEV void probe_wait(unsigned& d) { asm volatile("s_waitcnt vmcnt(%1)" : "+v"(d) : "i"(N) : "memory"); }
#define PSL 2
template <int R> DEV unsigned probe_turn(rsrc_t rs, unsigned voff, unsigned soff) {
  unsigned v;
  asm volatile("buffer_load_dword a%c4, %1, %2, %3 offen sc1\n\ts_waitcnt vmcnt(1)\n\tv_accvgpr_read_b32 %0, a%c5" : "=v"(v) : "v"(voff), "s"(rs), "s"(soff), "i"(R), "i"(R ^ 1) : "memory");
  return v;
}
template <int R> DEV void poll_probe(rsrc_t rs, unsigned voff, unsigned soff, const Tmo& tm) {
  unsigned sp = 0;
  asm volatile("buffer_load_dword a%c3, %0, %1, %2 offen sc1" :: "v"(voff), "s"(rs), "s"(soff), "i"(R) : "memory");
  for (;;) {
    __builtin_amdgcn_s_sleep(PSL);
    if (!any_sent(probe_turn<R + 1>(rs, voff, soff))) break;
    __builtin_amdgcn_s_sleep(PSL);
    if (!any_sent(probe_turn<R>(rs, voff, soff))) break;
    if (give_up(sp, tm)) break;
  }
}
template <int I> struct IC { static constexpr int v = I; };
template <int I, int N, class F> DEV void sfor(F&& f) { if constexpr (I < N) { f(IC<I>{}); sfor<I + 1, N>(f); } }
#define SFOR(var, n) sfor<0, n>([&](auto var##_) __attribute__((always_inline)) { constexpr int var = decltype(var##_)::v;
#define SEND })
template <bool NORM, class F> DEV float phi_map(const float (&a)[20], float s, float post, unsigned (&ph)[20], F slot) {
  float mx = max3f(a[0], a[1], a[2]), mn = min3f(a[0], a[1], a[2]), mx2 = max3f(a[3], a[4], a[5]), mn2 = min3f(a[3], a[4], a[5]);
#pragma unroll
  for (int i = 6; i < 18; i += 4) { mx = max3f(mx, a[i], a[i + 1]); mn = min3f(mn, a[i], a[i + 1]); mx2 = max3f(mx2, a[i + 2], a[i + 3]); mn2 = min3f(mn2, a[i + 2], a[i + 3]); }
  mx = max3f(mx, a[18], a[19]); mn = min3f(mn, a[18], a[19]); mx = max3f(mx, mx2, mx2); mn = min3f(mn, mn2, mn2);
  const float bp = -s * mx, bn = s * mn, ns = -s;
  float sp = 0.f;
  SFOR(sg, 2)
    const f32x2 sc = sg ? f32x2{ns, ns} : f32x2{s, s}, bi = sg ? f32x2{bn, bn} : f32x2{bp, bp};
    f32x2 e[10];
    slot(IC<5 * sg>{});
#pragma unroll
    for (int i = 0; i < 10; ++i) e[i] = pkfma(f32x2{a[2 * i], a[2 * i + 1]}, sc, bi);
    SB();
    SFOR(i, 10)
      if constexpr (i == 3 || i == 7) slot(IC<5 * sg + 1 + (i >> 2)>{});
      e[i] = f32x2{__builtin_amdgcn_exp2f(e[i][0]), __builtin_amdgcn_exp2f(e[i][1])};
    SEND;
    f32x2 s0 = pkadd(e[0], e[1]), s1 = pkadd(e[2], e[3]), s2 = pkadd(e[4], e[5]);
    s0 = pkadd(s0, e[6]); s1 = pkadd(s1, e[7]); s2 = pkadd(s2, e[8]); s0 = pkadd(s0, e[9]);
    slot(IC<5 * sg + 3>{});
    if constexpr (!NORM && sg == 0) {
#pragma unroll
      for (int i = 0; i < 10; ++i) ph[i] = pk2(e[i][0], e[i][1]);
    }
    s0 = pkadd(pkadd(s0, s1), s2);
    const float sm = s0[0] + s0[1];
    if constexpr (NORM || sg == 1) {
      const float r = __builtin_amdgcn_rcpf(sm) * (NORM ? post : sp);
#pragma unroll
      for (int i = 0; i < 10; ++i) { f32x2 t = pkmul(e[i], f32x2{r, r}); ph[10 * sg + i] = pk2(t[0], t[1]); }
    }
    sp = sg ? sp : sm;
    slot(IC<5 * sg + 4>{});
  SEND;
  return sp;
}

#define BARE() asm volatile("s_barrier" ::: "memory")
template <int OFF> DEV void dsr(v4u& d, unsigned addr) { asm volatile("ds_read_b128 %0, %1 offset:%2" : "=v"(d) : "v"(addr), "i"(OFF)); }
template <int R, int OFF, bool COH> DEV void lda(rsrc_t rs, unsigned voff, unsigned soff) {
  if constexpr (COH) asm volatile("buffer_load_dwordx4 a[%c0:%c1], %2, %3, %4 offen offset:%5 sc1" :: "i"(R), "i"(R + 3), "v"(voff), "s"(rs), "s"(soff), "i"(OFF) : "memory");
  else asm volatile("buffer_load_dwordx4 a[%c0:%c1], %2, %3, %4 offen offset:%5" :: "i"(R), "i"(R + 3), "v"(voff), "s"(rs), "s"(soff), "i"(OFF) : "memory");
}
DEV void sbump(unsigned& so) { asm volatile("s_add_u32 %0, %0, 0x1000" : "+s"(so) :: "scc"); }
template <bool Z, int R> DEV void mf32a(f32x16& c, const v4u& a) {
  if constexpr (Z) asm volatile("v_mfma_f32_32x32x16_bf16 %0, %1, a[%c2:%c3], 0" : "=&v"(c) : "v"(a), "i"(R), "i"(R + 3));
  else asm volatile("v_mfma_f32_32x32x16_bf16 %0, %1, a[%c2:%c3], %0" : "+v"(c) : "v"(a), "i"(R), "i"(R + 3));
}
template <bool Z> DEV void mf32v(f32x16& c, const v4u& a, const v4u& b) {
  if constexpr (Z) asm volatile("v_mfma_f32_32x32x16_bf16 %0, %1, %2, 0" : "=&v"(c) : "v"(a), "v"(b));
  else asm volatile("v_mfma_f32_32x32x16_bf16 %0, %1, %2, %0" : "+v"(c) : "v"(a), "v"(b));
}
template <bool Z, int R> DEV void mf16a(f32x4& c, const v4u& b) {
  if constexpr (Z) asm volatile("v_mfma_f32_16x16x32_bf16 %0, a[%c2:%c3], %1, 0" : "=&v"(c) : "v"(b), "i"(R), "i"(R + 3));
  else asm volatile("v_mfma_f32_16x16x32_bf16 %0, a[%c2:%c3], %1, %0" : "+v"(c) : "v"(b), "i"(R), "i"(R + 3));
}
template <bool Z> DEV void mf16v(f32x4& c, const v4u& a, const v4u& b) {
  if constexpr (Z) asm volatile("v_mfma_f32_16x16x32_bf16 %0, %1, %2, 0" : "=&v"(c) : "v"(a), "v"(b));
  else asm volatile("v_mfma_f32_16x16x32_bf16 %0, %1, %2, %0" : "+v"(c) : "v"(a), "v"(b));
}
template <int N> DEV void vmc() { asm volatile("s_waitcnt vmcnt(%0)" :: "i"(N) : "memory"); }
template <int I> DEV void dmaw(rsrc_t rw, unsigned l16, unsigned wb) {
  asm volatile("s_or_b32 m0, %0, %1\n\ts_nop 0\n\tbuffer_load_dwordx4 %2, %3, m0 offen" DSC1 " lds" :: "s"(wb), "i"(I * 4096), "v"(l16), "s"(rw) : "memory", "scc");
}
template <int N> DEV void lgkm() { asm volatile("s_waitcnt lgkmcnt(%0)" :: "i"(N)); }
#define MFMA_SETTLE() asm volatile("s_nop 7\n\ts_nop 3")
DEV bool any_nan(float x) { return __builtin_amdgcn_ballot_w64(x != x) != 0; }
#define PFD 3

template <int S, int NW, int VM, int VI> DEV void p2pair(f32x16& a0, v4u& w0, v4u& w1, unsigned bq, unsigned bcs, rsrc_t r, unsigned l16, unsigned so, const v4u& pv) {
#define P2_M0 "s_waitcnt lgkmcnt(%c[nw])\n\tv_mfma_f32_32x32x16_bf16 %[c], %[w0], a[%c[r0]:%c[r1]], "
#define P2_R0 "\n\tds_read_b128 %[w0], %[a0] offset:%c[o0]\n\t"
#define P2_M1 "v_mfma_f32_32x32x16_bf16 %[c], %[w1], a[%c[r2]:%c[r3]], %[c]"
#define P2_R1 "\n\tds_read_b128 %[w1], %[bq] offset:%c[o1]"
#define P2_IN [nw] "i"(NW), [r0] "i"(4 * S), [r1] "i"(4 * S + 3), [r2] "i"(4 * S + 4), [r3] "i"(4 * S + 7), [a0] "v"(S + 6 == 40 ? bcs : bq), [bq] "v"(bq), \
              [o0] "i"(S + 6 < 40 ? (S + 6) * 1024 : 0), [o1] "i"((S + 7) * 1024), [rs] "s"(r), [l16] "v"(l16), [so] "s"(so), [d0] "i"(VM == 1 ? 160 + 4 * VI : 0), [d1] "i"(VM == 1 ? 163 + 4 * VI : 3), \
              [vo] "i"((VI & 3) * 1024), [dm] "i"(VM == 2 ? VI * 4096 : VM == 3 ? VI : 0), [pv] "v"(pv), [p2] "v"(v2u{pv.x, pv.y})
#define P2_LD "buffer_load_dwordx4 a[%c[d0]:%c[d1]], %[l16], %[rs], %[so] offen offset:%c[vo] sc1\n\t"
#define P2_DMA "s_or_b32 m0, %[so], %[dm]\n\t"
#define P2_DMB "buffer_load_dwordx4 %[l16], %[rs], m0 offen" DSC1 " lds\n\t"
#define P2_ST "buffer_store_dwordx4 %[pv], %[l16], %[rs], %[so] offen offset:%c[dm] sc1\n\t"
#define P2_S2 "buffer_store_dwordx2 %[p2], %[l16], %[rs], %[so] offen sc1\n\t"
  if constexpr (S == 0) {
    static_assert(S != 0 || VM == 0, "");
    asm volatile(P2_M0 "0" P2_R0 P2_M1 P2_R1 : [c] "=&v"(a0), [w0] "+v"(w0), [w1] "+v"(w1) : P2_IN : "memory");
  } else if constexpr (S + 7 < 40) {
    if constexpr (VM == 0) asm volatile(P2_M0 "%[c]" P2_R0 P2_M1 P2_R1 : [c] "+v"(a0), [w0] "+v"(w0), [w1] "+v"(w1) : P2_IN : "memory");
    else if constexpr (VM == 1) asm volatile(P2_M0 "%[c]" P2_R0 P2_LD P2_M1 P2_R1 : [c] "+v"(a0), [w0] "+v"(w0), [w1] "+v"(w1) : P2_IN : "memory");
    else if constexpr (VM == 3) asm volatile(P2_M0 "%[c]" P2_R0 P2_ST P2_M1 P2_R1 : [c] "+v"(a0), [w0] "+v"(w0), [w1] "+v"(w1) : P2_IN : "memory");
    else if constexpr (VM == 4) asm volatile(P2_M0 "%[c]" P2_R0 P2_S2 P2_M1 P2_R1 : [c] "+v"(a0), [w0] "+v"(w0), [w1] "+v"(w1) : P2_IN : "memory");
    else asm volatile(P2_M0 "%[c]\n\t" P2_DMA "ds_read_b128 %[w0], %[a0] offset:%c[o0]\n\t" P2_DMB P2_M1 P2_R1 : [c] "+v"(a0), [w0] "+v"(w0), [w1] "+v"(w1) : P2_IN : "memory", "scc");
  } else if constexpr (S + 6 == 40) {
    static_assert(S + 6 != 40 || VM == 0, "");
    asm volatile(P2_M0 "%[c]" P2_R0 P2_M1 : [c] "+v"(a0), [w0] "+v"(w0), [w1] "+v"(w1) : P2_IN : "memory");
  } else {
    static_assert(VM == 0, "");
    asm volatile(P2_M0 "%[c]\n\t" P2_M1 : [c] "+v"(a0), [w0] "+v"(w0), [w1] "+v"(w1) : P2_IN : "memory");
  }
}
template <bool Z, int RA, int RB> DEV void mf16aa(f32x4& c) {
  if constexpr (Z) asm volatile("v_mfma_f32_16x16x32_bf16 %0, a[%c1:%c2], a[%c3:%c4], 0" : "=v"(c) : "i"(RA), "i"(RA + 3), "i"(RB), "i"(RB + 3));
  else asm volatile("v_mfma_f32_16x16x32_bf16 %0, a[%c1:%c2], a[%c3:%c4], %0" : "+v"(c) : "i"(RA), "i"(RA + 3), "i"(RB), "i"(RB + 3));
}
template <int RB> DEV void mf16va(f32x4& c, const v4u& a) { asm volatile("v_mfma_f32_16x16x32_bf16 %0, %1, a[%c2:%c3], %0" : "+v"(c) : "v"(a), "i"(RB), "i"(RB + 3)); }
#define DSW(addr, off, val) asm volatile("ds_write_b128 %0, %1 offset:%2" :: "v"(addr), "v"(val), "i"(off) : "memory")
#define ADM 12
#define ODM 0
#define GDM 10
#define DLY_O 28
#define DLY_X 22
#define HLP 17
#define KPS 8

__global__ void __launch_bounds__(256, 1) k_main(Params p) {
  extern __shared__ __attribute__((aligned(16))) unsigned char smem[];
  LAS unsigned char* L = (LAS unsigned char*)smem;
  {
    const unsigned long long pc = __builtin_amdgcn_s_getpc();
    const unsigned t16 = __builtin_amdgcn_workitem_id_x() * 16;
    asm volatile("global_load_dwordx4 a[0:3], %0, %1\n\tglobal_load_dwordx4 a[4:7], %0, %1 offset:4080" :: "v"(t16), "s"(pc) : "memory");
    asm volatile("global_load_dwordx4 a[8:11], %0, %1" :: "v"(t16 + 8192 + ((__builtin_amdgcn_workgroup_id_x() >> 3) & 3) * 4096), "s"(pc) : "memory");
    SB();
  }
  const int b = __builtin_amdgcn_workgroup_id_x(), w = __builtin_amdgcn_readfirstlane(__builtin_amdgcn_workitem_id_x() >> 6);
  const int rg = map_rg(b), cg = map_cg(b), half = o_half(cg), c8 = o_c8(cg);
  const rsrc_t rs = __builtin_amdgcn_make_buffer_rsrc(p.ws, 0, WS_TOTAL, 0x00020000);
  asm volatile("" ::: "a255");
  unsigned vKa, vVa, vHv, vImg, vA0, vA2, vB, vS0, vS1, vK0, vK1, vF0, vF1, vKv, vKp, vOv, vOp, vXv, vSt, vB3;
  auto addresses = [&]() __attribute__((always_inline)) {
    RELANE();
    const int g = lane >> 4, hh = w >> 1, dt = w & 1, q = (lane >> 2) & 3, p4 = lane & 3, r = lane & 15;
    const unsigned r_ = h * 144 + 2 + 32 * w + tok;
    vKa = L_K + r_ * 48; vVa = L_V + r_ * 80; vHv = (w == 3 && tok >= 16) ? (h * 16 + (tok - 16)) * 16 : 0x80000000u;
    const unsigned t_ = 32 * w + tok, key = (t_ & 7) ^ (((t_ >> 3) & 1) << 2);
    vImg = L_SCR + (h * 128 + t_) * 128 + (key << 4);
    const unsigned k0 = q ^ ((g & 1) << 2), rb0 = L_SCR + (hh * 128 + 8 * g + q) * 128, cB = dt ? 7 : 5 + (p4 >> 1);
    vA0 = rb0 + ((p4 ^ k0) << 4); vA2 = rb0 + ((4 ^ k0) << 4) + (p4 & 1) * 8; vB = rb0 + ((cB ^ k0) << 4) + (p4 & 1) * 8;
    vS0 = dt ? (r < 8 ? 2560 + ((hh * 5 + g) * 8 + r) * 16 : 0x80000000u) : ((hh * 5 + g) * 16 + r) * 16;
    vS1 = g ? 0x80000000u : dt ? (r < 8 ? 2560 + ((hh * 5 + 4) * 8 + r) * 16 : 0x80000000u) : ((hh * 5 + 4) * 16 + r) * 16;
    const unsigned ty0 = L_KVA + r * 16 + g * 8, ty1 = L_KVA + (r >> 3) * 400 + 256 + (r & 7) * 16 + g * 8;
    vK0 = w < 3 ? ty0 + w * 1600 : ty1 + 800;
    vK1 = w < 2 ? ty0 + w * 1600 + 800 : ty1 + (w == 2 ? 0 : 2400);
    const bool match = ((tok >> 2) & 1) == h;
    const unsigned row = 4 * (tok >> 3) + (tok & 3);
    vF0 = L_KVA + h * 2000 + (match ? row * 16 : 384); vF1 = L_KVA + h * 2000 + (match && row < 8 ? 256 + row * 16 : 384);
    vKv = g * 4096 + r * 16; vKp = (lane >> 2) * 4096 + 3712 + (lane & 3) * 4;
    vOv = (tok >> 4) * 20480 + (tok & 15) * 16 + h * 2048; vOp = g * 20480 + q * 256 + p4 * 4;
    vXv = ((w & 1) * 16 + r) * 16 + g * 1024; vSt = h * 4096 + tok * 8; vB3 = 2048 + h * 384 + tok * 16;
    asm volatile("" : "+v"(vKa), "+v"(vVa), "+v"(vHv), "+v"(vImg), "+v"(vA0), "+v"(vA2), "+v"(vB), "+v"(vS0), "+v"(vS1), "+v"(vK0));
    asm volatile("" : "+v"(vK1), "+v"(vF0), "+v"(vF1), "+v"(vKv), "+v"(vKp), "+v"(vOv), "+v"(vOp), "+v"(vXv), "+v"(vSt), "+v"(vB3));
    if (tid < 10) sts128(L, L_KVA + tid * 400 + 384, v4u{0u, 0u, 0u, 0u});
  };
  unsigned xr[10];
  Tmo tm;
  { RELANE();
    const int l = 0; (void)l;
    STAMP(0);
    constexpr unsigned SG_B = L_P0 + 2048, SG_O = SG_B + 10240, SG_P = SG_O + 10240, OOR = 0x80000000u;
    const int j = (tid >> 4) & 7, cc = lane & 15, seg = (cc * 13) >> 6, g = lane >> 4;
    v4u ra[3], rb[3], ro[3], rp;
    const unsigned wcol = (seg * 640 + 40 * cg + 8 * (cc - 5 * seg)) * 2;
    unsigned wrow[3];
#pragma unroll
    for (int i = 0; i < 3; ++i) {
      const int kap = 2 * i + (w >> 1) < 4 ? 2 * i + (w >> 1) : 4, lc = 5 * (rg & 1) + kap;
      const int t8 = lc >> 3, rb0 = 80 * (rg >> 1) + (1 - t8) * (20 * (lc >> 1) + 8 * (lc & 1)) + t8 * (40 * (lc - 8) + 16);
      wrow[i] = (rb0 + j + t8 * (16 * (j >> 2))) * 3840 + wcol;
    }
    { const rsrc_t rA = __builtin_amdgcn_make_buffer_rsrc((void*)p.wqA, 0, 640 * 1920 * 2, 0x00020000);
#pragma unroll
      for (int i = 0; i < 3; ++i) ra[i] = ldg(rA, wrow[i], 0); }
    load_x_in(p.x, rg, cg, w, lane, xr);
    const unsigned ep = __builtin_amdgcn_raw_buffer_load_b32(rs, 0, WS_CTRL + 128, SC1);
    FENCE(); SB();
    { const rsrc_t rB = __builtin_amdgcn_make_buffer_rsrc((void*)p.wqB, 0, 640 * 1920 * 2, 0x00020000);
      const rsrc_t rO = __builtin_amdgcn_make_buffer_rsrc((void*)(half ? p.woB : p.woA), 0, 640 * 640 * 2, 0x00020000);
      const rsrc_t rP = __builtin_amdgcn_make_buffer_rsrc((void*)(p.wphi + cg * 800), 0, 1600, 0x00020000);
#pragma unroll
      for (int i = 0; i < 3; ++i) rb[i] = ldg(rB, wrow[i], 0);
#pragma unroll
      for (int i = 0; i < 3; ++i) { const int so = 2 * i + (w >> 1) < 4 ? 2 * i + (w >> 1) : 4, t4 = so >> 2;
        ro[i] = ldg(rO, (40 * rg + (1 - t4) * (20 * (so >> 1) + 8 * (so & 1)) + t4 * 16 + j + t4 * (16 * (j >> 2))) * 1280 + (80 * c8 + 8 * (cc < 10 ? cc : 0)) * 2, 0); }
      rp = ldg(rP, tid * 16, 0);
    FENCE(); SB();
    STAMP(53);
    const unsigned sa = tid * 16;
#pragma unroll
    for (int i = 0; i < 3; ++i) if (i < 2 || w < 2) sts128(L, sa + i * 4096, ra[i]);
    BAR();
    STAMP(54);
    { v4u fr[3];
      build_wq_read(L, 0, w, lane, fr);
      publish_x(rs, WS_X, WS_ST0, rg, cg, w, lane, xr);
      build_wq_store(rs, 0, rg, cg, w, lane, 0, w < 3 ? 32 : 24, fr); }
    tm.ws = p.ws; tm.epoch = __builtin_amdgcn_readfirstlane(ep) + 0x9E3779B1u;
    STAMP(55);
#pragma unroll
    for (int i = 0; i < 3; ++i) if (i < 2 || w < 2) { sts128(L, SG_B + sa + i * 4096, rb[i]); sts128(L, SG_O + sa + i * 4096, ro[i]); }
    if (w < 2) sts128(L, SG_P + sa, rp);
    }
    DRAIN();
    BAR();
    if (tid == 0) __builtin_amdgcn_raw_buffer_store_b32(tm.epoch, rs, 0, WS_FLAG + (cg * 16 + rg) * 128, SC1);
    STAMP(60);
    v4u fv[4], fb[3], fo[2];
    {
      const int sg = w >> 1, hw = w & 1;
      v4u bf[2], af[4];
#pragma unroll
      for (int ct = 0; ct < 2; ++ct) {
        const int q = (lane >> 2) & 3, pp = lane & 3, fcol = ct ? 16 : 4 * pp;
        const int d0 = 8 * g + q < 19 ? 8 * g + q : 19, d1 = 8 * g + 4 + q < 19 ? 8 * g + 4 + q : 19;
        const v2u t0 = ldstr(L, SG_P + hw * 800 + d0 * 40 + fcol * 2), t1 = ldstr(L, SG_P + hw * 800 + d1 * 40 + fcol * 2);
        bf[ct] = v4u{g < 3 ? t0.x : 0u, g < 3 ? t0.y : 0u, g < 2 ? t1.x : 0u, g < 2 ? t1.y : 0u};
      }
#pragma unroll
      for (int i = 0; i < 4; ++i) { const int r = (i < 2 ? 8 * (cc >> 2) : 32) + 4 * (i & 1) + (cc & 3); const unsigned a = SG_B + r * 256 + sg * 80 + hw * 40 + g * 16;
        const v2u t0 = lds64(L, a), t1 = lds64(L, a + 8); af[i] = v4u{t0.x, t0.y, t1.x, t1.y}; }
      build_wq_read(L, SG_B, w, lane, fb);
      { const int q = (lane >> 2) & 3, pp = lane & 3;
#pragma unroll
        for (int it = 0; it < 2; ++it) {
          const int pc = it * 16 + (tid >> 4), pcc = pc < 25 ? pc : 24, so = (pcc * 13) >> 6, ft = pcc - 5 * so;
          const unsigned a = SG_O + (so * 8 + q) * 256 + 40 * pp + 8 * ft;
          const v2u t0 = ldstr(L, a), t1 = ldstr(L, a + 1024);
          fo[it] = v4u{t0.x, t0.y, t1.x, t1.y};
        } }
      f32x4 d[8];
      asm volatile("s_nop 1" : "+v"(bf[0]), "+v"(bf[1]), "+v"(af[0]), "+v"(af[1]), "+v"(af[2]), "+v"(af[3]));
      SFOR(i, 8) mf16v<true>(d[i], af[i & 3], bf[i >> 2]); SEND;
      asm volatile("s_nop 7");
      SB();
#pragma unroll
      for (int i = 0; i < 4; ++i) fv[i] = v4u{pk2(d[2 * i][0], d[2 * i][1]), pk2(d[2 * i][2], d[2 * i][3]), pk2(d[2 * i + 1][0], d[2 * i + 1][1]), pk2(d[2 * i + 1][2], d[2 * i + 1][3])};
    }
    STAMP(58);
    { const int sg = w >> 1, hw = w & 1;
#pragma unroll
      for (int ct = 0; ct < 2; ++ct) {
        const int row = ct ? (sg ? 0 : 16) + 4 * hw + (cc & 3) : 8 * (cc >> 2) + 4 * hw + (cc & 3);
        const unsigned tb = ct ? 2048 : (sg ? 0 : L_P0), ts = (ct || sg) ? KSTEP : 1024, th = ct ? 384 : 512;
        const bool in = ct == 0 || cc < 4;
#pragma unroll
        for (int u = 0; u < 2; ++u) { const int c = 5 * rg + (u ? 4 : g);
          stg_sc1(rs, (in && (u == 0 || g == 0)) ? (c >> 1) * ts + (c & 1) * th + row * 16 : OOR, WS_WQ + (16 + cg) * WQ_SZ + tb, fv[2 * ct + u]); }
        float cs = sum8(fv[2 * ct], 0.f);
        cs = g ? cs : sum8(fv[2 * ct + 1], cs);
        { auto t = __builtin_amdgcn_permlane16_swap(f2u(cs), f2u(cs), false, false); cs = u2f(t[0]) + u2f(t[1]); }
        { auto t = __builtin_amdgcn_permlane32_swap(f2u(cs), f2u(cs), false, false); cs = u2f(t[0]) + u2f(t[1]); }
        __builtin_amdgcn_raw_buffer_store_b32(f2u(cs), rs, (in && g == 0) ? row * 4 : OOR, WS_CSP + ((16 + cg) * 16 + rg) * 512 + (ct ? 3 : sg) * 128, SC1);
      }
      if (w >= 2) build_wq_store(rs, 1, rg, cg, w, lane, w == 2 ? 0 : 8, w == 2 ? 32 : 16, fb);
#pragma unroll
      for (int it = 0; it < 2; ++it) { const int pc = it * 16 + (tid >> 4);
        stg_sc1(rs, pc < 25 ? pc * 1024 + cc * 16 : OOR, WS_WO + (half * 8 + c8) * WO_SZ + (rg >> 2) * 25600 + (rg & 3) * 256, fo[it]); }
    }
    poison_o(rs, WS_O, rg, cg, w, lane);
    stg_sc1(rs, l16, WS_KV + (cg * 16 + rg) * 4096 + w * 1024, v4u{SENT, SENT, SENT, SENT});
    stg_sc1(rs, l16, WS_HALO + (rg * 16 + cg) * 4096 + w * 1024, v4u{SENT, SENT, SENT, SENT});
    addresses();
    STAMP(61);
    if (w == 1) wait_flags(rs, (h == 0 ? cc * 16 + rg : cg * 16 + cc) * 128, WS_FLAG, tm);
    BAR();
    STAMP(62);
    const rsrc_t rw = __builtin_amdgcn_make_buffer_rsrc(p.ws + WS_WQ + cg * WQ_SZ, 0, WQ_SZ, 0x00020000);
    SFOR(i, 22) dmaw<i>(rw, l16, w * 1024); SEND;
    float csv[16];
#pragma unroll
    for (int r = 0; r < 16; ++r) csv[r] = u2f(__builtin_amdgcn_raw_buffer_load_b32(rs, (tid & 127) * 4 + (r & 7) * 512, WS_CSP + cg * 8192 + (r >> 3) * 4096, SC1));
    FENCE(); SB();
    STAMP(63);
    FENCE(); SB();
    STAMP(57);
    { float cs = 0.f;
#pragma unroll
      for (int r = 0; r < 16; ++r) cs += csv[r];
      const unsigned ch = pk2(cs, 0.f) & 0xffffu, cl = pk2(cs - lo16(ch), 0.f) & 0xffffu;
      asm volatile("" :: "v"(cs));
      if (tid < 128) sts128(L, L_CS + tid * 16, v4u{ch | (cl << 16), ch, 0u, 0u}); }
    BAR();
    if (tid == 0) __builtin_amdgcn_raw_buffer_store_b32(tm.epoch, rs, 0, WS_FLAG + (256 + cg * 16 + rg) * 128, SC1);
  }

  unsigned sX = WS_X + (rg * 4 + w) * 40960, sSt = WS_ST0 + (rg * 1024 + w * 32) * 8, sWo = WS_WO + (c8 * 4 + w) * 25600;
  unsigned sOp = WS_O + ((rg * 8 + 2 * w) * 4 + (cg >> 2)) * 5120 + (cg & 3) * 256, sOr = WS_O + ((rg * 8 + half * 4) * 4 + w) * 5120;
  unsigned sXp = WS_X + XB + ((rg * 4 + half * 2 + (w >> 1)) * 40 + 5 * c8) * 1024, sSp = WS_ST1 + ((rg * 8 + c8) * 128 + half * 64 + w * 16) * 8;
  pin_s(sX); pin_s(sSt); pin_s(sWo); pin_s(sOp); pin_s(sOr); pin_s(sXp); pin_s(sSp);
  for (int l = 0; l < 4; ++l) {
    const int m = l & 1;
    const rsrc_t rw = __builtin_amdgcn_make_buffer_rsrc(p.ws + WS_WQ + ((m ^ 1) * 16 + cg) * WQ_SZ, 0, l < 3 ? WQ_SZ : 0u, 0x00020000);
    const unsigned wb1k = w * 1024;
#define DM(i) do { SB(); dmaw<(i)>(rw, l16, wb1k); SB(); } while (0)
    unsigned wos;
#define LDWO(i) do { lda<160 + 4 * (i), ((i) & 3) * 1024, true>(rs, l16, wos); if constexpr (((i) & 3) == 3) sbump(wos); } while (0)
    v4u wo24, mfr;
    f32x16 a0, a1, a2, a3;
    float rstd, sd, o[20];
    unsigned osb[4], ops, cXp, cSp, cOp;
    {
      RELANE();
      STAMP(1 + 12 * l);
      asm volatile("" : "+v"(vSt), "+v"(vB3));
      const unsigned xs = sX, sts = sSt;
#define LDX(s) do { lda<4 * (s), ((s) & 3) * 1024, true>(rs, l16, xso); if constexpr (((s) & 3) == 3) sbump(xso); } while (0)
      const unsigned stv = vSt;
      constexpr int NB = PFD + 1, HI = 23;
      constexpr int XR = 15 - XW;
      const unsigned b12 = l16, b3 = vB3;
      unsigned b12h, b3h, bcs;
      unsigned f1 = 0, f1v = 0;
      const rsrc_t rl = __builtin_amdgcn_make_buffer_rsrc(p.ws + WS_WQ + (m * 16 + cg) * WQ_SZ, 0, WQ_SZ, 0x00020000);
      for (unsigned att = 0, att2 = 0, fresh = 1;; fresh = 0) {
        v2u st[4];
        unsigned xso = xs;
        wos = sWo;
        asm volatile("buffer_load_dwordx2 %0, %4, %5, %6 offen sc1\n\tbuffer_load_dwordx2 %1, %4, %5, %6 offen offset:1024 sc1\n\t"
                     "buffer_load_dwordx2 %2, %4, %5, %6 offen offset:2048 sc1\n\tbuffer_load_dwordx2 %3, %4, %5, %6 offen offset:3072 sc1"
                     : "=&v"(st[0]), "=&v"(st[1]), "=&v"(st[2]), "=&v"(st[3]) : "v"(stv), "s"(rs), "s"(sts) : "memory");
        SFOR(s, XW) SB(); LDX(s); if constexpr (s == 1 || s == 5) { SB(); if (fresh) BARE(); } SEND;
        FENCE(); SB();
        v4u wb[NB][3];
        auto rd = [&](auto s_, auto t_) __attribute__((always_inline)) {
          constexpr int s = decltype(s_)::v, t = decltype(t_)::v;
          if constexpr (s < 40) dsr<(s < HI ? s : s - HI) * KSTEP + (t == 1 ? 1024 : 0)>(wb[s % NB][t], t == 2 ? (s < HI ? b3 : b3h) : (s < HI ? b12 : b12h));
          else if constexpr (s == 40) dsr<(t + 1) * 512>(wb[s % NB][t], bcs);
        };
        SFOR(j, PFD) rd(IC<j>{}, IC<2>{}); rd(IC<j>{}, IC<1>{}); rd(IC<j>{}, IC<0>{}); SEND;
        asm volatile("s_waitcnt vmcnt(%4)" : "+v"(st[0]), "+v"(st[1]), "+v"(st[2]), "+v"(st[3]) : "i"(XW) : "memory");
        unsigned ms = max3u(max3u(st[0].x, st[0].y, st[1].x), max3u(st[1].y, st[2].x, st[2].y), umax(st[3].x, st[3].y));
        if (__builtin_expect(any_sent(ms), 0)) {
          CNT(0);
          if (++att2 < 64) {
            poll_probe<200>(rs, (lane >> 3) * 1024 + (lane & 4) * 32 + 4, sts, tm);
            lgkm<0>();
            continue;
          }
        }
        float sa, sq, nm;
        auto stat = [&](auto s_) __attribute__((always_inline)) {
          constexpr int s = decltype(s_)::v;
          if constexpr (s == 1) { sa = u2f(st[0].x) + u2f(st[1].x); asm volatile("" : "+v"(sa)); sq = u2f(st[0].y) + u2f(st[1].y); asm volatile("" : "+v"(sq)); }
          if constexpr (s == 2) { sa += u2f(st[2].x); asm volatile("" : "+v"(sa)); sq += u2f(st[2].y); asm volatile("" : "+v"(sq)); }
          if constexpr (s == 3) { sa += u2f(st[3].x); asm volatile("" : "+v"(sa)); sq += u2f(st[3].y); asm volatile("" : "+v"(sq)); }
          if constexpr (s == 5) { auto x0 = __builtin_amdgcn_permlane32_swap(f2u(sa), f2u(sa), false, false); nm = u2f(x0[0]) + u2f(x0[1]); asm volatile("" : "+v"(nm)); }
          if constexpr (s == 6) { auto x1 = __builtin_amdgcn_permlane32_swap(f2u(sq), f2u(sq), false, false); sq = u2f(x1[0]) + u2f(x1[1]); asm volatile("" : "+v"(sq)); }
          if constexpr (s == 7) { nm *= -1.f / 640.f; sq = sq * (1.f / 640.f) - nm * nm; asm volatile("" : "+v"(nm), "+v"(sq)); }
          if constexpr (s == 8) { sq = __builtin_fmaxf(sq, 0.f) + 1e-5f; rstd = __builtin_amdgcn_rsqf(sq); asm volatile("" : "+v"(rstd), "+v"(sq)); }
          if constexpr (s == 9) { sd = sq * rstd; mfr.x = pk2(nm, nm); asm volatile("" : "+v"(sd), "+v"(mfr.x)); }
          if constexpr (s == 10) { mfr.y = pk2(nm - lo16(mfr.x), 0.f); asm volatile("" : "+v"(mfr.y)); }
          if constexpr (s == 11) { mfr = v4u{h ? 0u : mfr.x, h ? 0u : mfr.y, 0u, 0u}; asm volatile("" : "+v"(mfr)); }
          if constexpr (s == 12) { b12h = b12 + HI * KSTEP; b3h = b3 + HI * KSTEP; asm volatile("" : "+v"(b12h), "+v"(b3h)); }
          if constexpr (s == 13) { bcs = L_CS + m * 2048 + (l16 & 0x1f0); asm volatile("" : "+v"(bcs)); }
        };
        SFOR(s, 40)
          SB();
          if constexpr (s == 20) {
            if (l == 0) { const int ln = fresh_lane(); f1v = (ln < 16 ? ln * 16 + rg : (ln < 32 ? cg : (c8 >> 2) * 8 + ((ln >> 4) & 1) * 4 + (c8 & 3)) * 16 + (ln & 15)) * 128; probe_issue(f1, rs, f1v, WS_FLAG + 256 * 128); }
            SB();
          }
          if constexpr (s == 25) {
            if (l == 0) {
              probe_wait<5>(f1);
              if (__builtin_amdgcn_ballot_w64(f1 != tm.epoch) != 0) wait_flags(rs, f1v, WS_FLAG + 256 * 128, tm);
            }
            SB();
          }
          if constexpr (s == 26) { if (att == 0) BARE(); SB(); }
          if constexpr (s == 10) { if (att == 0) BARE(); SB(); }
          constexpr int c = s % NB;
          constexpr int issued = s <= XR ? XW + 2 * s : (15 + s < 40 ? 15 + s : 40);
          constexpr int t = s < XW + 2 * XR ? -1 : s - 15, plo = t + 1 > 5 ? t + 1 : 5, phi_ = s < 21 ? s : 21, wy = phi_ > plo ? phi_ - plo : 0;
          asm volatile("s_waitcnt vmcnt(%0) lgkmcnt(%1)" :: "i"(issued - 1 - s + (s > 25 ? s - 25 : 0) + wy), "i"(3 * ((s + PFD <= 41 ? PFD : 41 - s) - 1)) : "memory");
          mf32a<s == 0, 4 * s>(a1, wb[c][0]);
          if constexpr (s == 0) STAMP(2 + 12 * l);
          rd(IC<s + PFD>{}, IC<2>{});
          if constexpr (s < XR) LDX(issued);
          SB();
          mf32a<s == 0, 4 * s>(a2, wb[c][1]);
          rd(IC<s + PFD>{}, IC<1>{});
          if constexpr (s >= 5 && s < 20) dmaw<22 + (s >= 5 ? s - 5 : 0)>(rl, l16, wb1k);
          if constexpr (s == 20) dmaw<0>(rl, l16, wb1k + (w < 2 ? 37 : 36) * 4096);
          stat(IC<s>{});
          SB();
          mf32a<s == 0, 4 * s>(a3, wb[c][2]);
          rd(IC<s + PFD>{}, IC<0>{});
                    if constexpr (s < 25) LDX(issued + (s < XR ? 1 : 0)); else LDWO(s - 25);
        SEND;
        SB();
        lgkm<0>();
        mf32v<false>(a1, wb[40 % NB][0], mfr); mf32v<false>(a3, wb[40 % NB][2], mfr); mf32v<false>(a2, wb[40 % NB][1], mfr);
        MFMA_SETTLE();
        SB();
        if (__builtin_expect(!any_nan(a1[0]), 1)) break;
        CNT(1);
        if (++att > 32) break;
      }
      STAMP(3 + 12 * l);
    }
    BARE();
    v4u PV;
    asm volatile("v_mov_b32 %0, -1\n\tv_mov_b32 %1, -1\n\tv_mov_b32 %2, -1\n\tv_mov_b32 %3, -1" : "=v"(PV.x), "=v"(PV.y), "=v"(PV.z), "=v"(PV.w));
    if (l == 0) {
      RELANE();
      stg_sc1(rs, vXv, sXp, PV); stg_sc1(rs, vXv + 512, sXp, PV); stg_sc1(rs, (vXv & 0x1f0) + (h << 9), sXp + 4096, PV);
      __builtin_amdgcn_raw_buffer_store_b64(v2u{PV.x, PV.y}, rs, (lane & 15) * 8, sSp, SC1);
      stg_sc1(rs, l16, WS_KV + (256 + cg * 16 + rg) * 4096 + w * 1024, PV);
      stg_sc1(rs, l16, WS_HALO + (256 + rg * 16 + cg) * 4096 + w * 1024, PV);
    }
    unsigned pz[5], psx[4];
    { const unsigned ko = l < 3 ? 0u : 0x80000000u, kx = l < 2 ? 0u : 0x80000000u, ln = fresh_lane();
      pz[0] = vOv | ko; pz[1] = (vOv & ~2048u) | ko; pz[2] = vXv | kx; pz[3] = ((vXv & 0x1f0) + ((ln >> 5) << 9)) | kx; pz[4] = ((ln & 15) * 8) | kx;
    }
#pragma unroll
    for (int i = 0; i < 4; ++i) osb[i] = sOr + ((w + 1 + i) & 3) * 20480;
    ops = sOr + 4096; cXp = sXp; cSp = sSp; cOp = sOp;
    sX += XB; sSt += STB; sWo += m ? -8 * WO_SZ : 8 * WO_SZ; sOp += XB; sOr += XB; sXp += XB; sSp += STB;
    psx[0] = sOp; psx[1] = sOp + 4096; psx[2] = sXp; psx[3] = sXp + 4096;
#define P2N(pp) ((pp) - (pp) / 3 - 1)
#define P2P(pp) ((pp) % 3 == 2 ? (pp) / 3 : (pp) - 10)
#define P2VM(pp) ((pp) == 0 || (pp) > 16 ? 0 : ((pp) % 3 != 2 && P2N(pp) < 9) ? 1 : P2P(pp) == 6 ? 4 : 3)
#define P2VI(pp) (P2VM(pp) == 1 ? 15 + P2N(pp) : P2VM(pp) == 3 ? (P2P(pp) == 1 ? 1024 : P2P(pp) == 4 ? 512 : 0) : 0)
#define P2VO(pp) (P2VM(pp) < 3 ? l16 : P2P(pp) < 2 ? pz[0] : P2P(pp) == 2 ? pz[1] : P2P(pp) < 5 ? pz[2] : P2P(pp) == 5 ? pz[3] : pz[4])
#define P2SO(pp) (P2VM(pp) < 3 ? wos : P2P(pp) < 2 ? psx[0] : P2P(pp) == 2 ? psx[1] : P2P(pp) < 5 ? psx[2] : P2P(pp) == 5 ? psx[3] : sSp)
#define P2PAIR(pp, nw) do { p2pair<2 * (pp), nw, P2VM(pp), P2VI(pp)>(a0, wq[(2 * (pp)) % 6], wq[(2 * (pp) + 1) % 6], bq, bcs, rs, P2VO(pp), P2SO(pp), PV); \
                            if constexpr (P2VM(pp) == 1 && (P2VI(pp) & 3) == 3) sbump(wos); } while (0)
    constexpr int NDM2 = 0;
    if (m == 0) {
      RELANE();
      const unsigned bq = L_P0 + l16, bcs = L_CS + tok * 16;
      asm volatile("" : "+v"(vKa), "+v"(vVa), "+v"(vHv));
      v4u wq[6], kc0, kc1, kc2, hd[4];
      const unsigned hsrc = WS_HALO + (((l >> 1) * 16 + rg - 1) * 16 + cg) * 4096;
#define VCH(i) ((i) < 4 ? v4u{f2u(a2[4 * (i)]), f2u(a2[4 * (i) + 1]), f2u(a2[4 * (i) + 2]), f2u(a2[4 * (i) + 3])} : v4u{f2u(a3[4]), f2u(a3[5]), f2u(a3[6]), f2u(a3[7])})
      SFOR(j, 6) dsr<j * 1024>(wq[j], bq); SEND;
      {
        f32x2 t[10];
#pragma unroll
        for (int i = 0; i < 8; ++i) t[i] = pkmul(f32x2{a1[2 * i], a1[2 * i + 1]}, f32x2{rstd, rstd});
#pragma unroll
        for (int i = 0; i < 2; ++i) t[8 + i] = pkmul(f32x2{a3[2 * i], a3[2 * i + 1]}, f32x2{rstd, rstd});
        kc0 = v4u{pk2(t[0][0], t[0][1]), pk2(t[1][0], t[1][1]), pk2(t[2][0], t[2][1]), pk2(t[3][0], t[3][1])};
        kc1 = v4u{pk2(t[4][0], t[4][1]), pk2(t[5][0], t[5][1]), pk2(t[6][0], t[6][1]), pk2(t[7][0], t[7][1])};
        kc2 = v4u{pk2(t[8][0], t[8][1]), pk2(t[9][0], t[9][1]), 0u, 0u};
#pragma unroll
        for (int i = 0; i < 8; ++i) { const f32x2 u = pkmul(f32x2{a2[2 * i], a2[2 * i + 1]}, f32x2{rstd, rstd}); a2[2 * i] = u[0]; a2[2 * i + 1] = u[1]; }
#pragma unroll
        for (int i = 2; i < 4; ++i) { const f32x2 u = pkmul(f32x2{a3[2 * i], a3[2 * i + 1]}, f32x2{rstd, rstd}); a3[2 * i] = u[0]; a3[2 * i + 1] = u[1]; }
        asm volatile("" : "+v"(kc0), "+v"(kc1), "+v"(kc2), "+v"(a2), "+v"(a3)); }
      unsigned qp[10];
      const rsrc_t rh = __builtin_amdgcn_make_buffer_rsrc(p.ws + hsrc, 0, rg > 0 ? 4096u : 0u, 0x00020000);
      SFOR(pp, 20)
        SB();
        constexpr int nwr = (pp - 1 >= 1 ? ((pp - 1 > 15 ? 15 : pp - 1) + 1) / 2 : 0) - (pp - 4 >= 1 ? ((pp - 4 > 15 ? 15 : pp - 4) + 1) / 2 : 0);
        P2PAIR(pp, (2 * pp + 6 <= 41 ? 4 : 39 - 2 * pp) + nwr);
        if constexpr (pp == 0) {
          if (w == 3) {
            const unsigned hs = hsrc + 65536;
            stg_sc1(rs, vHv, hs, kc0); stg_sc1(rs, vHv + 512, hs, kc1); stg_sc1(rs, vHv + 1024, hs, kc2);
#pragma unroll
            for (int i = 0; i < 5; ++i) stg_sc1(rs, vHv + 1536 + i * 512, hs, VCH(i));
          }
        }
        if constexpr (pp == 1) DSW(vKa, 14 * 48, kc0);
        if constexpr (pp == 3) DSW(vKa, 14 * 48 + 16, kc1);
        if constexpr (pp == 5) DSW(vKa, 14 * 48 + 32, kc2);
        if constexpr (pp >= 7 && pp < 16 && (pp & 1)) { constexpr int vi = pp >= 7 ? (pp - 7) / 2 : 0; DSW(vVa, 14 * 80 + vi * 16, VCH(vi)); }
        if constexpr (pp == 12) { qp[8] = pk2(a3[8], a3[9]); qp[9] = pk2(a3[10], a3[11]); asm volatile("" : "+v"(qp[8]), "+v"(qp[9])); }
        if constexpr (pp == 17) wo24 = ldg_sc1(rs, l16, wos);
        if constexpr (pp == HLP) {
          if (w == 0) {
#pragma unroll
            for (int i = 0; i < 4; ++i) hd[i] = ldg_sc1(rh, l16 + i * 1024, 0);
          }
        }
      SEND;
      SB();
      lgkm<0>();
      mf32v<false>(a0, wq[40 % 6], mfr);
      vmc<17>();
      BARE();
      const unsigned ka = vKa, va = vVa;
      const float fq = rstd * (LOG2E * 0.22360679774997896f);
      STAMP(5 + 12 * l);
      if (w == 0) {
        const unsigned q_ = ((lane >> 4) & 1) * 144 + (lane & 15);
        unsigned h0 = L_K + q_ * 48 + h * 16, h1 = h ? L_V + q_ * 80 : L_K + q_ * 48 + 32, h2 = L_V + q_ * 80 + 16 + h * 16;
        asm volatile("" : "+v"(h0), "+v"(h1), "+v"(h2));
        unsigned mm = 0;
#pragma unroll
        for (int i = 0; i < 4; ++i) mm = mx4(mm, hd[i]);
        if (__builtin_expect(any_sent(mm), 0)) {
          unsigned sp = 0;
          do {
            FENCE();
            mm = 0;
#pragma unroll
            for (int i = 0; i < 4; ++i) hd[i] = ldg_sc1(rh, l16 + i * 1024, 0);
#pragma unroll
            for (int i = 0; i < 4; ++i) mm = mx4(mm, hd[i]);
            CNT(2);
          } while (any_sent(mm) && !give_up(sp, tm));
        }
        sts128(L, h0, hd[0]);
        sts128(L, h1, hd[1]);
        sts128(L, h2, hd[2]);
        sts128(L, h2 + 32, hd[3]);
      }
      STAMP(4 + 12 * l);
      float lg[8];
      v4u kb[8][2], vb[8][5];
      v2u kt[8];
      auto rdk = [&](auto j_) __attribute__((always_inline)) { constexpr int j = decltype(j_)::v;
        kb[j][0] = lds128(L, ka + (7 - j) * 96); kb[j][1] = lds128(L, ka + (7 - j) * 96 + 16); kt[j] = lds64(L, ka + (7 - j) * 96 + 32); };
      auto rdv = [&](auto j_) __attribute__((always_inline)) { constexpr int j = decltype(j_)::v;
#pragma unroll
        for (int i = 0; i < 5; ++i) vb[j][i] = lds128(L, va + (7 - j) * 160 + i * 16); };
      SB();
      rdk(IC<0>{}); rdk(IC<1>{}); rdk(IC<2>{}); rdk(IC<3>{});
      SB();
#pragma unroll
      for (int i = 0; i < 8; ++i) qp[i] = pk2(a0[2 * i], a0[2 * i + 1]);
      SFOR(jj, 4)
        SB();
        if constexpr (jj < ADM) DM(NDM2 + jj);
        if constexpr (jj < 2) { rdk(IC<2 * jj + 4>{}); rdk(IC<2 * jj + 5>{}); } else rdv(IC<jj - 2>{});
        SB();
        float s0 = 0.f, s1 = 0.f;
#pragma unroll
        for (int i = 0; i < 8; ++i) { s0 = dot2(kb[2 * jj][i >> 2][i & 3], qp[i], s0); s1 = dot2(kb[2 * jj + 1][i >> 2][i & 3], qp[i], s1); }
#pragma unroll
        for (int i = 0; i < 2; ++i) { s0 = dot2(kt[2 * jj][i], qp[8 + i], s0); s1 = dot2(kt[2 * jj + 1][i], qp[8 + i], s1); }
        asm volatile("" : "+v"(s0), "+v"(s1));
        lg[2 * jj] = s0; lg[2 * jj + 1] = s1;
      SEND;
      SB();
      if constexpr (4 < ADM) DM(NDM2 + 4);
      rdv(IC<2>{});
      SB();
      if (rg == 0 && w == 0) {
#pragma unroll
        for (int j = 1; j < 8; ++j) lg[j] = tok >= 2 * j ? lg[j] : -__builtin_inff();
      }
      const float mx = max3f(max3f(lg[0], lg[1], lg[2]), max3f(lg[3], lg[4], lg[5]), max3f(lg[6], lg[7], lg[7]));
      const float nb = -fq * mx;
#pragma unroll
      for (int j = 0; j < 8; j += 2) { const f32x2 t = pkfma(f32x2{lg[j], lg[j + 1]}, f32x2{fq, fq}, f32x2{nb, nb}); lg[j] = t[0]; lg[j + 1] = t[1]; }
      SB();
#pragma unroll
      for (int j = 0; j < 8; ++j) lg[j] = __builtin_amdgcn_exp2f(lg[j]);
      SB();
      if constexpr (5 < ADM) DM(NDM2 + 5);
      SB();
      const float inv = __builtin_amdgcn_rcpf(((lg[0] + lg[1]) + (lg[2] + lg[3])) + ((lg[4] + lg[5]) + (lg[6] + lg[7])));
      f32x2 oa[10];
      SFOR(j, 8)
        SB();
        if constexpr (6 + j < ADM) DM(NDM2 + 6 + j);
        if constexpr (j < 5) rdv(IC<j + 3>{});
        SB();
        const float pj = lg[j];
        SFOR(i, 5)
          const f32x4 v = __builtin_bit_cast(f32x4, vb[j][i]);
          if constexpr (j == 0) { oa[2 * i] = pkmul(f32x2{pj, pj}, f32x2{v[0], v[1]}); oa[2 * i + 1] = pkmul(f32x2{pj, pj}, f32x2{v[2], v[3]}); }
          else { oa[2 * i] = pkfma(f32x2{pj, pj}, f32x2{v[0], v[1]}, oa[2 * i]); oa[2 * i + 1] = pkfma(f32x2{pj, pj}, f32x2{v[2], v[3]}, oa[2 * i + 1]); }
        SEND;
      SEND;
#pragma unroll
      for (int i = 0; i < 10; ++i) oa[i] = pkmul(oa[i], f32x2{inv, inv});
#pragma unroll
      for (int i = 0; i < 10; ++i) { o[2 * i] = oa[i][0]; o[2 * i + 1] = oa[i][1]; }
    } else {
      RELANE();
      asm volatile("" : "+v"(vImg), "+v"(vA0), "+v"(vA2), "+v"(vB), "+v"(vS0), "+v"(vS1), "+v"(vK0), "+v"(vK1), "+v"(vF0), "+v"(vF1), "+v"(vKv), "+v"(vKp));
      const float sl = rstd * LOG2E;
      {
        const unsigned rb = vImg;
        float kk[20];
#pragma unroll
        for (int i = 0; i < 16; ++i) kk[i] = a1[i];
#pragma unroll
        for (int i = 0; i < 4; ++i) kk[16 + i] = a3[i];
        unsigned ph[20];
        phi_map<true>(kk, sl, rstd, ph, [&](auto i_) __attribute__((always_inline)) { constexpr int i = decltype(i_)::v; SB();
          dmaw<NDM2 + i>(rw, l16, wb1k);
          if (i == 0) sts128(L, rb ^ (5 << 4), v4u{pk2(a2[0], a2[1]), pk2(a2[2], a2[3]), pk2(a2[4], a2[5]), pk2(a2[6], a2[7])});
          if (i == 1) sts128(L, rb ^ (6 << 4), v4u{pk2(a2[8], a2[9]), pk2(a2[10], a2[11]), pk2(a2[12], a2[13]), pk2(a2[14], a2[15])});
          if (i == 2) sts128(L, rb ^ (7 << 4), v4u{pk2(a3[4], a3[5]), pk2(a3[6], a3[7]), pk2(sd, 0.f), 0u});
          if (i == 5) sts128(L, rb ^ (0 << 4), v4u{ph[0], ph[1], ph[2], ph[3]});
          if (i == 6) sts128(L, rb ^ (1 << 4), v4u{ph[4], ph[5], ph[6], ph[7]});
          SB(); });
#pragma unroll
        for (int c = 2; c < 5; ++c) sts128(L, rb ^ (c << 4), v4u{ph[4 * c], ph[4 * c + 1], ph[4 * c + 2], ph[4 * c + 3]});
      }
      const unsigned bq = L_P0 + l16, bcs = L_CS + 2048 + tok * 16;
      v4u wq[6];
      {
        const unsigned aA0 = vA0, aA2 = vA2, aB = vB;
        unsigned aA0x = (aA0 ^ 64) + 512, aA2x = (aA2 ^ 64) + 512, aBx = (aB ^ 64) + 512;
        asm volatile("" : "+v"(aA0x), "+v"(aA2x), "+v"(aBx));
        BAR();
        f32x4 d0, d1, d2;
        v2u t[4][8];
        auto trd = [&](auto k_) __attribute__((always_inline)) {
          constexpr int ks = decltype(k_)::v; constexpr unsigned o_ = ks * 4096;
          t[ks][0] = ldstr(L, aB + o_); t[ks][1] = ldstr(L, aBx + o_); t[ks][6] = ldstr(L, aA2 + o_); t[ks][7] = ldstr(L, aA2x + o_);
          t[ks][2] = ldstr(L, aA0 + o_); t[ks][3] = ldstr(L, aA0x + o_); t[ks][4] = ldstr(L, aA0 + 8 + o_); t[ks][5] = ldstr(L, aA0x + 8 + o_);
          SB();
        };
        trd(IC<0>{});
        SFOR(ks, 4)
          if constexpr (ks < 3) trd(IC<ks + 1>{});
          const v4u bb = {t[ks][0].x, t[ks][0].y, t[ks][1].x, t[ks][1].y};
          mf16v<ks == 0>(d2, v4u{t[ks][6].x, t[ks][6].y, t[ks][7].x, t[ks][7].y}, bb);
          mf16v<ks == 0>(d0, v4u{t[ks][2].x, t[ks][2].y, t[ks][3].x, t[ks][3].y}, bb);
          mf16v<ks == 0>(d1, v4u{t[ks][4].x, t[ks][4].y, t[ks][5].x, t[ks][5].y}, bb);
          SB();
        SEND;
        SFOR(j, 6) dsr<j * 1024>(wq[j], bq); SEND;
        asm volatile("s_nop 1");
        SB();
        const unsigned e0 = pk2(d2[0], d2[1]), e1 = pk2(d2[2], d2[3]);
        auto s0 = __builtin_amdgcn_permlane16_swap(e0, e0, false, false);
        auto s1 = __builtin_amdgcn_permlane16_swap(e1, e1, false, false);
        const unsigned ks_ = WS_KV + (((l >> 1) * 16 + cg) * 16 + rg) * 4096;
        stg_sc1(rs, vS0, ks_, v4u{pk2(d0[0], d0[1]), pk2(d0[2], d0[3]), pk2(d1[0], d1[1]), pk2(d1[2], d1[3])});
        stg_sc1(rs, vS1, ks_, v4u{e0, e1, s0[1], s1[1]});
      }
      STAMP(4 + 12 * l);
      const unsigned ks_ = WS_KV + ((l >> 1) * 16 + cg) * 65536 + (w < 3 ? w * 1024 : 11 * 256);
      const unsigned kvv = vKv;
      v4u kp[16];
      unsigned kpr;
      const unsigned kps = WS_KV + ((l >> 1) * 16 + cg) * 65536, kpv = vKp;
      SFOR(pp, 20) SB(); P2PAIR(pp, (2 * pp + 6 <= 41 ? 4 : 39 - 2 * pp)); if constexpr (pp == 16) wo24 = ldg_sc1(rs, l16, wos);
        if constexpr (pp == 17) probe_issue(kpr, rs, kpv, kps);
        if constexpr (pp >= 18 && 2 * (pp - 18) < KPS) { constexpr int r = 2 * (pp - 18); kp[r] = ldg_sc1(rs, kvv, ks_ + (r & 3) * 16384 + (r >> 2) * 256); kp[r + 1] = ldg_sc1(rs, kvv, ks_ + ((r + 1) & 3) * 16384 + (r >> 2) * 256); }
      SEND;
      SB();
      lgkm<0>();
      mf32v<false>(a0, wq[40 % 6], mfr);
#pragma unroll
      for (int r = (KPS > 4 ? 4 : KPS); r < 16; ++r) kp[r] = ldg_sc1(rs, kvv + (r >> 2) * 256, ks_ + (r & 3) * 16384);
      FENCE(); SB();
      STAMP(5 + 12 * l);
      unsigned ph[20];
      float eps;
      { float qq[20];
#pragma unroll
        for (int i = 0; i < 4; ++i) qq[16 + i] = a3[8 + i];
#pragma unroll
        for (int i = 0; i < 16; ++i) qq[i] = a0[i];
        eps = 1e-6f * phi_map<false>(qq, sl, 1.f, ph, [&](auto i_) __attribute__((always_inline)) {
          if constexpr (decltype(i_)::v == 4) {
            SB();
            probe_wait<16>(kpr);
            if (__builtin_expect(any_sent(kpr), 0)) {
              CNT(3);
              poll_probe<100>(rs, kpv, kps, tm);
#pragma unroll
              for (int r = 0; r < 16; ++r) kp[r] = ldg_sc1(rs, kvv + (r >> 2) * 256, ks_ + (r & 3) * 16384);
            }
            FENCE(); SB();
          } });
      }
      v4u f[10], sel;
      { const int r = lane & 15;
#pragma unroll
        for (int j = 0; j < 4; ++j) sel[j] = r == 2 * j ? 0x00003F80u : r == 2 * j + 1 ? 0x3F800000u : 0u;
      }
#pragma unroll
      for (int i = 0; i < 20; i += 4) asm volatile("" : "+v"(ph[i]), "+v"(ph[i + 1]), "+v"(ph[i + 2]), "+v"(ph[i + 3]));
      asm volatile("s_nop 1" : "+v"(sel));
      SB();
      f32x4 sm[4];
      for (unsigned sp = 0;;) {
        SFOR(r, 16) mf16v<(r & 3) == 0>(sm[r >> 2], sel, kp[r]); SEND;
        asm volatile("s_nop 7");
        SB();
        if (__builtin_expect(!any_nan((sm[0][0] + sm[1][0]) + (sm[2][0] + sm[3][0])), 1)) break;
        if (give_up(sp, tm)) break;
        CNT(1);
        FENCE();
#pragma unroll
        for (int r = 0; r < 16; ++r) kp[r] = ldg_sc1(rs, kvv + (r >> 2) * 256, ks_ + (r & 3) * 16384);
      }
      if (lane < 32) {
        const unsigned st0 = w < 3 ? 400 : 800, st1 = w < 2 ? 400 : 800;
#pragma unroll
        for (int j = 0; j < 4; ++j) *(LAS v2u*)(L + (j < 2 ? vK0 + j * st0 : vK1 + (j - 2) * st1)) = v2u{pk2(sm[j][0], sm[j][1]), pk2(sm[j][2], sm[j][3])};
      }
      SFOR(i, ADM - 10) DM(10 + i); SEND;
      BAR();
#pragma unroll
      for (int s = 0; s < 5; ++s) { f[2 * s + 1] = lds128(L, vF1 + s * 400); f[2 * s] = lds128(L, vF0 + s * 400); }
      f32x16 n0, n1;
      SFOR(s, 5)
        const v4u bb = {ph[4 * s], ph[4 * s + 1], ph[4 * s + 2], ph[4 * s + 3]};
        mf32v<s == 0>(n1, f[2 * s + 1], bb); mf32v<s == 0>(n0, f[2 * s], bb);
      SEND;
      MFMA_SETTLE();
      SB();
      const float inv = __builtin_amdgcn_rcpf(n1[4] + eps);
#pragma unroll
      for (int i = 0; i < 2; ++i) { const f32x2 t = pkmul(f32x2{n1[2 * i], n1[2 * i + 1]}, f32x2{inv, inv}); o[16 + 2 * i] = t[0]; o[17 + 2 * i] = t[1]; }
#pragma unroll
      for (int i = 0; i < 8; ++i) { const f32x2 t = pkmul(f32x2{n0[2 * i], n0[2 * i + 1]}, f32x2{inv, inv}); o[2 * i] = t[0]; o[2 * i + 1] = t[1]; }
    }
    {
      RELANE();
      asm volatile("" : "+v"(vOv), "+v"(vOp), "+v"(vXv));
      asm volatile("" : "+v"(wo24));
      const unsigned os = cOp;
      stg_sc1(rs, vOv, os, v4u{pk2(o[0], o[1]), pk2(o[2], o[3]), pk2(o[4], o[5]), pk2(o[6], o[7])});
      stg_sc1(rs, vOv + 1024, os, v4u{pk2(o[8], o[9]), pk2(o[10], o[11]), pk2(o[12], o[13]), pk2(o[14], o[15])});
      const unsigned e0 = pk2(o[16], o[17]), e1 = pk2(o[18], o[19]);
      auto s0 = __builtin_amdgcn_permlane32_swap(e0, e0, false, false);
      auto s1 = __builtin_amdgcn_permlane32_swap(e1, e1, false, false);
      if (h == 0) stg_sc1(rs, vOv + 2048, os + 2048, v4u{e0, e1, s0[1], s1[1]});
      STAMP(6 + 12 * l);
      if constexpr (DLY_O > 0) __builtin_amdgcn_s_sleep(DLY_O);
      STAMP(7 + 12 * l);
      const unsigned l16b = l16 + 1024;
#define LDO(i, s) do { if constexpr ((s) == 0) lda<20 * (i), 0, true>(rs, l16, osb[i]); else lda<20 * (i) + 4 * (s), ((s) - 1) * 1024, true>(rs, l16b, osb[i]); } while (0)
      unsigned opr;
      const unsigned opv = vOp;
      probe_issue(opr, rs, opv, ops);
      SFOR(s, 5) LDO(0, s); if constexpr (s == 1) BARE(); SEND;
      BARE();
      SFOR(s, 5) LDO(1, s); SEND;
      SB();
      unsigned rwa[3], rra = L_RED + w * 15360 + l16;
      f32x2 rsum[10];
#pragma unroll
      for (int i = 0; i < 3; ++i) rwa[i] = L_RED + l16 + (((w + 1 + i) & 3) * 3 + i) * 5120;
#pragma unroll
      for (int i = 0; i < 10; ++i) rsum[i] = f32x2{lo16(xr[i]), hi16(xr[i])};
      asm volatile("" : "+v"(rsum[0]), "+v"(rsum[1]), "+v"(rsum[2]), "+v"(rsum[3]), "+v"(rsum[4]), "+v"(rsum[5]), "+v"(rsum[6]), "+v"(rsum[7]), "+v"(rsum[8]), "+v"(rsum[9]));
      asm volatile("" : "+v"(rwa[0]), "+v"(rwa[1]), "+v"(rwa[2]), "+v"(rra));
      f32x4 acc[2][5], part[15];
      probe_wait<10>(opr);
      if (__builtin_expect(any_sent(opr), 0)) {
        CNT(4);
        poll_probe<100>(rs, opv, ops, tm);
        SFOR(s, 5) LDO(0, s); SEND;
        SFOR(s, 5) LDO(1, s); SEND;
      }
      auto redo = [&](auto i_) __attribute__((always_inline)) {
        constexpr int i = decltype(i_)::v;
        unsigned sp = 0;
        do {
          CNT(1);
          SFOR(s, 5) LDO(i, s); SEND;
          vmc<0>();
          SFOR(j, 25) if constexpr (j < 24) mf16aa<(j < 5), 160 + 4 * j, 20 * i + 4 * (j / 5)>(acc[i & 1][j % 5]); else mf16va<20 * i + 16>(acc[i & 1][4], wo24); SEND;
          asm volatile("s_nop 7");
          SB();
        } while (any_nan(acc[i & 1][4][0]) && !give_up(sp, tm));
      };
      constexpr int NDO = NDM2 + ADM + ODM;
      static_assert(NDO + GDM <= 22 && NDM2 + ADM + ODM <= 17 && GDM <= 10 && ADM <= 14, "");
      SFOR(i, 4)
        SFOR(j, 25)
          constexpr int s = j / 5, ft = j % 5, WS0 = 4, wstep = 4;
          SB();
          constexpr int gi = 5 * i + s;
          if constexpr (ft == 0) vmc<(gi < 10 ? 9 : 19 - gi) + (gi > 20 - GDM ? gi - (20 - GDM) : 0)>();
          if constexpr (j < 24) mf16aa<s == 0, 160 + 4 * j, 20 * i + 4 * s>(acc[i & 1][ft]); else mf16va<20 * i + 16>(acc[i & 1][ft], wo24);
          if constexpr (i == 0 && j == 0) STAMP(8 + 12 * l);
          if constexpr (ft == 2) {
            if constexpr (i < 2) LDO(i + 2, s);
            else if constexpr (gi >= 20 - GDM) dmaw<NDO + gi - (20 - GDM)>(rw, l16, wb1k);
          }
          if constexpr (i > 0 && j == WS0 - 1) { asm volatile("s_nop 1"); SB(); if (__builtin_expect(any_nan(acc[(i - 1) & 1][4][0]), 0)) redo(IC<i - 1>{}); }
          if constexpr (i > 0 && j >= WS0 && j <= WS0 + 4 * wstep && (j - WS0) % wstep == 0)
            DSW(rwa[i > 0 ? i - 1 : 0], ((j - WS0) / wstep) * 1024, acc[(i - 1) & 1][(j - WS0) / wstep]);
          if constexpr (i == 3 && j == 23) { SB(); BAR(); }
          if constexpr (i == 3 && j >= 23) {
#pragma unroll
            for (int q = (j - 23) * 8; q < (j - 23) * 8 + 8 && q < 15; ++q) part[q] = __builtin_bit_cast(f32x4, lds128(L, rra + (q % 3) * 5120 + (q / 3) * 1024));
          }
        SEND;
      SEND;
      SB();
      STAMP(9 + 12 * l);
#pragma unroll
      for (int ft = 0; ft < 5; ++ft)
#pragma unroll
        for (int q = 0; q < 3; ++q) { rsum[2 * ft] = pkadd(rsum[2 * ft], f32x2{part[ft * 3 + q][0], part[ft * 3 + q][1]}); rsum[2 * ft + 1] = pkadd(rsum[2 * ft + 1], f32x2{part[ft * 3 + q][2], part[ft * 3 + q][3]}); }
      asm volatile("" : "+v"(rsum[8]), "+v"(rsum[9]));
      SB();
      if (__builtin_expect(any_nan(acc[1][4][0]), 0)) redo(IC<3>{});
      auto fin = [&](int ft) __attribute__((always_inline)) {
        const f32x2 lo = pkadd(rsum[2 * ft], f32x2{acc[1][ft][0], acc[1][ft][1]}), hi = pkadd(rsum[2 * ft + 1], f32x2{acc[1][ft][2], acc[1][ft][3]});
        xr[2 * ft] = pk2(lo[0], lo[1]); xr[2 * ft + 1] = pk2(hi[0], hi[1]);
      };
      fin(0); fin(1);
      if (l < 3) {
        stg_sc1(rs, vXv, cXp, v4u{xr[0], xr[1], xr[2], xr[3]});
        SB();
        fin(2); fin(3);
        stg_sc1(rs, vXv + 512, cXp, v4u{xr[4], xr[5], xr[6], xr[7]});
        SB();
        fin(4);
        { auto s0 = __builtin_amdgcn_permlane16_swap(xr[8], xr[8], false, false);
          auto s1 = __builtin_amdgcn_permlane16_swap(xr[9], xr[9], false, false);
          stg_sc1(rs, (lane & 16) ? 0x80000000u : (vXv & 0x1f0) + (h << 9), cXp + 4096, v4u{xr[8], xr[9], s0[1], s1[1]}); }
        SB();
        { float sa = 0.f, sq = 0.f;
#pragma unroll
          for (int i = 0; i < 10; ++i) { sa = dot2(xr[i], 0x3F803F80u, sa); sq = dot2(xr[i], xr[i], sq); }
          { auto a = __builtin_amdgcn_permlane16_swap(f2u(sa), f2u(sa), false, false); sa = u2f(a[0]) + u2f(a[1]);
            auto c = __builtin_amdgcn_permlane16_swap(f2u(sq), f2u(sq), false, false); sq = u2f(c[0]) + u2f(c[1]); }
          { auto a = __builtin_amdgcn_permlane32_swap(f2u(sa), f2u(sa), false, false); sa = u2f(a[0]) + u2f(a[1]);
            auto c = __builtin_amdgcn_permlane32_swap(f2u(sq), f2u(sq), false, false); sq = u2f(c[0]) + u2f(c[1]); }
          __builtin_amdgcn_raw_buffer_store_b64(v2u{f2u(sa), f2u(sq)}, rs, lane < 16 ? lane * 8 : 0x80000000u, cSp, SC1); }
        STAMP(10 + 12 * l);
        if (l == 0) load_cs(L, rs, 1, cg, tid);
        if constexpr (DLY_X > 0) __builtin_amdgcn_s_sleep(DLY_X);
      } else {
        fin(2); fin(3); fin(4);
        GAS v2u* dst = (GAS v2u*)(p.out + (size_t)(rg * 128 + half * 64 + w * 16 + (lane & 15)) * 640 + c8 * 80 + (lane >> 4) * 20);
#pragma unroll
        for (int i = 0; i < 5; ++i) dst[i] = v2u{xr[2 * i], xr[2 * i + 1]};
        if (b == 0 && tid == 0) __builtin_amdgcn_raw_buffer_store_b32(tm.epoch, rs, 0, WS_CTRL + 128, SC1);
      }
      STAMP(11 + 12 * l);
    }
  }
}

extern "C" void kernel_launch(void* const* d_in, const int* in_sizes, int n_in, void* d_out, int out_size, void* d_ws, size_t ws_size, hipStream_t stream) {
  Params p;
  p.x = (const u16*)d_in[0]; p.wqA = (const u16*)d_in[1]; p.woA = (const u16*)d_in[2];
  p.wqB = (const u16*)d_in[4]; p.wphi = (const u16*)d_in[5]; p.woB = (const u16*)d_in[6];
  p.out = (u16*)d_out; p.ws = (unsigned char*)d_ws;
  (void)hipFuncSetAttribute((const void*)k_main, hipFuncAttributeMaxDynamicSharedMemorySize, LDS_ALLOC);
  k_main<<<256, 256, LDS_ALLOC, stream>>>(p);
}
```

```cpp
#include <hip/hip_runtime.h>
#include <stdint.h>

typedef unsigned short u16;
typedef unsigned v4u __attribute__((ext_vector_type(4)));
typedef unsigned v2u __attribute__((ext_vector_type(2)));
typedef float f32x2 __attribute__((ext_vector_type(2)));
typedef float f32x4 __attribute__((ext_vector_type(4)));
typedef float f32x16 __attribute__((ext_vector_type(16)));
typedef __bf16 bf16x8 __attribute__((ext_vector_type(8)));
typedef __bf16 bf16x2 __attribute__((ext_vector_type(2)));
typedef short s16x4 __attribute__((ext_vector_type(4)));
#define LAS __attribute__((address_space(3)))
#define GAS __attribute__((address_space(1)))
typedef __amdgpu_buffer_rsrc_t rsrc_t;
#define DEV __device__ __forceinline__

#define DSC1 " sc1"
constexpr unsigned WQ_SZ = 153600, WO_SZ = 102400, XB = 2621440, STB = 131072;
constexpr unsigned WS_CTRL = 0;
constexpr unsigned WS_FLAG = 4096;
constexpr unsigned WS_WQ = WS_FLAG + 4 * 256 * 128;
constexpr unsigned WS_WO = WS_WQ + 32 * WQ_SZ;
constexpr unsigned WS_CSP = WS_WO + 16 * WO_SZ;
constexpr unsigned WS_ST0 = WS_CSP + 2 * 16 * 16 * 512;
constexpr unsigned WS_ST1 = WS_ST0 + STB;
constexpr unsigned WS_X = WS_ST0 + 4 * STB;
constexpr unsigned WS_O = WS_X + 4 * XB;
constexpr unsigned WS_KV = WS_O + 4 * XB;
constexpr unsigned WS_HALO = WS_KV + 2 * 256 * 4096;
constexpr unsigned WS_END = WS_HALO + 2 * 256 * 4096;
constexpr unsigned WS_STAMP = 40u << 20;
constexpr unsigned WS_TOTAL = WS_STAMP + 256 * 4 * 64 * 8;
constexpr unsigned KSTEP = 2816, L_P0 = 112640, L_KVA = 153600, L_CS = L_KVA + 4096;
constexpr unsigned L_SCR = 73216;
constexpr unsigned L_K = L_SCR, L_V = L_SCR + 2 * 144 * 48;
constexpr unsigned L_RED = 90112;
constexpr unsigned LDS_BYTES = L_CS + 4096;
constexpr unsigned SENT = 0xFFFFFFFFu;
constexpr unsigned CODE_BYTES = 24576;
constexpr int SC1 = 16;
constexpr float LOG2E = 1.4426950408889634f;

struct Params { const u16 *x, *wqA, *woA, *wqB, *wphi, *woB; u16* out; unsigned char* ws; };

DEV unsigned pk2(float a, float b) { bf16x2 v = {(__bf16)a, (__bf16)b}; return __builtin_bit_cast(unsigned, v); }
DEV float lo16(unsigned u) { return __builtin_bit_cast(float, u << 16); }
DEV float hi16(unsigned u) { return __builtin_bit_cast(float, u & 0xffff0000u); }
DEV unsigned f2u(float f) { return __builtin_bit_cast(unsigned, f); }
DEV float u2f(unsigned u) { return __builtin_bit_cast(float, u); }
DEV unsigned umax(unsigned a, unsigned b) { return __builtin_elementwise_max(a, b); }
DEV unsigned umin(unsigned a, unsigned b) { return __builtin_elementwise_min(a, b); }
DEV float max3f(float a, float b, float c) { float d; asm("v_max3_f32 %0, %1, %2, %3" : "=v"(d) : "v"(a), "v"(b), "v"(c)); return d; }
DEV float min3f(float a, float b, float c) { float d; asm("v_min3_f32 %0, %1, %2, %3" : "=v"(d) : "v"(a), "v"(b), "v"(c)); return d; }
DEV float dot2(unsigned a, unsigned b, float c) { return __builtin_amdgcn_fdot2_f32_bf16(__builtin_bit_cast(bf16x2, a), __builtin_bit_cast(bf16x2, b), c, false); }
DEV f32x2 pkfma(f32x2 a, f32x2 b, f32x2 c) { return __builtin_elementwise_fma(a, b, c); }
DEV f32x2 pkmul(f32x2 a, f32x2 b) { return a * b; }
DEV f32x2 pkadd(f32x2 a, f32x2 b) { return a + b; }
DEV f32x16 mfma32(v4u a, v4u b, f32x16 c) { return __builtin_amdgcn_mfma_f32_32x32x16_bf16(__builtin_bit_cast(bf16x8, a), __builtin_bit_cast(bf16x8, b), c, 0, 0, 0); }
DEV f32x4 mfma16(v4u a, v4u b, f32x4 c) { return __builtin_amdgcn_mfma_f32_16x16x32_bf16(__builtin_bit_cast(bf16x8, a), __builtin_bit_cast(bf16x8, b), c, 0, 0, 0); }
DEV v4u lds128(LAS unsigned char* L, unsigned a) { return *(LAS v4u*)(L + a); }
DEV v2u lds64(LAS unsigned char* L, unsigned a) { return *(LAS v2u*)(L + a); }
DEV void sts128(LAS unsigned char* L, unsigned a, v4u v) { *(LAS v4u*)(L + a) = v; }
DEV v2u ldstr(LAS unsigned char* L, unsigned a) { return __builtin_bit_cast(v2u, __builtin_amdgcn_ds_read_tr16_b64_v4i16((LAS s16x4*)(L + a))); }
DEV v4u ldg(rsrc_t rs, unsigned voff, unsigned soff) { return __builtin_amdgcn_raw_buffer_load_b128(rs, voff, soff, 0); }
DEV v4u ldg_sc1(rsrc_t rs, unsigned voff, unsigned soff) { return __builtin_amdgcn_raw_buffer_load_b128(rs, voff, soff, SC1); }
DEV v2u ldg64_sc1(rsrc_t rs, unsigned voff, unsigned soff) { return __builtin_amdgcn_raw_buffer_load_b64(rs, voff, soff, SC1); }
DEV void stg_sc1(rsrc_t rs, unsigned voff, unsigned soff, v4u v) { __builtin_amdgcn_raw_buffer_store_b128(v, rs, voff, soff, SC1); }
DEV unsigned mx4(unsigned m, v4u d) { return umax(umax(umax(m, d.x), d.y), umax(d.z, d.w)); }
DEV unsigned max3u(unsigned a, unsigned b, unsigned c) { return umax(umax(a, b), c); }
DEV bool any_sent(unsigned m) { return __builtin_amdgcn_ballot_w64(m == SENT) != 0; }
#define BAR() asm volatile("s_waitcnt lgkmcnt(0)\n\ts_barrier" ::: "memory")
#define DRAIN() asm volatile("s_waitcnt vmcnt(0)" ::: "memory")
#define FENCE() asm volatile("" ::: "memory")
#define SB() __builtin_amdgcn_sched_barrier(0)
DEV void dma4(rsrc_t rs, unsigned voff, unsigned soff, unsigned ldsa) {
  asm volatile("s_mov_b32 m0, %0\n\ts_nop 0\n\tbuffer_load_dwordx4 %1, %2, %3 offen sc1 lds\n\tbuffer_load_dwordx4 %1, %2, %3 offen offset:1024 sc1 lds\n\t"
               "buffer_load_dwordx4 %1, %2, %3 offen offset:2048 sc1 lds\n\tbuffer_load_dwordx4 %1, %2, %3 offen offset:3072 sc1 lds"
               :: "s"(__builtin_amdgcn_readfirstlane(ldsa)), "v"(voff), "s"(rs), "s"(__builtin_amdgcn_readfirstlane(soff)) : "memory");
}
DEV void dma1(rsrc_t rs, unsigned voff, unsigned soff, unsigned ldsa) {
  asm volatile("s_mov_b32 m0, %0\n\ts_nop 0\n\tbuffer_load_dwordx4 %1, %2, %3 offen sc1 lds"
               :: "s"(__builtin_amdgcn_readfirstlane(ldsa)), "v"(voff), "s"(rs), "s"(__builtin_amdgcn_readfirstlane(soff)) : "memory");
}
DEV void dma_range(rsrc_t rs, unsigned l16, unsigned wsrc, int w, int k0, int k1) {
  int k = k0 + 4 * w;
  for (; k + 4 <= k1; k += 16) dma4(rs, l16, wsrc + k * 1024, k * 1024);
  for (; k < k1; ++k) dma1(rs, l16, wsrc + k * 1024, k * 1024);
}
struct Tmo { unsigned char* ws; unsigned epoch; };
DEV bool give_up(unsigned& sp, const Tmo& tm) {
  ++sp;
  if (sp > 32) __builtin_amdgcn_s_sleep(8);
  if ((sp & 63u) == 0u) {
    GAS unsigned* t = (GAS unsigned*)(tm.ws + WS_CTRL);
    if (sp >= (1u << 16)) { __hip_atomic_store(t, tm.epoch, __ATOMIC_RELAXED, __HIP_MEMORY_SCOPE_AGENT); return true; }
    if (__builtin_amdgcn_readfirstlane(__hip_atomic_load(t, __ATOMIC_RELAXED, __HIP_MEMORY_SCOPE_AGENT)) == tm.epoch) return true;
  }
  return false;
}
#define MAPV 1
#define XW 10
DEV int map_rg(int b) { return ((b & 7) >> 1) * 4 + (b >> 6); }
DEV int map_cg(int b) { return (b & 1) * 8 + ((b >> 3) & 7); }
DEV int o_half(int cg) { return (cg >> 2) & 1; }
DEV int o_c8(int cg) { return (cg >> 3) * 4 + (cg & 3); }
DEV int xfeat(int c, int j) { int c8 = c / 10, l = c % 10; return 80 * c8 + (l < 8 ? 20 * (l >> 1) + 8 * (l & 1) + j : 40 * (l - 8) + 20 * (j >> 2) + 16 + (j & 3)); }
DEV int ocol(int cgp, int s, int j) { return 40 * cgp + (s < 4 ? 20 * (s >> 1) + 8 * (s & 1) + j : 20 * (j >> 2) + 16 + (j & 3)); }
DEV void pin_s(unsigned& v) { asm volatile("" : "+s"(v)); }
DEV int fresh_lane() { int l; asm volatile("v_mbcnt_lo_u32_b32 %0, -1, 0\n\tv_mbcnt_hi_u32_b32 %0, -1, %0" : "=v"(l)); return l; }
#define RELANE() const int lane = fresh_lane(), h = lane >> 5, tok = lane & 31, tid = w * 64 + lane; const unsigned l16 = lane * 16; (void)h; (void)tok; (void)tid; (void)l16
#define STAMP(i) do {} while (0)
#define LDS_ALLOC LDS_BYTES
#define CNT(i) do {} while (0)

DEV void publish_x(rsrc_t rs, unsigned xbuf, unsigned stbuf, int rg, int cg, int w, int lane, const unsigned (&xr)[10]) {
  const int g = lane >> 4, tok = lane & 15, half = o_half(cg), c8 = o_c8(cg);
  const unsigned sb = xbuf + ((rg * 4 + half * 2 + (w >> 1)) * 40 + 5 * c8) * 1024;
  const unsigned vb = ((w & 1) * 16 + tok) * 16;
  stg_sc1(rs, vb + g * 1024, sb, v4u{xr[0], xr[1], xr[2], xr[3]});
  stg_sc1(rs, vb + g * 1024 + 512, sb, v4u{xr[4], xr[5], xr[6], xr[7]});
  auto s0 = __builtin_amdgcn_permlane16_swap(xr[8], xr[8], false, false);
  auto s1 = __builtin_amdgcn_permlane16_swap(xr[9], xr[9], false, false);
  if (!(g & 1)) stg_sc1(rs, vb + 4096 + (g >> 1) * 512, sb, v4u{xr[8], xr[9], s0[1], s1[1]});
  SB();
  float sa = 0.f, sq = 0.f;
#pragma unroll
  for (int i = 0; i < 10; ++i) { sa = dot2(xr[i], 0x3F803F80u, sa); sq = dot2(xr[i], xr[i], sq); }
  { auto a = __builtin_amdgcn_permlane16_swap(f2u(sa), f2u(sa), false, false); sa = u2f(a[0]) + u2f(a[1]);
    auto b = __builtin_amdgcn_permlane16_swap(f2u(sq), f2u(sq), false, false); sq = u2f(b[0]) + u2f(b[1]); }
  { auto a = __builtin_amdgcn_permlane32_swap(f2u(sa), f2u(sa), false, false); sa = u2f(a[0]) + u2f(a[1]);
    auto b = __builtin_amdgcn_permlane32_swap(f2u(sq), f2u(sq), false, false); sq = u2f(b[0]) + u2f(b[1]); }
  if (g == 0) __builtin_amdgcn_raw_buffer_store_b64(v2u{f2u(sa), f2u(sq)}, rs, tok * 8, stbuf + ((rg * 8 + c8) * 128 + half * 64 + w * 16) * 8, SC1);
}
DEV void load_x_in(const u16* x, int rg, int cg, int w, int lane, unsigned (&xr)[10]) {
  const int g = lane >> 4, tok = lane & 15;
  const GAS v2u* src = (const GAS v2u*)(x + (size_t)(rg * 128 + o_half(cg) * 64 + w * 16 + tok) * 640 + o_c8(cg) * 80 + g * 20);
#pragma unroll
  for (int i = 0; i < 5; ++i) { v2u t = src[i]; xr[2 * i] = t.x; xr[2 * i + 1] = t.y; }
}

DEV void poison_x(rsrc_t rs, unsigned xbuf, unsigned stbuf, int rg, int cg, int w, int lane) {
  const int g = lane >> 4, tok = lane & 15, half = o_half(cg), c8 = o_c8(cg);
  const unsigned sb = xbuf + ((rg * 4 + half * 2 + (w >> 1)) * 40 + 5 * c8) * 1024, vb = ((w & 1) * 16 + tok) * 16;
  const v4u P = {SENT, SENT, SENT, SENT};
  stg_sc1(rs, vb + g * 1024, sb, P); stg_sc1(rs, vb + g * 1024 + 512, sb, P); stg_sc1(rs, vb + 4096 + (g >> 1) * 512, sb, P);
  __builtin_amdgcn_raw_buffer_store_b64(v2u{SENT, SENT}, rs, tok * 8, stbuf + ((rg * 8 + c8) * 128 + half * 64 + w * 16) * 8, SC1);
}
DEV void poison_o(rsrc_t rs, unsigned obuf, int rg, int cg, int w, int lane) {
  const int h = lane >> 5, tok = lane & 31;
  const unsigned os = obuf + ((rg * 8 + 2 * w) * 4 + (cg >> 2)) * 5120 + (cg & 3) * 256, ov = (tok >> 4) * 20480 + (tok & 15) * 16;
  const v4u P = {SENT, SENT, SENT, SENT};
  stg_sc1(rs, ov + h * 2048, os, P); stg_sc1(rs, ov + h * 2048 + 1024, os, P); stg_sc1(rs, ov + 4096, os, P);
}
DEV void wait_flags(rsrc_t rs, unsigned voff, unsigned soff, const Tmo& tm) {
  unsigned sp = 0, v;
  do { FENCE(); v = __builtin_amdgcn_raw_buffer_load_b32(rs, voff, soff, SC1); } while (__builtin_amdgcn_ballot_w64(v != tm.epoch) != 0 && !give_up(sp, tm));
}
DEV void build_wq_read(LAS unsigned char* L, unsigned stage, int w, int lane, v4u (&fr)[3]) {
  const int gam = lane >> 4, q = (lane >> 2) & 3, pp = lane & 3, a = 2 * (gam & 1) + (pp >> 1), h = pp & 1;
  const int eoff = (w < 3) ? 40 * w + 20 * h + 4 * a : (a == 0 ? 40 : a == 1 ? 80 : 0) + 20 * h + 16;
#pragma unroll
  for (int it = 0; it < 3; ++it) {
    const int kap = 2 * it + (gam >> 1), kc = kap < 4 ? kap : 4;
    const unsigned ad = stage + (kc * 8 + q) * 256 + eoff * 2;
    const v2u t0 = ldstr(L, ad), t1 = ldstr(L, ad + 1024);
    fr[it] = v4u{t0.x, t0.y, t1.x, t1.y};
  }
}
DEV float sum8(v4u v, float c) { c = dot2(v.x, 0x3F803F80u, c); c = dot2(v.y, 0x3F803F80u, c); c = dot2(v.z, 0x3F803F80u, c); return dot2(v.w, 0x3F803F80u, c); }
DEV void build_wq_store(rsrc_t rs, int m, int rg, int cg, int w, int lane, int r0, int r1, const v4u (&fr)[3]) {
  const int row = lane & 31, hi = lane >> 5;
  const unsigned tb = w == 0 ? L_P0 : (w - 1) * 1024, ts = w == 0 ? 1024 : KSTEP, th = w == 3 ? 384 : 512;
  const bool in = row >= r0 && row < r1;
#pragma unroll
  for (int it = 0; it < 3; ++it) {
    const int c = 5 * rg + 2 * it + hi;
    stg_sc1(rs, (in && (it < 2 || !hi)) ? (c >> 1) * ts + (c & 1) * th + row * 16 : 0x80000000u, WS_WQ + (m * 16 + cg) * WQ_SZ + tb, fr[it]);
  }
  float cs = sum8(fr[1], sum8(fr[0], 0.f));
  cs = hi ? cs : sum8(fr[2], cs);
  auto t = __builtin_amdgcn_permlane32_swap(f2u(cs), f2u(cs), false, false);
  __builtin_amdgcn_raw_buffer_store_b32(f2u(u2f(t[0]) + u2f(t[1])), rs, (in && !hi) ? row * 4 : 0x80000000u, WS_CSP + ((m * 16 + cg) * 16 + rg) * 512 + w * 128, SC1);
}
DEV void load_cs(LAS unsigned char* L, rsrc_t rs, int m, int cg, int tid) {
  {
    float cs = 0.f;
#pragma unroll
    for (int r = 0; r < 16; ++r) cs += u2f(__builtin_amdgcn_raw_buffer_load_b32(rs, tid * 4 + (r & 7) * 512, WS_CSP + (m * 16 + cg) * 8192 + (r >> 3) * 4096, SC1));
    const unsigned ch = pk2(cs, 0.f) & 0xffffu, cl = pk2(cs - lo16(ch), 0.f) & 0xffffu;
    sts128(L, L_CS + m * 2048 + tid * 16, v4u{ch | (cl << 16), ch, 0u, 0u});
  }
}

DEV void probe_issue(unsigned& d, rsrc_t rs, unsigned voff, unsigned soff) {
  asm volatile("buffer_load_dword %0, %1, %2, %3 offen sc1" : "=v"(d) : "v"(voff), "s"(rs), "s"(soff) : "memory");
}
template <int N> DEV void probe_wait(unsigned& d) { asm volatile("s_waitcnt vmcnt(%1)" : "+v"(d) : "i"(N) : "memory"); }
#define PSL 2
template <int R> DEV unsigned probe_turn(rsrc_t rs, unsigned voff, unsigned soff) {
  unsigned v;
  asm volatile("buffer_load_dword a%c4, %1, %2, %3 offen sc1\n\ts_waitcnt vmcnt(1)\n\tv_accvgpr_read_b32 %0, a%c5" : "=v"(v) : "v"(voff), "s"(rs), "s"(soff), "i"(R), "i"(R ^ 1) : "memory");
  return v;
}
template <int R> DEV void poll_flag(rsrc_t rs, unsigned voff, unsigned soff, const Tmo& tm) {
  unsigned sp = 0;
  asm volatile("buffer_load_dword a%c3, %0, %1, %2 offen sc1" :: "v"(voff), "s"(rs), "s"(soff), "i"(R) : "memory");
  for (;;) {
    __builtin_amdgcn_s_sleep(PSL);
    if (__builtin_amdgcn_ballot_w64(probe_turn<R + 1>(rs, voff, soff) != tm.epoch) == 0) break;
    __builtin_amdgcn_s_sleep(PSL);
    if (__builtin_amdgcn_ballot_w64(probe_turn<R>(rs, voff, soff) != tm.epoch) == 0) break;
    if (give_up(sp, tm)) break;
  }
}
template <int R> DEV void poll_probe(rsrc_t rs, unsigned voff, unsigned soff, const Tmo& tm) {
  unsigned sp = 0;
  asm volatile("buffer_load_dword a%c3, %0, %1, %2 offen sc1" :: "v"(voff), "s"(rs), "s"(soff), "i"(R) : "memory");
  for (;;) {
    __builtin_amdgcn_s_sleep(PSL);
    if (!any_sent(probe_turn<R + 1>(rs, voff, soff))) break;
    __builtin_amdgcn_s_sleep(PSL);
    if (!any_sent(probe_turn<R>(rs, voff, soff))) break;
    if (give_up(sp, tm)) break;
  }
}
template <int I> struct IC { static constexpr int v = I; };
template <int I, int N, class F> DEV void sfor(F&& f) { if constexpr (I < N) { f(IC<I>{}); sfor<I + 1, N>(f); } }
#define SFOR(var, n) sfor<0, n>([&](auto var##_) __attribute__((always_inline)) { constexpr int var = decltype(var##_)::v;
#define SEND })
template <bool NORM, class F> DEV float phi_map(const float (&a)[20], float s, float post, unsigned (&ph)[20], F slot) {
  float mx = max3f(a[0], a[1], a[2]), mn = min3f(a[0], a[1], a[2]), mx2 = max3f(a[3], a[4], a[5]), mn2 = min3f(a[3], a[4], a[5]);
#pragma unroll
  for (int i = 6; i < 18; i += 4) { mx = max3f(mx, a[i], a[i + 1]); mn = min3f(mn, a[i], a[i + 1]); mx2 = max3f(mx2, a[i + 2], a[i + 3]); mn2 = min3f(mn2, a[i + 2], a[i + 3]); }
  mx = max3f(mx, a[18], a[19]); mn = min3f(mn, a[18], a[19]); mx = max3f(mx, mx2, mx2); mn = min3f(mn, mn2, mn2);
  const float bp = -s * mx, bn = s * mn, ns = -s;
  float sp = 0.f;
  SFOR(sg, 2)
    const f32x2 sc = sg ? f32x2{ns, ns} : f32x2{s, s}, bi = sg ? f32x2{bn, bn} : f32x2{bp, bp};
    f32x2 e[10];
    slot(IC<5 * sg>{});
#pragma unroll
    for (int i = 0; i < 10; ++i) e[i] = pkfma(f32x2{a[2 * i], a[2 * i + 1]}, sc, bi);
    SB();
    SFOR(i, 10)
      if constexpr (i == 3 || i == 7) slot(IC<5 * sg + 1 + (i >> 2)>{});
      e[i] = f32x2{__builtin_amdgcn_exp2f(e[i][0]), __builtin_amdgcn_exp2f(e[i][1])};
    SEND;
    f32x2 s0 = pkadd(e[0], e[1]), s1 = pkadd(e[2], e[3]), s2 = pkadd(e[4], e[5]);
    s0 = pkadd(s0, e[6]); s1 = pkadd(s1, e[7]); s2 = pkadd(s2, e[8]); s0 = pkadd(s0, e[9]);
    slot(IC<5 * sg + 3>{});
    if constexpr (!NORM && sg == 0) {
#pragma unroll
      for (int i = 0; i < 10; ++i) ph[i] = pk2(e[i][0], e[i][1]);
    }
    s0 = pkadd(pkadd(s0, s1), s2);
    const float sm = s0[0] + s0[1];
    if constexpr (NORM || sg == 1) {
      const float r = __builtin_amdgcn_rcpf(sm) * (NORM ? post : sp);
#pragma unroll
      for (int i = 0; i < 10; ++i) { f32x2 t = pkmul(e[i], f32x2{r, r}); ph[10 * sg + i] = pk2(t[0], t[1]); }
    }
    sp = sg ? sp : sm;
    slot(IC<5 * sg + 4>{});
  SEND;
  return sp;
}

#define BARE() asm volatile("s_barrier" ::: "memory")
template <int OFF> DEV void dsr(v4u& d, unsigned addr) { asm volatile("ds_read_b128 %0, %1 offset:%2" : "=v"(d) : "v"(addr), "i"(OFF)); }
template <int R, int OFF, bool COH> DEV void lda(rsrc_t rs, unsigned voff, unsigned soff) {
  if constexpr (COH) asm volatile("buffer_load_dwordx4 a[%c0:%c1], %2, %3, %4 offen offset:%5 sc1" :: "i"(R), "i"(R + 3), "v"(voff), "s"(rs), "s"(soff), "i"(OFF) : "memory");
  else asm volatile("buffer_load_dwordx4 a[%c0:%c1], %2, %3, %4 offen offset:%5" :: "i"(R), "i"(R + 3), "v"(voff), "s"(rs), "s"(soff), "i"(OFF) : "memory");
}
DEV void sbump(unsigned& so) { asm volatile("s_add_u32 %0, %0, 0x1000" : "+s"(so) :: "scc"); }
template <bool Z, int R> DEV void mf32a(f32x16& c, const v4u& a) {
  if constexpr (Z) asm volatile("v_mfma_f32_32x32x16_bf16 %0, %1, a[%c2:%c3], 0" : "=&v"(c) : "v"(a), "i"(R), "i"(R + 3));
  else asm volatile("v_mfma_f32_32x32x16_bf16 %0, %1, a[%c2:%c3], %0" : "+v"(c) : "v"(a), "i"(R), "i"(R + 3));
}
template <bool Z> DEV void mf32v(f32x16& c, const v4u& a, const v4u& b) {
  if constexpr (Z) asm volatile("v_mfma_f32_32x32x16_bf16 %0, %1, %2, 0" : "=&v"(c) : "v"(a), "v"(b));
  else asm volatile("v_mfma_f32_32x32x16_bf16 %0, %1, %2, %0" : "+v"(c) : "v"(a), "v"(b));
}
template <bool Z, int R> DEV void mf16a(f32x4& c, const v4u& b) {
  if constexpr (Z) asm volatile("v_mfma_f32_16x16x32_bf16 %0, a[%c2:%c3], %1, 0" : "=&v"(c) : "v"(b), "i"(R), "i"(R + 3));
  else asm volatile("v_mfma_f32_16x16x32_bf16 %0, a[%c2:%c3], %1, %0" : "+v"(c) : "v"(b), "i"(R), "i"(R + 3));
}
template <bool Z> DEV void mf16v(f32x4& c, const v4u& a, const v4u& b) {
  if constexpr (Z) asm volatile("v_mfma_f32_16x16x32_bf16 %0, %1, %2, 0" : "=&v"(c) : "v"(a), "v"(b));
  else asm volatile("v_mfma_f32_16x16x32_bf16 %0, %1, %2, %0" : "+v"(c) : "v"(a), "v"(b));
}
template <int N> DEV void vmc() { asm volatile("s_waitcnt vmcnt(%0)" :: "i"(N) : "memory"); }
template <int I> DEV void dmaw(rsrc_t rw, unsigned l16, unsigned wb) {
  asm volatile("s_or_b32 m0, %0, %1\n\ts_nop 0\n\tbuffer_load_dwordx4 %2, %3, m0 offen" DSC1 " lds" :: "s"(wb), "i"(I * 4096), "v"(l16), "s"(rw) : "memory", "scc");
}
template <int N> DEV void lgkm() { asm volatile("s_waitcnt lgkmcnt(%0)" :: "i"(N)); }
#define MFMA_SETTLE() asm volatile("s_nop 7\n\ts_nop 3")
DEV bool any_nan(float x) { return __builtin_amdgcn_ballot_w64(x != x) != 0; }
#define PFD 3

template <int S, int NW, int VM, int VI> DEV void p2pair(f32x16& a0, v4u& w0, v4u& w1, unsigned bq, unsigned bcs, rsrc_t r, unsigned l16, unsigned so, const v4u& pv) {
#define P2_M0 "s_waitcnt lgkmcnt(%c[nw])\n\tv_mfma_f32_32x32x16_bf16 %[c], %[w0], a[%c[r0]:%c[r1]], "
#define P2_R0 "\n\tds_read_b128 %[w0], %[a0] offset:%c[o0]\n\t"
#define P2_M1 "v_mfma_f32_32x32x16_bf16 %[c], %[w1], a[%c[r2]:%c[r3]], %[c]"
#define P2_R1 "\n\tds_read_b128 %[w1], %[bq] offset:%c[o1]"
#define P2_IN [nw] "i"(NW), [r0] "i"(4 * S), [r1] "i"(4 * S + 3), [r2] "i"(4 * S + 4), [r3] "i"(4 * S + 7), [a0] "v"(S + 6 == 40 ? bcs : bq), [bq] "v"(bq), \
              [o0] "i"(S + 6 < 40 ? (S + 6) * 1024 : 0), [o1] "i"((S + 7) * 1024), [rs] "s"(r), [l16] "v"(l16), [so] "s"(so), [d0] "i"(VM == 1 ? 160 + 4 * VI : 0), [d1] "i"(VM == 1 ? 163 + 4 * VI : 3), \
              [vo] "i"((VI & 3) * 1024), [dm] "i"(VM == 2 ? VI * 4096 : VM == 3 ? VI : 0), [pv] "v"(pv), [p2] "v"(v2u{pv.x, pv.y})
#define P2_LD "buffer_load_dwordx4 a[%c[d0]:%c[d1]], %[l16], %[rs], %[so] offen offset:%c[vo] sc1\n\t"
#define P2_DMA "s_or_b32 m0, %[so], %[dm]\n\t"
#define P2_DMB "buffer_load_dwordx4 %[l16], %[rs], m0 offen" DSC1 " lds\n\t"
#define P2_ST "buffer_store_dwordx4 %[pv], %[l16], %[rs], %[so] offen offset:%c[dm] sc1\n\t"
#define P2_S2 "buffer_store_dwordx2 %[p2], %[l16], %[rs], %[so] offen sc1\n\t"
  if constexpr (S == 0) {
    static_assert(S != 0 || VM == 0, "");
    asm volatile(P2_M0 "0" P2_R0 P2_M1 P2_R1 : [c] "=&v"(a0), [w0] "+v"(w0), [w1] "+v"(w1) : P2_IN : "memory");
  } else if constexpr (S + 7 < 40) {
    if constexpr (VM == 0) asm volatile(P2_M0 "%[c]" P2_R0 P2_M1 P2_R1 : [c] "+v"(a0), [w0] "+v"(w0), [w1] "+v"(w1) : P2_IN : "memory");
    else if constexpr (VM == 1) asm volatile(P2_M0 "%[c]" P2_R0 P2_LD P2_M1 P2_R1 : [c] "+v"(a0), [w0] "+v"(w0), [w1] "+v"(w1) : P2_IN : "memory");
    else if constexpr (VM == 3) asm volatile(P2_M0 "%[c]" P2_R0 P2_ST P2_M1 P2_R1 : [c] "+v"(a0), [w0] "+v"(w0), [w1] "+v"(w1) : P2_IN : "memory");
    else if constexpr (VM == 4) asm volatile(P2_M0 "%[c]" P2_R0 P2_S2 P2_M1 P2_R1 : [c] "+v"(a0), [w0] "+v"(w0), [w1] "+v"(w1) : P2_IN : "memory");
    else asm volatile(P2_M0 "%[c]\n\t" P2_DMA "ds_read_b128 %[w0], %[a0] offset:%c[o0]\n\t" P2_DMB P2_M1 P2_R1 : [c] "+v"(a0), [w0] "+v"(w0), [w1] "+v"(w1) : P2_IN : "memory", "scc");
  } else if constexpr (S + 6 == 40) {
    static_assert(S + 6 != 40 || VM == 0, "");
    asm volatile(P2_M0 "%[c]" P2_R0 P2_M1 : [c] "+v"(a0), [w0] "+v"(w0), [w1] "+v"(w1) : P2_IN : "memory");
  } else {
    static_assert(VM == 0, "");
    asm volatile(P2_M0 "%[c]\n\t" P2_M1 : [c] "+v"(a0), [w0] "+v"(w0), [w1] "+v"(w1) : P2_IN : "memory");
  }
}
template <bool Z, int RA, int RB> DEV void mf16aa(f32x4& c) {
  if constexpr (Z) asm volatile("v_mfma_f32_16x16x32_bf16 %0, a[%c1:%c2], a[%c3:%c4], 0" : "=v"(c) : "i"(RA), "i"(RA + 3), "i"(RB), "i"(RB + 3));
  else asm volatile("v_mfma_f32_16x16x32_bf16 %0, a[%c1:%c2], a[%c3:%c4], %0" : "+v"(c) : "i"(RA), "i"(RA + 3), "i"(RB), "i"(RB + 3));
}
template <int RB> DEV void mf16va(f32x4& c, const v4u& a) { asm volatile("v_mfma_f32_16x16x32_bf16 %0, %1, a[%c2:%c3], %0" : "+v"(c) : "v"(a), "i"(RB), "i"(RB + 3)); }
template <bool Z, int RB> DEV void mf16vz(f32x4& c, const v4u& a) {
  if constexpr (Z) asm volatile("v_mfma_f32_16x16x32_bf16 %0, %1, a[%c2:%c3], 0" : "=&v"(c) : "v"(a), "i"(RB), "i"(RB + 3));
  else asm volatile("v_mfma_f32_16x16x32_bf16 %0, %1, a[%c2:%c3], %0" : "+v"(c) : "v"(a), "i"(RB), "i"(RB + 3));
}
#define DSW(addr, off, val) asm volatile("ds_write_b128 %0, %1 offset:%2" :: "v"(addr), "v"(val), "i"(off) : "memory")
#define ADM 12
#define ODM 0
#define GDM 10
#define DLY_O 18
#define DLY_X 9
#define HLP 17
#define DLY_OB 28
#define KFP 12
#define KCK 3
#define LW 8
#define DX1 8
#define DO1 12
#define PX 1
#define KPS 8

__global__ void __launch_bounds__(256, 1) k_main(Params p) {
  extern __shared__ __attribute__((aligned(16))) unsigned char smem[];
  LAS unsigned char* L = (LAS unsigned char*)smem;
  {
    const unsigned long long pc = __builtin_amdgcn_s_getpc();
    const unsigned t16 = __builtin_amdgcn_workitem_id_x() * 16;
    asm volatile("global_load_dwordx4 a[0:3], %0, %1\n\tglobal_load_dwordx4 a[4:7], %0, %1 offset:4080" :: "v"(t16), "s"(pc) : "memory");
    asm volatile("global_load_dwordx4 a[8:11], %0, %1" :: "v"(t16 + 8192 + ((__builtin_amdgcn_workgroup_id_x() >> 3) % 6) * 4096), "s"(pc) : "memory");
    SB();
  }
  const int b = __builtin_amdgcn_workgroup_id_x(), w = __builtin_amdgcn_readfirstlane(__builtin_amdgcn_workitem_id_x() >> 6);
  const int rg = map_rg(b), cg = map_cg(b), half = o_half(cg), c8 = o_c8(cg);
  const rsrc_t rs = __builtin_amdgcn_make_buffer_rsrc(p.ws, 0, WS_TOTAL, 0x00020000);
  asm volatile("" ::: "a255");
  unsigned vKa, vVa, vHv, vImg, vA0, vA2, vB, vS0, vS1, vK0, vK1, vF0, vF1, vKv, vKp, vOv, vOp, vXv, vSt, vB3;
  auto addresses = [&]() __attribute__((always_inline)) {
    RELANE();
    const int g = lane >> 4, hh = w >> 1, dt = w & 1, q = (lane >> 2) & 3, p4 = lane & 3, r = lane & 15;
    const unsigned r_ = h * 144 + 2 + 32 * w + tok;
    vKa = L_K + r_ * 48; vVa = L_V + r_ * 80; vHv = (w == 3 && tok >= 16) ? (h * 16 + (tok - 16)) * 16 : 0x80000000u;
    const unsigned t_ = 32 * w + tok, key = (t_ & 7) ^ (((t_ >> 3) & 1) << 2);
    vImg = L_SCR + (h * 128 + t_) * 128 + (key << 4);
    const unsigned k0 = q ^ ((g & 1) << 2), rb0 = L_SCR + (hh * 128 + 8 * g + q) * 128, cB = dt ? 7 : 5 + (p4 >> 1);
    vA0 = rb0 + ((p4 ^ k0) << 4); vA2 = rb0 + ((4 ^ k0) << 4) + (p4 & 1) * 8; vB = rb0 + ((cB ^ k0) << 4) + (p4 & 1) * 8;
    vS0 = dt ? (r < 8 ? 2560 + ((hh * 5 + g) * 8 + r) * 16 : 0x80000000u) : ((hh * 5 + g) * 16 + r) * 16;
    vS1 = g ? 0x80000000u : dt ? (r < 8 ? 2560 + ((hh * 5 + 4) * 8 + r) * 16 : 0x80000000u) : ((hh * 5 + 4) * 16 + r) * 16;
    const unsigned ty0 = L_KVA + r * 16 + g * 8, ty1 = L_KVA + (r >> 3) * 400 + 256 + (r & 7) * 16 + g * 8;
    vK0 = w < 3 ? ty0 + w * 1600 : ty1 + 800;
    vK1 = w < 2 ? ty0 + w * 1600 + 800 : ty1 + (w == 2 ? 0 : 2400);
    if (h) vK0 = vK1 = L_RED + 8192 + tid * 8;
    const bool match = ((tok >> 2) & 1) == h;
    const unsigned row = 4 * (tok >> 3) + (tok & 3);
    vF0 = L_KVA + h * 2000 + (match ? row * 16 : 384); vF1 = L_KVA + h * 2000 + (match && row < 8 ? 256 + row * 16 : 384);
    vKv = g * 4096 + r * 16;
    vKp = (lane >> 2) * 4096 + 3712 + (lane & 3) * 4;
    vOv = (tok >> 4) * 20480 + (tok & 15) * 16 + h * 2048; vOp = g * 20480 + q * 256 + p4 * 4;
    vXv = ((w & 1) * 16 + r) * 16 + g * 1024; vSt = h * 4096 + tok * 8; vB3 = 2048 + h * 384 + tok * 16;
    asm volatile("" : "+v"(vKa), "+v"(vVa), "+v"(vHv), "+v"(vImg), "+v"(vA0), "+v"(vA2), "+v"(vB), "+v"(vS0), "+v"(vS1), "+v"(vK0));
    asm volatile("" : "+v"(vK1), "+v"(vF0), "+v"(vF1), "+v"(vKv), "+v"(vKp), "+v"(vOv), "+v"(vOp), "+v"(vXv), "+v"(vSt), "+v"(vB3));
    if (tid < 10) sts128(L, L_KVA + tid * 400 + 384, v4u{0u, 0u, 0u, 0u});
  };
  unsigned xr[10];
  Tmo tm;
  { RELANE();
    const int l = 0; (void)l;
    STAMP(0);
    constexpr unsigned SG_B = L_P0 + 2048, SG_O = SG_B + 10240, SG_P = SG_O + 10240, OOR = 0x80000000u;
    const int j = (tid >> 4) & 7, cc = lane & 15, seg = (cc * 13) >> 6, g = lane >> 4;
    v4u ra[3], rb[3], ro[3], rp;
    const unsigned wcol = (seg * 640 + 40 * cg + 8 * (cc - 5 * seg)) * 2;
    unsigned wrow[3];
#pragma unroll
    for (int i = 0; i < 3; ++i) {
      const int kap = 2 * i + (w >> 1) < 4 ? 2 * i + (w >> 1) : 4, lc = 5 * (rg & 1) + kap;
      const int t8 = lc >> 3, rb0 = 80 * (rg >> 1) + (1 - t8) * (20 * (lc >> 1) + 8 * (lc & 1)) + t8 * (40 * (lc - 8) + 16);
      wrow[i] = (rb0 + j + t8 * (16 * (j >> 2))) * 3840 + wcol;
    }
    { const rsrc_t rA = __builtin_amdgcn_make_buffer_rsrc((void*)p.wqA, 0, 640 * 1920 * 2, 0x00020000);
#pragma unroll
      for (int i = 0; i < 3; ++i) ra[i] = ldg(rA, wrow[i], 0); }
    load_x_in(p.x, rg, cg, w, lane, xr);
    const unsigned ep = __builtin_amdgcn_raw_buffer_load_b32(rs, 0, WS_CTRL + 128, SC1);
    FENCE(); SB();
    { const rsrc_t rB = __builtin_amdgcn_make_buffer_rsrc((void*)p.wqB, 0, 640 * 1920 * 2, 0x00020000);
      const rsrc_t rO = __builtin_amdgcn_make_buffer_rsrc((void*)(half ? p.woB : p.woA), 0, 640 * 640 * 2, 0x00020000);
      const rsrc_t rP = __builtin_amdgcn_make_buffer_rsrc((void*)(p.wphi + cg * 800), 0, 1600, 0x00020000);
#pragma unroll
      for (int i = 0; i < 3; ++i) rb[i] = ldg(rB, wrow[i], 0);
#pragma unroll
      for (int i = 0; i < 3; ++i) { const int so = 2 * i + (w >> 1) < 4 ? 2 * i + (w >> 1) : 4, t4 = so >> 2;
        ro[i] = ldg(rO, (40 * rg + (1 - t4) * (20 * (so >> 1) + 8 * (so & 1)) + t4 * 16 + j + t4 * (16 * (j >> 2))) * 1280 + (80 * c8 + 8 * (cc < 10 ? cc : 0)) * 2, 0); }
      rp = ldg(rP, tid * 16, 0);
    FENCE(); SB();
    STAMP(53);
    const unsigned sa = tid * 16;
#pragma unroll
    for (int i = 0; i < 3; ++i) if (i < 2 || w < 2) sts128(L, sa + i * 4096, ra[i]);
    BAR();
    STAMP(54);
    { v4u fr[3];
      build_wq_read(L, 0, w, lane, fr);
      publish_x(rs, WS_X, WS_ST0, rg, cg, w, lane, xr);
      build_wq_store(rs, 0, rg, cg, w, lane, 0, w < 3 ? 32 : 24, fr); }
    tm.ws = p.ws; tm.epoch = __builtin_amdgcn_readfirstlane(ep) + 0x9E3779B1u;
    STAMP(55);
#pragma unroll
    for (int i = 0; i < 3; ++i) if (i < 2 || w < 2) { sts128(L, SG_B + sa + i * 4096, rb[i]); sts128(L, SG_O + sa + i * 4096, ro[i]); }
    if (w < 2) sts128(L, SG_P + sa, rp);
    }
    DRAIN();
    BAR();
    if (tid == 0) __builtin_amdgcn_raw_buffer_store_b32(tm.epoch, rs, 0, WS_FLAG + (cg * 16 + rg) * 128, SC1);
    STAMP(60);
    v4u fv[4], fb[3], fo[2];
    {
      const int sg = w >> 1, hw = w & 1;
      v4u bf[2], af[4];
#pragma unroll
      for (int ct = 0; ct < 2; ++ct) {
        const int q = (lane >> 2) & 3, pp = lane & 3, fcol = ct ? 16 : 4 * pp;
        const int d0 = 8 * g + q < 19 ? 8 * g + q : 19, d1 = 8 * g + 4 + q < 19 ? 8 * g + 4 + q : 19;
        const v2u t0 = ldstr(L, SG_P + hw * 800 + d0 * 40 + fcol * 2), t1 = ldstr(L, SG_P + hw * 800 + d1 * 40 + fcol * 2);
        bf[ct] = v4u{g < 3 ? t0.x : 0u, g < 3 ? t0.y : 0u, g < 2 ? t1.x : 0u, g < 2 ? t1.y : 0u};
      }
#pragma unroll
      for (int i = 0; i < 4; ++i) { const int r = (i < 2 ? 8 * (cc >> 2) : 32) + 4 * (i & 1) + (cc & 3); const unsigned a = SG_B + r * 256 + sg * 80 + hw * 40 + g * 16;
        const v2u t0 = lds64(L, a), t1 = lds64(L, a + 8); af[i] = v4u{t0.x, t0.y, t1.x, t1.y}; }
      build_wq_read(L, SG_B, w, lane, fb);
      { const int q = (lane >> 2) & 3, pp = lane & 3;
#pragma unroll
        for (int it = 0; it < 2; ++it) {
          const int pc = it * 16 + (tid >> 4), pcc = pc < 25 ? pc : 24, so = (pcc * 13) >> 6, ft = pcc - 5 * so;
          const unsigned a = SG_O + (so * 8 + q) * 256 + 40 * pp + 8 * ft;
          const v2u t0 = ldstr(L, a), t1 = ldstr(L, a + 1024);
          fo[it] = v4u{t0.x, t0.y, t1.x, t1.y};
        } }
      f32x4 d[8];
      asm volatile("s_nop 1" : "+v"(bf[0]), "+v"(bf[1]), "+v"(af[0]), "+v"(af[1]), "+v"(af[2]), "+v"(af[3]));
      SFOR(i, 8) mf16v<true>(d[i], af[i & 3], bf[i >> 2]); SEND;
      asm volatile("s_nop 7");
      SB();
#pragma unroll
      for (int i = 0; i < 4; ++i) fv[i] = v4u{pk2(d[2 * i][0], d[2 * i][1]), pk2(d[2 * i][2], d[2 * i][3]), pk2(d[2 * i + 1][0], d[2 * i + 1][1]), pk2(d[2 * i + 1][2], d[2 * i + 1][3])};
    }
    STAMP(58);
    { const int sg = w >> 1, hw = w & 1;
#pragma unroll
      for (int ct = 0; ct < 2; ++ct) {
        const int row = ct ? (sg ? 0 : 16) + 4 * hw + (cc & 3) : 8 * (cc >> 2) + 4 * hw + (cc & 3);
        const unsigned tb = ct ? 2048 : (sg ? 0 : L_P0), ts = (ct || sg) ? KSTEP : 1024, th = ct ? 384 : 512;
        const bool in = ct == 0 || cc < 4;
#pragma unroll
        for (int u = 0; u < 2; ++u) { const int c = 5 * rg + (u ? 4 : g);
          stg_sc1(rs, (in && (u == 0 || g == 0)) ? (c >> 1) * ts + (c & 1) * th + row * 16 : OOR, WS_WQ + (16 + cg) * WQ_SZ + tb, fv[2 * ct + u]); }
        float cs = sum8(fv[2 * ct], 0.f);
        cs = g ? cs : sum8(fv[2 * ct + 1], cs);
        { auto t = __builtin_amdgcn_permlane16_swap(f2u(cs), f2u(cs), false, false); cs = u2f(t[0]) + u2f(t[1]); }
        { auto t = __builtin_amdgcn_permlane32_swap(f2u(cs), f2u(cs), false, false); cs = u2f(t[0]) + u2f(t[1]); }
        __builtin_amdgcn_raw_buffer_store_b32(f2u(cs), rs, (in && g == 0) ? row * 4 : OOR, WS_CSP + ((16 + cg) * 16 + rg) * 512 + (ct ? 3 : sg) * 128, SC1);
      }
      if (w >= 2) build_wq_store(rs, 1, rg, cg, w, lane, w == 2 ? 0 : 8, w == 2 ? 32 : 16, fb);
#pragma unroll
      for (int it = 0; it < 2; ++it) { const int pc = it * 16 + (tid >> 4);
        stg_sc1(rs, pc < 25 ? pc * 1024 + cc * 16 : OOR, WS_WO + (half * 8 + c8) * WO_SZ + (rg >> 2) * 25600 + (rg & 3) * 256, fo[it]); }
    }
    poison_o(rs, WS_O, rg, cg, w, lane);
    stg_sc1(rs, l16, WS_KV + (cg * 16 + rg) * 4096 + w * 1024, v4u{SENT, SENT, SENT, SENT});
    stg_sc1(rs, l16, WS_HALO + (rg * 16 + cg) * 4096 + w * 1024, v4u{SENT, SENT, SENT, SENT});
    addresses();
    STAMP(61);
    if (w == 1) wait_flags(rs, (h == 0 ? cc * 16 + rg : cg * 16 + cc) * 128, WS_FLAG, tm);
    BAR();
    STAMP(62);
    const rsrc_t rw = __builtin_amdgcn_make_buffer_rsrc(p.ws + WS_WQ + cg * WQ_SZ, 0, WQ_SZ, 0x00020000);
    SFOR(i, 22 + LW) dmaw<i>(rw, l16, w * 1024); SEND;
    float csv[16];
#pragma unroll
    for (int r = 0; r < 16; ++r) csv[r] = u2f(__builtin_amdgcn_raw_buffer_load_b32(rs, (tid & 127) * 4 + (r & 7) * 512, WS_CSP + cg * 8192 + (r >> 3) * 4096, SC1));
    FENCE(); SB();
    STAMP(63);
    FENCE(); SB();
    STAMP(57);
    { float cs = 0.f;
#pragma unroll
      for (int r = 0; r < 16; ++r) cs += csv[r];
      const unsigned ch = pk2(cs, 0.f) & 0xffffu, cl = pk2(cs - lo16(ch), 0.f) & 0xffffu;
      asm volatile("" :: "v"(cs));
      if (tid < 128) sts128(L, L_CS + tid * 16, v4u{ch | (cl << 16), ch, 0u, 0u}); }
    BAR();
    if (tid == 0) __builtin_amdgcn_raw_buffer_store_b32(tm.epoch, rs, 0, WS_FLAG + (256 + cg * 16 + rg) * 128, SC1);
  }

  unsigned sX = WS_X + (rg * 4 + w) * 40960, sSt = WS_ST0 + (rg * 1024 + w * 32) * 8, sWo = WS_WO + (c8 * 4 + w) * 25600;
  unsigned sOp = WS_O + ((rg * 8 + 2 * w) * 4 + (cg >> 2)) * 5120 + (cg & 3) * 256, sOr = WS_O + ((rg * 8 + half * 4) * 4 + w) * 5120;
  unsigned sXp = WS_X + XB + ((rg * 4 + half * 2 + (w >> 1)) * 40 + 5 * c8) * 1024, sSp = WS_ST1 + ((rg * 8 + c8) * 128 + half * 64 + w * 16) * 8;
  pin_s(sX); pin_s(sSt); pin_s(sWo); pin_s(sOp); pin_s(sOr); pin_s(sXp); pin_s(sSp);
  auto layer = [&](auto m_, const int ll) __attribute__((always_inline)) {
    constexpr int m = decltype(m_)::v;
    const int l = 2 * ll + m;
    const rsrc_t rw = __builtin_amdgcn_make_buffer_rsrc(p.ws + WS_WQ + ((m ^ 1) * 16 + cg) * WQ_SZ, 0, l < 3 ? WQ_SZ : 0u, 0x00020000);
    const unsigned wb1k = w * 1024;
#define DM(i) do { SB(); dmaw<(i)>(rw, l16, wb1k); SB(); } while (0)
    unsigned wos;
#define LDWO(i) do { lda<160 + 4 * (i), ((i) & 3) * 1024, true>(rs, l16, wos); if constexpr (((i) & 3) == 3) sbump(wos); } while (0)
#define P2VM(pp) ((pp) >= 2 && (pp) <= 16 && (pp) != 6 && (pp) != 11 ? 1 : 0)
#define P2VI(pp) (P2VM(pp) ? 7 + (pp) - 2 - ((pp) > 6) - ((pp) > 11) : 0)
#define P2CNT(pp) ((pp) < 2 ? 0 : (pp) - 1 - ((pp) >= 6) - ((pp) >= 11))
#define P2PAIR(pp, nw) do { p2pair<2 * (pp), nw, P2VM(pp), P2VI(pp)>(a0, wq[(2 * (pp)) % 6], wq[(2 * (pp) + 1) % 6], bq, bcq, rs, l16, wos, mfr); \
                            if constexpr (P2VM(pp) == 1 && (P2VI(pp) & 3) == 3) sbump(wos); } while (0)
    v4u wo24, mfr, wq[6];
    unsigned bq, bcq;
    f32x16 a0, a1, a2, a3;
    float rstd, sd, o[20];
    unsigned osb[4], ops, cXp, cSp, cOp, cWo;
    {
      RELANE();
      STAMP(1 + 12 * l);
      asm volatile("" : "+v"(vSt), "+v"(vB3));
      const unsigned xs = sX, sts = sSt;
#define LDX(s) do { lda<4 * (s), ((s) & 3) * 1024, true>(rs, l16, xso); if constexpr (((s) & 3) == 3) sbump(xso); } while (0)
      const unsigned stv = vSt;
      constexpr int NB = PFD + 1, HI = 23;
      constexpr int XR = 15 - XW;
      const unsigned b12 = l16, b3 = vB3;
      unsigned b12h, b3h, bcs;
      unsigned f1 = 0, f1v = 0;
      const rsrc_t rl = __builtin_amdgcn_make_buffer_rsrc(p.ws + WS_WQ + (m * 16 + cg) * WQ_SZ, 0, WQ_SZ, 0x00020000);
      for (unsigned att = 0, att2 = 0, fresh = 1;; fresh = 0) {
        v2u st[4];
        unsigned xso = xs;
        wos = sWo;
        asm volatile("buffer_load_dwordx2 %0, %4, %5, %6 offen sc1\n\tbuffer_load_dwordx2 %1, %4, %5, %6 offen offset:1024 sc1\n\t"
                     "buffer_load_dwordx2 %2, %4, %5, %6 offen offset:2048 sc1\n\tbuffer_load_dwordx2 %3, %4, %5, %6 offen offset:3072 sc1"
                     : "=&v"(st[0]), "=&v"(st[1]), "=&v"(st[2]), "=&v"(st[3]) : "v"(stv), "s"(rs), "s"(sts) : "memory");
        SFOR(s, XW) SB(); LDX(s); if constexpr (s == 1 || s == 5) { SB(); if (fresh) BARE(); } SEND;
        FENCE(); SB();
        v4u wb[NB][3];
        auto rd = [&](auto s_, auto t_) __attribute__((always_inline)) {
          constexpr int s = decltype(s_)::v, t = decltype(t_)::v;
          if constexpr (s < 40) dsr<(s < HI ? s : s - HI) * KSTEP + (t == 1 ? 1024 : 0)>(wb[s % NB][t], t == 2 ? (s < HI ? b3 : b3h) : (s < HI ? b12 : b12h));
          else if constexpr (s == 40) dsr<(t + 1) * 512>(wb[s % NB][t], bcs);
        };
        SFOR(j, PFD) rd(IC<j>{}, IC<2>{}); rd(IC<j>{}, IC<1>{}); rd(IC<j>{}, IC<0>{}); SEND;
        asm volatile("s_waitcnt vmcnt(%4)" : "+v"(st[0]), "+v"(st[1]), "+v"(st[2]), "+v"(st[3]) : "i"(XW) : "memory");
        unsigned ms = max3u(max3u(st[0].x, st[0].y, st[1].x), max3u(st[1].y, st[2].x, st[2].y), umax(st[3].x, st[3].y));
        if (__builtin_expect(any_sent(ms), 0)) {
          CNT(0);
          if (++att2 < 64) {
            poll_probe<200>(rs, (lane >> 3) * 1024 + (lane & 4) * 32 + 4, sts, tm);
            lgkm<0>();
            continue;
          }
        }
        float sa, sq, nm;
        auto stat = [&](auto s_) __attribute__((always_inline)) {
          constexpr int s = decltype(s_)::v;
          if constexpr (s == 1) { sa = u2f(st[0].x) + u2f(st[1].x); asm volatile("" : "+v"(sa)); sq = u2f(st[0].y) + u2f(st[1].y); asm volatile("" : "+v"(sq)); }
          if constexpr (s == 2) { sa += u2f(st[2].x); asm volatile("" : "+v"(sa)); sq += u2f(st[2].y); asm volatile("" : "+v"(sq)); }
          if constexpr (s == 3) { sa += u2f(st[3].x); asm volatile("" : "+v"(sa)); sq += u2f(st[3].y); asm volatile("" : "+v"(sq)); }
          if constexpr (s == 5) { auto x0 = __builtin_amdgcn_permlane32_swap(f2u(sa), f2u(sa), false, false); nm = u2f(x0[0]) + u2f(x0[1]); asm volatile("" : "+v"(nm)); }
          if constexpr (s == 6) { auto x1 = __builtin_amdgcn_permlane32_swap(f2u(sq), f2u(sq), false, false); sq = u2f(x1[0]) + u2f(x1[1]); asm volatile("" : "+v"(sq)); }
          if constexpr (s == 7) { nm *= -1.f / 640.f; sq = sq * (1.f / 640.f) - nm * nm; asm volatile("" : "+v"(nm), "+v"(sq)); }
          if constexpr (s == 8) { sq = __builtin_fmaxf(sq, 0.f) + 1e-5f; rstd = __builtin_amdgcn_rsqf(sq); asm volatile("" : "+v"(rstd), "+v"(sq)); }
          if constexpr (s == 9) { sd = sq * rstd; mfr.x = pk2(nm, nm); asm volatile("" : "+v"(sd), "+v"(mfr.x)); }
          if constexpr (s == 10) { mfr.y = pk2(nm - lo16(mfr.x), 0.f); asm volatile("" : "+v"(mfr.y)); }
          if constexpr (s == 11) { mfr = v4u{h ? 0u : mfr.x, h ? 0u : mfr.y, 0u, 0u}; asm volatile("" : "+v"(mfr)); }
          if constexpr (s == 12) { b12h = b12 + HI * KSTEP; b3h = b3 + HI * KSTEP; asm volatile("" : "+v"(b12h), "+v"(b3h)); }
          if constexpr (s == 13) { bcs = L_CS + m * 2048 + (l16 & 0x1f0); bq = L_P0 + l16; asm volatile("" : "+v"(bcs), "+v"(bq)); bcq = bcs; }
        };
        SFOR(s, 40)
          SB();
          if constexpr (s == 27) {
            if (l == 0) { const int ln = fresh_lane(); f1v = (ln < 16 ? ln * 16 + rg : (ln < 32 ? cg : (c8 >> 2) * 8 + ((ln >> 4) & 1) * 4 + (c8 & 3)) * 16 + (ln & 15)) * 128; probe_issue(f1, rs, f1v, WS_FLAG + 256 * 128); }
            SB();
          }
          if constexpr (s == 33) {
            if (l == 0) {
              probe_wait<6>(f1);
              if (__builtin_amdgcn_ballot_w64(f1 != tm.epoch) != 0) wait_flags(rs, f1v, WS_FLAG + 256 * 128, tm);
            }
            SB();
          }
          if constexpr (s == 10) { if (att == 0) BARE(); SB(); }
          constexpr int c = s % NB;
          constexpr int issued = s <= XR ? XW + 2 * s : (15 + s < 40 ? 15 + s : 40);
          asm volatile("s_waitcnt vmcnt(%0) lgkmcnt(%1)" :: "i"(issued - 1 - s + (s > 25 ? s - 25 : 0)), "i"(s == 39 ? 6 : 3 * ((s + PFD <= 41 ? PFD : 41 - s) - 1)) : "memory");
          mf32a<s == 0, 4 * s>(a1, wb[c][0]);
          if constexpr (s == 0) STAMP(2 + 12 * l);
          rd(IC<s + PFD>{}, IC<2>{});
          if constexpr (s < XR) LDX(issued);
          SB();
          mf32a<s == 0, 4 * s>(a2, wb[c][1]);
          rd(IC<s + PFD>{}, IC<1>{});
          stat(IC<s>{});
          SB();
          mf32a<s == 0, 4 * s>(a3, wb[c][2]);
          rd(IC<s + PFD>{}, IC<0>{});
                    if constexpr (s < 25) LDX(issued + (s < XR ? 1 : 0));
          else if constexpr (s < 32) dmaw<30 + (s >= 25 ? s - 25 : 0)>(rl, l16, wb1k);
          else if constexpr (s == 32) dmaw<0>(rl, l16, wb1k + (w < 2 ? 37 : 36) * 4096);
          else LDWO(s - 33);
          if constexpr (s >= 38) { dsr<(s - 38) * 3072>(wq[(s - 38) * 3], bq); dsr<(s - 38) * 3072 + 1024>(wq[(s - 38) * 3 + 1], bq); dsr<(s - 38) * 3072 + 2048>(wq[(s - 38) * 3 + 2], bq); }
        SEND;
        SB();
        lgkm<6>();
        mf32v<false>(a1, wb[40 % NB][0], mfr); mf32v<false>(a3, wb[40 % NB][2], mfr); mf32v<false>(a2, wb[40 % NB][1], mfr);
        P2PAIR(0, 4); P2PAIR(1, 4);
        asm volatile("s_nop 3");
        SB();
        if (__builtin_expect(!any_nan(a1[0]), 1)) break;
        CNT(1);
        if (++att > 32) break;
      }
      STAMP(3 + 12 * l);
    }
    vmc<7>();
    BARE();
    if (l == 0) {
      RELANE();
    v4u PV;
    asm volatile("v_mov_b32 %0, -1\n\tv_mov_b32 %1, -1\n\tv_mov_b32 %2, -1\n\tv_mov_b32 %3, -1" : "=v"(PV.x), "=v"(PV.y), "=v"(PV.z), "=v"(PV.w));
      stg_sc1(rs, vXv, sXp, PV); stg_sc1(rs, vXv + 512, sXp, PV); stg_sc1(rs, (vXv & 0x1f0) + (h << 9), sXp + 4096, PV);
      __builtin_amdgcn_raw_buffer_store_b64(v2u{PV.x, PV.y}, rs, (lane & 15) * 8, sSp, SC1);
      stg_sc1(rs, vOv, sOp + XB, PV); stg_sc1(rs, vOv + 1024, sOp + XB, PV); stg_sc1(rs, vOv & ~2048u, sOp + XB + 4096, PV);
      stg_sc1(rs, l16, WS_KV + (256 + cg * 16 + rg) * 4096 + w * 1024, PV);
      stg_sc1(rs, l16, WS_HALO + (256 + rg * 16 + cg) * 4096 + w * 1024, PV);
    }
#pragma unroll
    for (int i = 0; i < 4; ++i) osb[i] = sOr + ((w + 1 + i) & 3) * 20480;
    ops = sOr + 4096; cWo = sWo + 20480; cXp = sXp; cSp = sSp; cOp = sOp;
    sX += XB; sSt += STB; sWo += m ? -8 * WO_SZ : 8 * WO_SZ; sOp += XB; sOr += XB; sXp += XB; sSp += STB;
    constexpr int NDM2 = 0;
    if constexpr (m == 0) {
      RELANE();
      asm volatile("" : "+v"(vKa), "+v"(vVa), "+v"(vHv));
      v4u kc0, kc1, kc2, hd[4];
      const unsigned hsrc = WS_HALO + (((l >> 1) * 16 + rg - 1) * 16 + cg) * 4096;
#define VCH(i) ((i) < 4 ? v4u{f2u(a2[4 * (i)]), f2u(a2[4 * (i) + 1]), f2u(a2[4 * (i) + 2]), f2u(a2[4 * (i) + 3])} : v4u{f2u(a3[4]), f2u(a3[5]), f2u(a3[6]), f2u(a3[7])})
      {
        f32x2 t[10];
#pragma unroll
        for (int i = 0; i < 8; ++i) t[i] = pkmul(f32x2{a1[2 * i], a1[2 * i + 1]}, f32x2{rstd, rstd});
#pragma unroll
        for (int i = 0; i < 2; ++i) t[8 + i] = pkmul(f32x2{a3[2 * i], a3[2 * i + 1]}, f32x2{rstd, rstd});
        kc0 = v4u{pk2(t[0][0], t[0][1]), pk2(t[1][0], t[1][1]), pk2(t[2][0], t[2][1]), pk2(t[3][0], t[3][1])};
        kc1 = v4u{pk2(t[4][0], t[4][1]), pk2(t[5][0], t[5][1]), pk2(t[6][0], t[6][1]), pk2(t[7][0], t[7][1])};
        kc2 = v4u{pk2(t[8][0], t[8][1]), pk2(t[9][0], t[9][1]), 0u, 0u};
#pragma unroll
        for (int i = 0; i < 8; ++i) { const f32x2 u = pkmul(f32x2{a2[2 * i], a2[2 * i + 1]}, f32x2{rstd, rstd}); a2[2 * i] = u[0]; a2[2 * i + 1] = u[1]; }
#pragma unroll
        for (int i = 2; i < 4; ++i) { const f32x2 u = pkmul(f32x2{a3[2 * i], a3[2 * i + 1]}, f32x2{rstd, rstd}); a3[2 * i] = u[0]; a3[2 * i + 1] = u[1]; }
        asm volatile("" : "+v"(kc0), "+v"(kc1), "+v"(kc2), "+v"(a2), "+v"(a3)); }
      unsigned qp[10];
      const rsrc_t rh = __builtin_amdgcn_make_buffer_rsrc(p.ws + hsrc, 0, rg > 0 ? 4096u : 0u, 0x00020000);
      if (w == 3) {
        const unsigned hs = hsrc + 65536;
        stg_sc1(rs, vHv, hs, kc0); stg_sc1(rs, vHv + 512, hs, kc1); stg_sc1(rs, vHv + 1024, hs, kc2);
#pragma unroll
        for (int i = 0; i < 5; ++i) stg_sc1(rs, vHv + 1536 + i * 512, hs, VCH(i));
      }
      SFOR(p_, 18)
        constexpr int pp = p_ + 2;
        SB();
        constexpr int nwr = (pp - 1 >= 2 ? (pp - 1 > 16 ? 16 : pp - 1) / 2 : 0) - (pp - 4 >= 2 ? (pp - 4 > 16 ? 16 : pp - 4) / 2 : 0);
        P2PAIR(pp, (2 * pp + 6 <= 41 ? 4 : 39 - 2 * pp) + nwr);
        if constexpr (pp == 2) DSW(vKa, 14 * 48, kc0);
        if constexpr (pp == 4) DSW(vKa, 14 * 48 + 16, kc1);
        if constexpr (pp == 6) DSW(vKa, 14 * 48 + 32, kc2);
        if constexpr (pp >= 8 && pp <= 16 && !(pp & 1)) { constexpr int vi = pp >= 8 ? (pp - 8) / 2 : 0; DSW(vVa, 14 * 80 + vi * 16, VCH(vi)); }
        if constexpr (pp == 11) { qp[8] = pk2(a3[8], a3[9]); qp[9] = pk2(a3[10], a3[11]); asm volatile("" : "+v"(qp[8]), "+v"(qp[9])); }
        if constexpr (pp == HLP) {
          if (w == 0) {
#pragma unroll
            for (int i = 0; i < 4; ++i) hd[i] = ldg_sc1(rh, l16 + i * 1024, 0);
          }
        }
      SEND;
      SB();
      lgkm<0>();
      mf32v<false>(a0, wq[40 % 6], mfr);
      vmc<13>();
      BARE();
      const unsigned ka = vKa, va = vVa;
      const float fq = rstd * (LOG2E * 0.22360679774997896f);
      STAMP(5 + 12 * l);
      if (w == 0) {
        const unsigned q_ = ((lane >> 4) & 1) * 144 + (lane & 15);
        unsigned h0 = L_K + q_ * 48 + h * 16, h1 = h ? L_V + q_ * 80 : L_K + q_ * 48 + 32, h2 = L_V + q_ * 80 + 16 + h * 16;
        asm volatile("" : "+v"(h0), "+v"(h1), "+v"(h2));
        unsigned mm = 0;
#pragma unroll
        for (int i = 0; i < 4; ++i) mm = mx4(mm, hd[i]);
        if (__builtin_expect(any_sent(mm), 0)) {
          unsigned sp = 0;
          do {
            FENCE();
            mm = 0;
#pragma unroll
            for (int i = 0; i < 4; ++i) hd[i] = ldg_sc1(rh, l16 + i * 1024, 0);
#pragma unroll
            for (int i = 0; i < 4; ++i) mm = mx4(mm, hd[i]);
            CNT(2);
          } while (any_sent(mm) && !give_up(sp, tm));
        }
        sts128(L, h0, hd[0]);
        sts128(L, h1, hd[1]);
        sts128(L, h2, hd[2]);
        sts128(L, h2 + 32, hd[3]);
      }
      STAMP(4 + 12 * l);
      float lg[8];
      v4u kb[8][2], vb[8][5];
      v2u kt[8];
      auto rdk = [&](auto j_) __attribute__((always_inline)) { constexpr int j = decltype(j_)::v;
        kb[j][0] = lds128(L, ka + (7 - j) * 96); kb[j][1] = lds128(L, ka + (7 - j) * 96 + 16); kt[j] = lds64(L, ka + (7 - j) * 96 + 32); };
      auto rdv = [&](auto j_) __attribute__((always_inline)) { constexpr int j = decltype(j_)::v;
#pragma unroll
        for (int i = 0; i < 5; ++i) vb[j][i] = lds128(L, va + (7 - j) * 160 + i * 16); };
      SB();
      rdk(IC<0>{}); rdk(IC<1>{}); rdk(IC<2>{}); rdk(IC<3>{});
      SB();
#pragma unroll
      for (int i = 0; i < 8; ++i) qp[i] = pk2(a0[2 * i], a0[2 * i + 1]);
      SFOR(jj, 4)
        SB();
        if constexpr (jj < ADM) DM(NDM2 + jj);
        if constexpr (jj < 2) { rdk(IC<2 * jj + 4>{}); rdk(IC<2 * jj + 5>{}); } else rdv(IC<jj - 2>{});
        SB();
        float s0 = 0.f, s1 = 0.f;
#pragma unroll
        for (int i = 0; i < 8; ++i) { s0 = dot2(kb[2 * jj][i >> 2][i & 3], qp[i], s0); s1 = dot2(kb[2 * jj + 1][i >> 2][i & 3], qp[i], s1); }
#pragma unroll
        for (int i = 0; i < 2; ++i) { s0 = dot2(kt[2 * jj][i], qp[8 + i], s0); s1 = dot2(kt[2 * jj + 1][i], qp[8 + i], s1); }
        asm volatile("" : "+v"(s0), "+v"(s1));
        lg[2 * jj] = s0; lg[2 * jj + 1] = s1;
      SEND;
      SB();
      if constexpr (4 < ADM) DM(NDM2 + 4);
      rdv(IC<2>{});
      SB();
      if (rg == 0 && w == 0) {
#pragma unroll
        for (int j = 1; j < 8; ++j) lg[j] = tok >= 2 * j ? lg[j] : -__builtin_inff();
      }
      const float mx = max3f(max3f(lg[0], lg[1], lg[2]), max3f(lg[3], lg[4], lg[5]), max3f(lg[6], lg[7], lg[7]));
      const float nb = -fq * mx;
#pragma unroll
      for (int j = 0; j < 8; j += 2) { const f32x2 t = pkfma(f32x2{lg[j], lg[j + 1]}, f32x2{fq, fq}, f32x2{nb, nb}); lg[j] = t[0]; lg[j + 1] = t[1]; }
      SB();
#pragma unroll
      for (int j = 0; j < 8; ++j) lg[j] = __builtin_amdgcn_exp2f(lg[j]);
      SB();
      if constexpr (5 < ADM) DM(NDM2 + 5);
      SB();
      const float inv = __builtin_amdgcn_rcpf(((lg[0] + lg[1]) + (lg[2] + lg[3])) + ((lg[4] + lg[5]) + (lg[6] + lg[7])));
      f32x2 oa[10];
      SFOR(j, 8)
        SB();
        if constexpr (6 + j < ADM) DM(NDM2 + 6 + j);
        if constexpr (j < 5) rdv(IC<j + 3>{});
        SB();
        const float pj = lg[j];
        SFOR(i, 5)
          const f32x4 v = __builtin_bit_cast(f32x4, vb[j][i]);
          if constexpr (j == 0) { oa[2 * i] = pkmul(f32x2{pj, pj}, f32x2{v[0], v[1]}); oa[2 * i + 1] = pkmul(f32x2{pj, pj}, f32x2{v[2], v[3]}); }
          else { oa[2 * i] = pkfma(f32x2{pj, pj}, f32x2{v[0], v[1]}, oa[2 * i]); oa[2 * i + 1] = pkfma(f32x2{pj, pj}, f32x2{v[2], v[3]}, oa[2 * i + 1]); }
        SEND;
      SEND;
#pragma unroll
      for (int i = 0; i < 10; ++i) oa[i] = pkmul(oa[i], f32x2{inv, inv});
#pragma unroll
      for (int i = 0; i < 10; ++i) { o[2 * i] = oa[i][0]; o[2 * i + 1] = oa[i][1]; }
    } else {
      RELANE();
      asm volatile("" : "+v"(vImg), "+v"(vA0), "+v"(vA2), "+v"(vB), "+v"(vS0), "+v"(vS1), "+v"(vK0), "+v"(vK1), "+v"(vF0), "+v"(vF1), "+v"(vKv), "+v"(vKp));
      const float sl = rstd * LOG2E;
      {
        const unsigned rb = vImg;
        float kk[20];
#pragma unroll
        for (int i = 0; i < 16; ++i) kk[i] = a1[i];
#pragma unroll
        for (int i = 0; i < 4; ++i) kk[16 + i] = a3[i];
        unsigned ph[20];
        phi_map<true>(kk, sl, rstd, ph, [&](auto i_) __attribute__((always_inline)) { constexpr int i = decltype(i_)::v; SB();
          if constexpr (i < ADM) dmaw<NDM2 + i>(rw, l16, wb1k);
          if (i == 0) sts128(L, rb ^ (5 << 4), v4u{pk2(a2[0], a2[1]), pk2(a2[2], a2[3]), pk2(a2[4], a2[5]), pk2(a2[6], a2[7])});
          if (i == 1) sts128(L, rb ^ (6 << 4), v4u{pk2(a2[8], a2[9]), pk2(a2[10], a2[11]), pk2(a2[12], a2[13]), pk2(a2[14], a2[15])});
          if (i == 2) sts128(L, rb ^ (7 << 4), v4u{pk2(a3[4], a3[5]), pk2(a3[6], a3[7]), pk2(sd, 0.f), 0u});
          if (i == 5) sts128(L, rb ^ (0 << 4), v4u{ph[0], ph[1], ph[2], ph[3]});
          if (i == 6) sts128(L, rb ^ (1 << 4), v4u{ph[4], ph[5], ph[6], ph[7]});
          SB(); });
#pragma unroll
        for (int c = 2; c < 5; ++c) sts128(L, rb ^ (c << 4), v4u{ph[4 * c], ph[4 * c + 1], ph[4 * c + 2], ph[4 * c + 3]});
      }
      {
        const unsigned aA0 = vA0, aA2 = vA2, aB = vB;
        unsigned aA0x = (aA0 ^ 64) + 512, aA2x = (aA2 ^ 64) + 512, aBx = (aB ^ 64) + 512;
        asm volatile("" : "+v"(aA0x), "+v"(aA2x), "+v"(aBx));
        BAR();
        f32x4 d0, d1, d2;
        v2u t[4][8];
        auto trd = [&](auto k_) __attribute__((always_inline)) {
          constexpr int ks = decltype(k_)::v; constexpr unsigned o_ = ks * 4096;
          t[ks][0] = ldstr(L, aB + o_); t[ks][1] = ldstr(L, aBx + o_); t[ks][6] = ldstr(L, aA2 + o_); t[ks][7] = ldstr(L, aA2x + o_);
          t[ks][2] = ldstr(L, aA0 + o_); t[ks][3] = ldstr(L, aA0x + o_); t[ks][4] = ldstr(L, aA0 + 8 + o_); t[ks][5] = ldstr(L, aA0x + 8 + o_);
          SB();
        };
        trd(IC<0>{});
        SFOR(ks, 4)
          if constexpr (ks < 3) trd(IC<ks + 1>{});
          const v4u bb = {t[ks][0].x, t[ks][0].y, t[ks][1].x, t[ks][1].y};
          mf16v<ks == 0>(d2, v4u{t[ks][6].x, t[ks][6].y, t[ks][7].x, t[ks][7].y}, bb);
          mf16v<ks == 0>(d0, v4u{t[ks][2].x, t[ks][2].y, t[ks][3].x, t[ks][3].y}, bb);
          mf16v<ks == 0>(d1, v4u{t[ks][4].x, t[ks][4].y, t[ks][5].x, t[ks][5].y}, bb);
          SB();
        SEND;
        asm volatile("s_nop 7");
        SB();
        const unsigned e0 = pk2(d2[0], d2[1]), e1 = pk2(d2[2], d2[3]);
        auto s0 = __builtin_amdgcn_permlane16_swap(e0, e0, false, false);
        auto s1 = __builtin_amdgcn_permlane16_swap(e1, e1, false, false);
        const unsigned ks_ = WS_KV + (((l >> 1) * 16 + cg) * 16 + rg) * 4096;
        stg_sc1(rs, vS0, ks_, v4u{pk2(d0[0], d0[1]), pk2(d0[2], d0[3]), pk2(d1[0], d1[1]), pk2(d1[2], d1[3])});
        stg_sc1(rs, vS1, ks_, v4u{e0, e1, s0[1], s1[1]});
      }
      STAMP(4 + 12 * l);
      if (l == 1) {
    v4u PV;
    asm volatile("v_mov_b32 %0, -1\n\tv_mov_b32 %1, -1\n\tv_mov_b32 %2, -1\n\tv_mov_b32 %3, -1" : "=v"(PV.x), "=v"(PV.y), "=v"(PV.z), "=v"(PV.w));
#pragma unroll
        for (int u = 0; u < 2; ++u) {
          stg_sc1(rs, vOv, sOp + u * XB, PV); stg_sc1(rs, vOv + 1024, sOp + u * XB, PV); stg_sc1(rs, vOv & ~2048u, sOp + u * XB + 4096, PV);
          stg_sc1(rs, vXv, cXp + u * XB, PV); stg_sc1(rs, vXv + 512, cXp + u * XB, PV); stg_sc1(rs, (vXv & 0x1f0) + (h << 9), cXp + u * XB + 4096, PV);
          __builtin_amdgcn_raw_buffer_store_b64(v2u{PV.x, PV.y}, rs, (lane & 15) * 8, cSp + u * STB, SC1);
        }
      }
      const unsigned ks_ = WS_KV + ((l >> 1) * 16 + cg) * 65536 + (w < 3 ? w * 1024 : 11 * 256);
      const unsigned kvv = vKv;
      unsigned kpr, kso[4];
#pragma unroll
      for (int i = 0; i < 4; ++i) kso[i] = ks_ + i * 16384;
#define LDKP(r) lda<4 * (r), ((r) >> 2) * 256, true>(rs, kvv, kso[(r) & 3])
      const unsigned kps = WS_KV + ((l >> 1) * 16 + cg) * 65536, kpv = vKp;
      SFOR(p_, 18) constexpr int pp = p_ + 2; SB(); P2PAIR(pp, (2 * pp + 6 <= 41 ? 4 : 39 - 2 * pp));
        if constexpr (pp == 17) probe_issue(kpr, rs, kpv, kps);
        if constexpr (pp >= 18 && 2 * (pp - 18) < KPS) { constexpr int r = pp >= 18 ? 2 * (pp - 18) : 0; LDKP(r); LDKP(r + 1); }
      SEND;
      SB();
      lgkm<0>();
      mf32v<false>(a0, wq[40 % 6], mfr);
      SFOR(r, 16) if constexpr (r >= (KPS > 4 ? 4 : KPS)) LDKP(r); SEND;
      FENCE(); SB();
      STAMP(5 + 12 * l);
      unsigned ph[20];
      float eps;
      { float qq[20];
#pragma unroll
        for (int i = 0; i < 4; ++i) qq[16 + i] = a3[8 + i];
#pragma unroll
        for (int i = 0; i < 16; ++i) qq[i] = a0[i];
        eps = 1e-6f * phi_map<false>(qq, sl, 1.f, ph, [&](auto i_) __attribute__((always_inline)) {
          if constexpr (decltype(i_)::v == 4) {
            SB();
            probe_wait<16>(kpr);
            if (__builtin_expect(any_sent(kpr), 0)) {
              CNT(3);
              poll_probe<100>(rs, kpv, kps, tm);
              SFOR(r, 16) LDKP(r); SEND;
            }
            FENCE(); SB();
          } });
      }
      v4u f[10], sel;
      { const int r = lane & 15;
#pragma unroll
        for (int j = 0; j < 4; ++j) sel[j] = r == 2 * j ? 0x00003F80u : r == 2 * j + 1 ? 0x3F800000u : 0u;
      }
#pragma unroll
      for (int i = 0; i < 20; i += 4) asm volatile("" : "+v"(ph[i]), "+v"(ph[i + 1]), "+v"(ph[i + 2]), "+v"(ph[i + 3]));
      asm volatile("s_nop 1" : "+v"(sel));
      SB();
      f32x4 sm[4];
      for (unsigned sp = 0;;) {
        SFOR(r, 16) if constexpr ((r & 3) == 0) vmc<12 - r>(); mf16vz<(r & 3) == 0, 4 * r>(sm[r >> 2], sel); SEND;
        asm volatile("s_nop 7");
        SB();
        if (__builtin_expect(!any_nan((sm[0][0] + sm[1][0]) + (sm[2][0] + sm[3][0])), 1)) break;
        if (give_up(sp, tm)) break;
        CNT(1);
        FENCE();
        SFOR(r, 16) LDKP(r); SEND;
      }
      {
        const unsigned st0 = w < 3 ? 400 : 800, st1 = w < 2 ? 400 : 800;
#pragma unroll
        for (int j = 0; j < 4; ++j) *(LAS v2u*)(L + (j < 2 ? vK0 + j * st0 : vK1 + (j - 2) * st1)) = v2u{pk2(sm[j][0], sm[j][1]), pk2(sm[j][2], sm[j][3])};
      }
      SFOR(i, (ADM > 10 ? ADM - 10 : 0)) DM(10 + i); SEND;
      BAR();
#pragma unroll
      for (int s = 0; s < 5; ++s) { f[2 * s + 1] = lds128(L, vF1 + s * 400); f[2 * s] = lds128(L, vF0 + s * 400); }
      f32x16 n0, n1;
      SFOR(s, 5)
        const v4u bb = {ph[4 * s], ph[4 * s + 1], ph[4 * s + 2], ph[4 * s + 3]};
        mf32v<s == 0>(n1, f[2 * s + 1], bb); mf32v<s == 0>(n0, f[2 * s], bb);
      SEND;
      MFMA_SETTLE();
      SB();
      const float inv = __builtin_amdgcn_rcpf(n1[4] + eps);
#pragma unroll
      for (int i = 0; i < 2; ++i) { const f32x2 t = pkmul(f32x2{n1[2 * i], n1[2 * i + 1]}, f32x2{inv, inv}); o[16 + 2 * i] = t[0]; o[17 + 2 * i] = t[1]; }
#pragma unroll
      for (int i = 0; i < 8; ++i) { const f32x2 t = pkmul(f32x2{n0[2 * i], n0[2 * i + 1]}, f32x2{inv, inv}); o[2 * i] = t[0]; o[2 * i + 1] = t[1]; }
    }
    {
      RELANE();
      asm volatile("" : "+v"(vOv), "+v"(vOp), "+v"(vXv));
      const unsigned os = cOp;
      stg_sc1(rs, vOv, os, v4u{pk2(o[0], o[1]), pk2(o[2], o[3]), pk2(o[4], o[5]), pk2(o[6], o[7])});
      stg_sc1(rs, vOv + 1024, os, v4u{pk2(o[8], o[9]), pk2(o[10], o[11]), pk2(o[12], o[13]), pk2(o[14], o[15])});
      const unsigned e0 = pk2(o[16], o[17]), e1 = pk2(o[18], o[19]);
      auto s0 = __builtin_amdgcn_permlane32_swap(e0, e0, false, false);
      auto s1 = __builtin_amdgcn_permlane32_swap(e1, e1, false, false);
      stg_sc1(rs, h ? 0x80000000u : vOv, os + 4096, v4u{e0, e1, s0[1], s1[1]});
      STAMP(6 + 12 * l);
      __builtin_amdgcn_s_sleep(DO1);
      wos = cWo;
      SFOR(i, 4) LDWO(20 + i); SEND;
      asm volatile("buffer_load_dwordx4 %0, %1, %2, %3 offen sc1" : "=v"(wo24) : "v"(l16), "s"(rs), "s"(wos) : "memory");
      SFOR(i, ODM) DM(NDM2 + ADM + i); SEND;
      __builtin_amdgcn_s_sleep(DLY_O);
      STAMP(7 + 12 * l);
      const unsigned l16b = l16 + 1024;
#define LDO(i, s) do { if constexpr ((s) == 0) lda<20 * (i), 0, true>(rs, l16, osb[i]); else lda<20 * (i) + 4 * (s), ((s) - 1) * 1024, true>(rs, l16b, osb[i]); } while (0)
      unsigned opr;
      const unsigned opv = vOp;
      probe_issue(opr, rs, opv, ops);
      SFOR(s, 5) LDO(0, s); if constexpr (s == 1) BARE(); SEND;
      BARE();
      SFOR(s, 5) LDO(1, s); SEND;
      SB();
      unsigned rwa[3], rra = L_RED + w * 15360 + l16;
      f32x2 rsum[10];
#pragma unroll
      for (int i = 0; i < 3; ++i) rwa[i] = L_RED + l16 + (((w + 1 + i) & 3) * 3 + i) * 5120;
#pragma unroll
      for (int i = 0; i < 10; ++i) rsum[i] = f32x2{lo16(xr[i]), hi16(xr[i])};
      asm volatile("" : "+v"(rsum[0]), "+v"(rsum[1]), "+v"(rsum[2]), "+v"(rsum[3]), "+v"(rsum[4]), "+v"(rsum[5]), "+v"(rsum[6]), "+v"(rsum[7]), "+v"(rsum[8]), "+v"(rsum[9]));
      asm volatile("" : "+v"(rwa[0]), "+v"(rwa[1]), "+v"(rwa[2]), "+v"(rra));
      f32x4 acc[2][5], part[15];
      probe_wait<10>(opr);
      asm volatile("" : "+v"(wo24));
      if (__builtin_expect(any_sent(opr), 0)) {
        CNT(4);
        poll_probe<100>(rs, opv, ops, tm);
        SFOR(s, 5) LDO(0, s); SEND;
        SFOR(s, 5) LDO(1, s); SEND;
      }
      auto redo = [&](auto i_) __attribute__((always_inline)) {
        constexpr int i = decltype(i_)::v;
        unsigned sp = 0;
        do {
          CNT(1);
          SFOR(s, 5) LDO(i, s); SEND;
          vmc<0>();
          SFOR(j, 25) if constexpr (j < 24) mf16aa<(j < 5), 160 + 4 * j, 20 * i + 4 * (j / 5)>(acc[i & 1][j % 5]); else mf16va<20 * i + 16>(acc[i & 1][4], wo24); SEND;
          asm volatile("s_nop 7");
          SB();
        } while (any_nan(acc[i & 1][4][0]) && !give_up(sp, tm));
      };
      constexpr int NDO = NDM2 + ADM + ODM;
      static_assert(NDO + GDM == 22 && NDM2 + ADM + ODM <= 17 && GDM <= 10 && ADM <= 14, "");
      SFOR(i, 4)
        SFOR(j, 25)
          constexpr int s = j / 5, ft = j % 5, WS0 = 4, wstep = 4;
          SB();
          constexpr int gi = 5 * i + s;
          if constexpr (ft == 0) vmc<(gi < 10 ? 9 : 19 - gi) + (gi > 20 - GDM ? gi - (20 - GDM) : 0)>();
          if constexpr (j < 24) mf16aa<s == 0, 160 + 4 * j, 20 * i + 4 * s>(acc[i & 1][ft]); else mf16va<20 * i + 16>(acc[i & 1][ft], wo24);
          if constexpr (i == 0 && j == 0) STAMP(8 + 12 * l);
          if constexpr (ft == 2) {
            if constexpr (i < 2) LDO(i + 2, s);
            else if constexpr (gi >= 20 - GDM) dmaw<NDO + gi - (20 - GDM)>(rw, l16, wb1k);
          }
          if constexpr (i > 0 && j == WS0 - 1) { asm volatile("s_nop 1"); SB(); if (__builtin_expect(any_nan(acc[(i - 1) & 1][4][0]), 0)) redo(IC<i - 1>{}); }
          if constexpr (i > 0 && j >= WS0 && j <= WS0 + 4 * wstep && (j - WS0) % wstep == 0)
            DSW(rwa[i > 0 ? i - 1 : 0], ((j - WS0) / wstep) * 1024, acc[(i - 1) & 1][(j - WS0) / wstep]);
          if constexpr (i == 3 && j == 23) { SB(); BAR(); }
          if constexpr (i == 3 && j >= 23) {
#pragma unroll
            for (int q = (j - 23) * 8; q < (j - 23) * 8 + 8 && q < 15; ++q) part[q] = __builtin_bit_cast(f32x4, lds128(L, rra + (q % 3) * 5120 + (q / 3) * 1024));
          }
        SEND;
      SEND;
      SB();
      STAMP(9 + 12 * l);
#pragma unroll
      for (int ft = 0; ft < 5; ++ft)
#pragma unroll
        for (int q = 0; q < 3; ++q) { rsum[2 * ft] = pkadd(rsum[2 * ft], f32x2{part[ft * 3 + q][0], part[ft * 3 + q][1]}); rsum[2 * ft + 1] = pkadd(rsum[2 * ft + 1], f32x2{part[ft * 3 + q][2], part[ft * 3 + q][3]}); }
      asm volatile("" : "+v"(rsum[8]), "+v"(rsum[9]));
      SB();
      if (__builtin_expect(any_nan(acc[1][4][0]), 0)) redo(IC<3>{});
      auto fin = [&](int ft) __attribute__((always_inline)) {
        const f32x2 lo = pkadd(rsum[2 * ft], f32x2{acc[1][ft][0], acc[1][ft][1]}), hi = pkadd(rsum[2 * ft + 1], f32x2{acc[1][ft][2], acc[1][ft][3]});
        xr[2 * ft] = pk2(lo[0], lo[1]); xr[2 * ft + 1] = pk2(hi[0], hi[1]);
      };
      fin(0); fin(1);
      if (l < 3) {
        stg_sc1(rs, vXv, cXp, v4u{xr[0], xr[1], xr[2], xr[3]});
        SB();
        fin(2); fin(3);
        stg_sc1(rs, vXv + 512, cXp, v4u{xr[4], xr[5], xr[6], xr[7]});
        SB();
        fin(4);
        { auto s0 = __builtin_amdgcn_permlane16_swap(xr[8], xr[8], false, false);
          auto s1 = __builtin_amdgcn_permlane16_swap(xr[9], xr[9], false, false);
          stg_sc1(rs, (lane & 16) ? 0x80000000u : (vXv & 0x1f0) + (h << 9), cXp + 4096, v4u{xr[8], xr[9], s0[1], s1[1]}); }
        SB();
        { float sa = 0.f, sq = 0.f;
#pragma unroll
          for (int i = 0; i < 10; ++i) { sa = dot2(xr[i], 0x3F803F80u, sa); sq = dot2(xr[i], xr[i], sq); }
          { auto a = __builtin_amdgcn_permlane16_swap(f2u(sa), f2u(sa), false, false); sa = u2f(a[0]) + u2f(a[1]);
            auto c = __builtin_amdgcn_permlane16_swap(f2u(sq), f2u(sq), false, false); sq = u2f(c[0]) + u2f(c[1]); }
          { auto a = __builtin_amdgcn_permlane32_swap(f2u(sa), f2u(sa), false, false); sa = u2f(a[0]) + u2f(a[1]);
            auto c = __builtin_amdgcn_permlane32_swap(f2u(sq), f2u(sq), false, false); sq = u2f(c[0]) + u2f(c[1]); }
          __builtin_amdgcn_raw_buffer_store_b64(v2u{f2u(sa), f2u(sq)}, rs, lane < 16 ? lane * 8 : 0x80000000u, cSp, SC1); }
        STAMP(10 + 12 * l);
        if (l == 0 && w < 2) load_cs(L, rs, 1, cg, tid);
        BARE();
        __builtin_amdgcn_s_sleep(DX1);
        SFOR(i, LW) DM(22 + i); SEND;
        __builtin_amdgcn_s_sleep(DLY_X);
      } else {
        fin(2); fin(3); fin(4);
        GAS v2u* dst = (GAS v2u*)(p.out + (size_t)(rg * 128 + half * 64 + w * 16 + (lane & 15)) * 640 + c8 * 80 + (lane >> 4) * 20);
#pragma unroll
        for (int i = 0; i < 5; ++i) dst[i] = v2u{xr[2 * i], xr[2 * i + 1]};
        if (b == 0 && w == 0) __builtin_amdgcn_raw_buffer_store_b32(tm.epoch, rs, lane ? 0x80000000u : 0u, WS_CTRL + 128, SC1);
      }
      STAMP(11 + 12 * l);
    }
  };
#pragma nounroll
  for (int ll = 0; ll < 2; ++ll) { layer(IC<0>{}, ll); layer(IC<1>{}, ll); }
}

extern "C" void kernel_launch(void* const* d_in, const int* in_sizes, int n_in, void* d_out, int out_size, void* d_ws, size_t ws_size, hipStream_t stream) {
  Params p;
  p.x = (const u16*)d_in[0]; p.wqA = (const u16*)d_in[1]; p.woA = (const u16*)d_in[2];
  p.wqB = (const u16*)d_in[4]; p.wphi = (const u16*)d_in[5]; p.woB = (const u16*)d_in[6];
  p.out = (u16*)d_out; p.ws = (unsigned char*)d_ws;
  (void)hipFuncSetAttribute((const void*)k_main, hipFuncAttributeMaxDynamicSharedMemorySize, LDS_ALLOC);
  k_main<<<256, 256, LDS_ALLOC, stream>>>(p);
}
```

```cpp
#include <hip/hip_runtime.h>
#include <stdint.h>

typedef unsigned short u16;
typedef unsigned v4u __attribute__((ext_vector_type(4)));
typedef unsigned v2u __attribute__((ext_vector_type(2)));
typedef float f32x2 __attribute__((ext_vector_type(2)));
typedef float f32x4 __attribute__((ext_vector_type(4)));
typedef float f32x16 __attribute__((ext_vector_type(16)));
typedef __bf16 bf16x8 __attribute__((ext_vector_type(8)));
typedef __bf16 bf16x2 __attribute__((ext_vector_type(2)));
typedef short s16x4 __attribute__((ext_vector_type(4)));
#define LAS __attribute__((address_space(3)))
#define GAS __attribute__((address_space(1)))
typedef __amdgpu_buffer_rsrc_t rsrc_t;
#define DEV __device__ __forceinline__

#define DSC1 " sc1"
constexpr unsigned WQ_SZ = 153600, WO_SZ = 102400, XB = 2621440, STB = 131072;
constexpr unsigned WS_CTRL = 0;
constexpr unsigned WS_FLAG = 4096;
constexpr unsigned WS_WQ = WS_FLAG + 4 * 256 * 128;
constexpr unsigned WS_WO = WS_WQ + 32 * WQ_SZ;
constexpr unsigned WS_CSP = WS_WO + 16 * WO_SZ;
constexpr unsigned WS_ST0 = WS_CSP + 2 * 16 * 16 * 512;
constexpr unsigned WS_ST1 = WS_ST0 + STB;
constexpr unsigned WS_X = WS_ST0 + 4 * STB;
constexpr unsigned WS_O = WS_X + 4 * XB;
constexpr unsigned WS_KV = WS_O + 4 * XB;
constexpr unsigned WS_HALO = WS_KV + 2 * 256 * 4096;
constexpr unsigned WS_END = WS_HALO + 2 * 256 * 4096;
constexpr unsigned WS_STAMP = 40u << 20;
constexpr unsigned WS_TOTAL = WS_STAMP + 256 * 4 * 64 * 8;
constexpr unsigned KSTEP = 2816, L_P0 = 112640, L_KVA = 153600, L_CS = L_KVA + 4096;
constexpr unsigned L_SCR = 73216;
constexpr unsigned L_K = L_SCR, L_V = L_SCR + 2 * 144 * 48;
constexpr unsigned L_RED = 90112;
constexpr unsigned LDS_BYTES = L_CS + 4096;
constexpr unsigned SENT = 0xFFFFFFFFu;
constexpr unsigned CODE_BYTES = 24576;
constexpr int SC1 = 16;
constexpr float LOG2E = 1.4426950408889634f;

struct Params { const u16 *x, *wqA, *woA, *wqB, *wphi, *woB; u16* out; unsigned char* ws; };

DEV unsigned pk2(float a, float b) { bf16x2 v = {(__bf16)a, (__bf16)b}; return __builtin_bit_cast(unsigned, v); }
DEV float lo16(unsigned u) { return __builtin_bit_cast(float, u << 16); }
DEV float hi16(unsigned u) { return __builtin_bit_cast(float, u & 0xffff0000u); }
DEV unsigned f2u(float f) { return __builtin_bit_cast(unsigned, f); }
DEV float u2f(unsigned u) { return __builtin_bit_cast(float, u); }
DEV unsigned umax(unsigned a, unsigned b) { return __builtin_elementwise_max(a, b); }
DEV unsigned umin(unsigned a, unsigned b) { return __builtin_elementwise_min(a, b); }
DEV float max3f(float a, float b, float c) { float d; asm("v_max3_f32 %0, %1, %2, %3" : "=v"(d) : "v"(a), "v"(b), "v"(c)); return d; }
DEV float min3f(float a, float b, float c) { float d; asm("v_min3_f32 %0, %1, %2, %3" : "=v"(d) : "v"(a), "v"(b), "v"(c)); return d; }
DEV float dot2(unsigned a, unsigned b, float c) { return __builtin_amdgcn_fdot2_f32_bf16(__builtin_bit_cast(bf16x2, a), __builtin_bit_cast(bf16x2, b), c, false); }
DEV f32x2 pkfma(f32x2 a, f32x2 b, f32x2 c) { return __builtin_elementwise_fma(a, b, c); }
DEV f32x2 pkmul(f32x2 a, f32x2 b) { return a * b; }
DEV f32x2 pkadd(f32x2 a, f32x2 b) { return a + b; }
DEV f32x16 mfma32(v4u a, v4u b, f32x16 c) { return __builtin_amdgcn_mfma_f32_32x32x16_bf16(__builtin_bit_cast(bf16x8, a), __builtin_bit_cast(bf16x8, b), c, 0, 0, 0); }
DEV f32x4 mfma16(v4u a, v4u b, f32x4 c) { return __builtin_amdgcn_mfma_f32_16x16x32_bf16(__builtin_bit_cast(bf16x8, a), __builtin_bit_cast(bf16x8, b), c, 0, 0, 0); }
DEV v4u lds128(LAS unsigned char* L, unsigned a) { return *(LAS v4u*)(L + a); }
DEV v2u lds64(LAS unsigned char* L, unsigned a) { return *(LAS v2u*)(L + a); }
DEV void sts128(LAS unsigned char* L, unsigned a, v4u v) { *(LAS v4u*)(L + a) = v; }
DEV v2u ldstr(LAS unsigned char* L, unsigned a) { return __builtin_bit_cast(v2u, __builtin_amdgcn_ds_read_tr16_b64_v4i16((LAS s16x4*)(L + a))); }
DEV v4u ldg(rsrc_t rs, unsigned voff, unsigned soff) { return __builtin_amdgcn_raw_buffer_load_b128(rs, voff, soff, 0); }
DEV v4u ldg_sc1(rsrc_t rs, unsigned voff, unsigned soff) { return __builtin_amdgcn_raw_buffer_load_b128(rs, voff, soff, SC1); }
DEV v2u ldg64_sc1(rsrc_t rs, unsigned voff, unsigned soff) { return __builtin_amdgcn_raw_buffer_load_b64(rs, voff, soff, SC1); }
DEV void stg_sc1(rsrc_t rs, unsigned voff, unsigned soff, v4u v) { __builtin_amdgcn_raw_buffer_store_b128(v, rs, voff, soff, SC1); }
DEV unsigned mx4(unsigned m, v4u d) { return umax(umax(umax(m, d.x), d.y), umax(d.z, d.w)); }
DEV unsigned max3u(unsigned a, unsigned b, unsigned c) { return umax(umax(a, b), c); }
DEV bool any_sent(unsigned m) { return __builtin_amdgcn_ballot_w64(m == SENT) != 0; }
#define BAR() asm volatile("s_waitcnt lgkmcnt(0)\n\ts_barrier" ::: "memory")
#define DRAIN() asm volatile("s_waitcnt vmcnt(0)" ::: "memory")
#define FENCE() asm volatile("" ::: "memory")
#define SB() __builtin_amdgcn_sched_barrier(0)
DEV void dma4(rsrc_t rs, unsigned voff, unsigned soff, unsigned ldsa) {
  asm volatile("s_mov_b32 m0, %0\n\ts_nop 0\n\tbuffer_load_dwordx4 %1, %2, %3 offen sc1 lds\n\tbuffer_load_dwordx4 %1, %2, %3 offen offset:1024 sc1 lds\n\t"
               "buffer_load_dwordx4 %1, %2, %3 offen offset:2048 sc1 lds\n\tbuffer_load_dwordx4 %1, %2, %3 offen offset:3072 sc1 lds"
               :: "s"(__builtin_amdgcn_readfirstlane(ldsa)), "v"(voff), "s"(rs), "s"(__builtin_amdgcn_readfirstlane(soff)) : "memory");
}
DEV void dma1(rsrc_t rs, unsigned voff, unsigned soff, unsigned ldsa) {
  asm volatile("s_mov_b32 m0, %0\n\ts_nop 0\n\tbuffer_load_dwordx4 %1, %2, %3 offen sc1 lds"
               :: "s"(__builtin_amdgcn_readfirstlane(ldsa)), "v"(voff), "s"(rs), "s"(__builtin_amdgcn_readfirstlane(soff)) : "memory");
}
DEV void dma_range(rsrc_t rs, unsigned l16, unsigned wsrc, int w, int k0, int k1) {
  int k = k0 + 4 * w;
  for (; k + 4 <= k1; k += 16) dma4(rs, l16, wsrc + k * 1024, k * 1024);
  for (; k < k1; ++k) dma1(rs, l16, wsrc + k * 1024, k * 1024);
}
struct Tmo { unsigned char* ws; unsigned epoch; };
DEV bool give_up(unsigned& sp, const Tmo& tm) {
  ++sp;
  if (sp > 32) __builtin_amdgcn_s_sleep(8);
  if ((sp & 63u) == 0u) {
    GAS unsigned* t = (GAS unsigned*)(tm.ws + WS_CTRL);
    if (sp >= (1u << 16)) { __hip_atomic_store(t, tm.epoch, __ATOMIC_RELAXED, __HIP_MEMORY_SCOPE_AGENT); return true; }
    if (__builtin_amdgcn_readfirstlane(__hip_atomic_load(t, __ATOMIC_RELAXED, __HIP_MEMORY_SCOPE_AGENT)) == tm.epoch) return true;
  }
  return false;
}
#define MAPV 1
#define XW 10
DEV int map_rg(int b) { return ((b & 7) >> 1) * 4 + (b >> 6); }
DEV int map_cg(int b) { return (b & 1) * 8 + ((b >> 3) & 7); }
DEV int o_half(int cg) { return (cg >> 2) & 1; }
DEV int o_c8(int cg) { return (cg >> 3) * 4 + (cg & 3); }
DEV int xfeat(int c, int j) { int c8 = c / 10, l = c % 10; return 80 * c8 + (l < 8 ? 20 * (l >> 1) + 8 * (l & 1) + j : 40 * (l - 8) + 20 * (j >> 2) + 16 + (j & 3)); }
DEV int ocol(int cgp, int s, int j) { return 40 * cgp + (s < 4 ? 20 * (s >> 1) + 8 * (s & 1) + j : 20 * (j >> 2) + 16 + (j & 3)); }
DEV void pin_s(unsigned& v) { asm volatile("" : "+s"(v)); }
DEV int fresh_lane() { int l; asm volatile("v_mbcnt_lo_u32_b32 %0, -1, 0\n\tv_mbcnt_hi_u32_b32 %0, -1, %0" : "=v"(l)); return l; }
#define RELANE() const int lane = fresh_lane(), h = lane >> 5, tok = lane & 31, tid = w * 64 + lane; const unsigned l16 = lane * 16; (void)h; (void)tok; (void)tid; (void)l16
#define STAMP(i) do {} while (0)
#define LDS_ALLOC LDS_BYTES
#define CNT(i) do {} while (0)

DEV void publish_x(rsrc_t rs, unsigned xbuf, unsigned stbuf, int rg, int cg, int w, int lane, const unsigned (&xr)[10]) {
  const int g = lane >> 4, tok = lane & 15, half = o_half(cg), c8 = o_c8(cg);
  const unsigned sb = xbuf + ((rg * 4 + half * 2 + (w >> 1)) * 40 + 5 * c8) * 1024;
  const unsigned vb = ((w & 1) * 16 + tok) * 16;
  stg_sc1(rs, vb + g * 1024, sb, v4u{xr[0], xr[1], xr[2], xr[3]});
  stg_sc1(rs, vb + g * 1024 + 512, sb, v4u{xr[4], xr[5], xr[6], xr[7]});
  auto s0 = __builtin_amdgcn_permlane16_swap(xr[8], xr[8], false, false);
  auto s1 = __builtin_amdgcn_permlane16_swap(xr[9], xr[9], false, false);
  if (!(g & 1)) stg_sc1(rs, vb + 4096 + (g >> 1) * 512, sb, v4u{xr[8], xr[9], s0[1], s1[1]});
  SB();
  float sa = 0.f, sq = 0.f;
#pragma unroll
  for (int i = 0; i < 10; ++i) { sa = dot2(xr[i], 0x3F803F80u, sa); sq = dot2(xr[i], xr[i], sq); }
  { auto a = __builtin_amdgcn_permlane16_swap(f2u(sa), f2u(sa), false, false); sa = u2f(a[0]) + u2f(a[1]);
    auto b = __builtin_amdgcn_permlane16_swap(f2u(sq), f2u(sq), false, false); sq = u2f(b[0]) + u2f(b[1]); }
  { auto a = __builtin_amdgcn_permlane32_swap(f2u(sa), f2u(sa), false, false); sa = u2f(a[0]) + u2f(a[1]);
    auto b = __builtin_amdgcn_permlane32_swap(f2u(sq), f2u(sq), false, false); sq = u2f(b[0]) + u2f(b[1]); }
  if (g == 0) __builtin_amdgcn_raw_buffer_store_b64(v2u{f2u(sa), f2u(sq)}, rs, tok * 8, stbuf + ((rg * 8 + c8) * 128 + half * 64 + w * 16) * 8, SC1);
}
DEV void load_x_in(const u16* x, int rg, int cg, int w, int lane, unsigned (&xr)[10]) {
  const int g = lane >> 4, tok = lane & 15;
  const GAS v2u* src = (const GAS v2u*)(x + (size_t)(rg * 128 + o_half(cg) * 64 + w * 16 + tok) * 640 + o_c8(cg) * 80 + g * 20);
#pragma unroll
  for (int i = 0; i < 5; ++i) { v2u t = src[i]; xr[2 * i] = t.x; xr[2 * i + 1] = t.y; }
}

DEV void poison_x(rsrc_t rs, unsigned xbuf, unsigned stbuf, int rg, int cg, int w, int lane) {
  const int g = lane >> 4, tok = lane & 15, half = o_half(cg), c8 = o_c8(cg);
  const unsigned sb = xbuf + ((rg * 4 + half * 2 + (w >> 1)) * 40 + 5 * c8) * 1024, vb = ((w & 1) * 16 + tok) * 16;
  const v4u P = {SENT, SENT, SENT, SENT};
  stg_sc1(rs, vb + g * 1024, sb, P); stg_sc1(rs, vb + g * 1024 + 512, sb, P); stg_sc1(rs, vb + 4096 + (g >> 1) * 512, sb, P);
  __builtin_amdgcn_raw_buffer_store_b64(v2u{SENT, SENT}, rs, tok * 8, stbuf + ((rg * 8 + c8) * 128 + half * 64 + w * 16) * 8, SC1);
}
DEV void poison_o(rsrc_t rs, unsigned obuf, int rg, int cg, int w, int lane) {
  const int h = lane >> 5, tok = lane & 31;
  const unsigned os = obuf + ((rg * 8 + 2 * w) * 4 + (cg >> 2)) * 5120 + (cg & 3) * 256, ov = (tok >> 4) * 20480 + (tok & 15) * 16;
  const v4u P = {SENT, SENT, SENT, SENT};
  stg_sc1(rs, ov + h * 2048, os, P); stg_sc1(rs, ov + h * 2048 + 1024, os, P); stg_sc1(rs, ov + 4096, os, P);
}
DEV void wait_flags(rsrc_t rs, unsigned voff, unsigned soff, const Tmo& tm) {
  unsigned sp = 0, v;
  do { FENCE(); v = __builtin_amdgcn_raw_buffer_load_b32(rs, voff, soff, SC1); } while (__builtin_amdgcn_ballot_w64(v != tm.epoch) != 0 && !give_up(sp, tm));
}
DEV void build_wq_read(LAS unsigned char* L, unsigned stage, int w, int lane, v4u (&fr)[3]) {
  const int gam = lane >> 4, q = (lane >> 2) & 3, pp = lane & 3, a = 2 * (gam & 1) + (pp >> 1), h = pp & 1;
  const int eoff = (w < 3) ? 40 * w + 20 * h + 4 * a : (a == 0 ? 40 : a == 1 ? 80 : 0) + 20 * h + 16;
#pragma unroll
  for (int it = 0; it < 3; ++it) {
    const int kap = 2 * it + (gam >> 1), kc = kap < 4 ? kap : 4;
    const unsigned ad = stage + (kc * 8 + q) * 256 + eoff * 2;
    const v2u t0 = ldstr(L, ad), t1 = ldstr(L, ad + 1024);
    fr[it] = v4u{t0.x, t0.y, t1.x, t1.y};
  }
}
DEV float sum8(v4u v, float c) { c = dot2(v.x, 0x3F803F80u, c); c = dot2(v.y, 0x3F803F80u, c); c = dot2(v.z, 0x3F803F80u, c); return dot2(v.w, 0x3F803F80u, c); }
DEV void build_wq_store(rsrc_t rs, int m, int rg, int cg, int w, int lane, int r0, int r1, const v4u (&fr)[3]) {
  const int row = lane & 31, hi = lane >> 5;
  const unsigned tb = w == 0 ? L_P0 : (w - 1) * 1024, ts = w == 0 ? 1024 : KSTEP, th = w == 3 ? 384 : 512;
  const bool in = row >= r0 && row < r1;
#pragma unroll
  for (int it = 0; it < 3; ++it) {
    const int c = 5 * rg + 2 * it + hi;
    stg_sc1(rs, (in && (it < 2 || !hi)) ? (c >> 1) * ts + (c & 1) * th + row * 16 : 0x80000000u, WS_WQ + (m * 16 + cg) * WQ_SZ + tb, fr[it]);
  }
  float cs = sum8(fr[1], sum8(fr[0], 0.f));
  cs = hi ? cs : sum8(fr[2], cs);
  auto t = __builtin_amdgcn_permlane32_swap(f2u(cs), f2u(cs), false, false);
  __builtin_amdgcn_raw_buffer_store_b32(f2u(u2f(t[0]) + u2f(t[1])), rs, (in && !hi) ? row * 4 : 0x80000000u, WS_CSP + ((m * 16 + cg) * 16 + rg) * 512 + w * 128, SC1);
}
DEV void probe_issue(unsigned& d, rsrc_t rs, unsigned voff, unsigned soff) {
  asm volatile("buffer_load_dword %0, %1, %2, %3 offen sc1" : "=v"(d) : "v"(voff), "s"(rs), "s"(soff) : "memory");
}
template <int N> DEV void probe_wait(unsigned& d) { asm volatile("s_waitcnt vmcnt(%1)" : "+v"(d) : "i"(N) : "memory"); }
#define PSL 2
template <int R> DEV unsigned probe_turn(rsrc_t rs, unsigned voff, unsigned soff) {
  unsigned v;
  asm volatile("buffer_load_dword a%c4, %1, %2, %3 offen sc1\n\ts_waitcnt vmcnt(1)\n\tv_accvgpr_read_b32 %0, a%c5" : "=v"(v) : "v"(voff), "s"(rs), "s"(soff), "i"(R), "i"(R ^ 1) : "memory");
  return v;
}
template <int R> DEV void poll_flag(rsrc_t rs, unsigned voff, unsigned soff, const Tmo& tm) {
  unsigned sp = 0;
  asm volatile("buffer_load_dword a%c3, %0, %1, %2 offen sc1" :: "v"(voff), "s"(rs), "s"(soff), "i"(R) : "memory");
  for (;;) {
    __builtin_amdgcn_s_sleep(PSL);
    if (__builtin_amdgcn_ballot_w64(probe_turn<R + 1>(rs, voff, soff) != tm.epoch) == 0) break;
    __builtin_amdgcn_s_sleep(PSL);
    if (__builtin_amdgcn_ballot_w64(probe_turn<R>(rs, voff, soff) != tm.epoch) == 0) break;
    if (give_up(sp, tm)) break;
  }
}
template <int R> DEV void poll_probe(rsrc_t rs, unsigned voff, unsigned soff, const Tmo& tm) {
  unsigned sp = 0;
  asm volatile("buffer_load_dword a%c3, %0, %1, %2 offen sc1" :: "v"(voff), "s"(rs), "s"(soff), "i"(R) : "memory");
  for (;;) {
    __builtin_amdgcn_s_sleep(PSL);
    if (!any_sent(probe_turn<R + 1>(rs, voff, soff))) break;
    __builtin_amdgcn_s_sleep(PSL);
    if (!any_sent(probe_turn<R>(rs, voff, soff))) break;
    if (give_up(sp, tm)) break;
  }
}
template <int I> struct IC { static constexpr int v = I; };
template <int I, int N, class F> DEV void sfor(F&& f) { if constexpr (I < N) { f(IC<I>{}); sfor<I + 1, N>(f); } }
#define SFOR(var, n) sfor<0, n>([&](auto var##_) __attribute__((always_inline)) { constexpr int var = decltype(var##_)::v;
#define SEND })
template <bool NORM, class F> DEV float phi_map(const float (&a)[20], float s, float post, unsigned (&ph)[20], F slot) {
  float mx = max3f(a[0], a[1], a[2]), mn = min3f(a[0], a[1], a[2]), mx2 = max3f(a[3], a[4], a[5]), mn2 = min3f(a[3], a[4], a[5]);
#pragma unroll
  for (int i = 6; i < 18; i += 4) { mx = max3f(mx, a[i], a[i + 1]); mn = min3f(mn, a[i], a[i + 1]); mx2 = max3f(mx2, a[i + 2], a[i + 3]); mn2 = min3f(mn2, a[i + 2], a[i + 3]); }
  mx = max3f(mx, a[18], a[19]); mn = min3f(mn, a[18], a[19]); mx = max3f(mx, mx2, mx2); mn = min3f(mn, mn2, mn2);
  const float bp = -s * mx, bn = s * mn, ns = -s;
  float sp = 0.f;
  SFOR(sg, 2)
    const f32x2 sc = sg ? f32x2{ns, ns} : f32x2{s, s}, bi = sg ? f32x2{bn, bn} : f32x2{bp, bp};
    f32x2 e[10];
    slot(IC<5 * sg>{});
#pragma unroll
    for (int i = 0; i < 10; ++i) e[i] = pkfma(f32x2{a[2 * i], a[2 * i + 1]}, sc, bi);
    SB();
    SFOR(i, 10)
      if constexpr (i == 3 || i == 7) slot(IC<5 * sg + 1 + (i >> 2)>{});
      e[i] = f32x2{__builtin_amdgcn_exp2f(e[i][0]), __builtin_amdgcn_exp2f(e[i][1])};
    SEND;
    f32x2 s0 = pkadd(e[0], e[1]), s1 = pkadd(e[2], e[3]), s2 = pkadd(e[4], e[5]);
    s0 = pkadd(s0, e[6]); s1 = pkadd(s1, e[7]); s2 = pkadd(s2, e[8]); s0 = pkadd(s0, e[9]);
    slot(IC<5 * sg + 3>{});
    if constexpr (!NORM && sg == 0) {
#pragma unroll
      for (int i = 0; i < 10; ++i) ph[i] = pk2(e[i][0], e[i][1]);
    }
    s0 = pkadd(pkadd(s0, s1), s2);
    const float sm = s0[0] + s0[1];
    if constexpr (NORM || sg == 1) {
      const float r = __builtin_amdgcn_rcpf(sm) * (NORM ? post : sp);
#pragma unroll
      for (int i = 0; i < 10; ++i) e[i] = pkmul(e[i], f32x2{r, r});
      SB();
#pragma unroll
      for (int i = 0; i < 10; ++i) ph[10 * sg + i] = pk2(e[i][0], e[i][1]);
    }
    sp = sg ? sp : sm;
    slot(IC<5 * sg + 4>{});
  SEND;
  return sp;
}

#define BARE() asm volatile("s_barrier" ::: "memory")
template <int OFF> DEV void dsr(v4u& d, unsigned addr) { asm volatile("ds_read_b128 %0, %1 offset:%2" : "=v"(d) : "v"(addr), "i"(OFF)); }
template <int R, int OFF, bool COH> DEV void lda(rsrc_t rs, unsigned voff, unsigned soff) {
  if constexpr (COH) asm volatile("buffer_load_dwordx4 a[%c0:%c1], %2, %3, %4 offen offset:%5 sc1" :: "i"(R), "i"(R + 3), "v"(voff), "s"(rs), "s"(soff), "i"(OFF) : "memory");
  else asm volatile("buffer_load_dwordx4 a[%c0:%c1], %2, %3, %4 offen offset:%5" :: "i"(R), "i"(R + 3), "v"(voff), "s"(rs), "s"(soff), "i"(OFF) : "memory");
}
template <int R, int OFF> DEV void lda1(rsrc_t rs, unsigned voff, unsigned soff) {
  asm volatile("buffer_load_dword a%c0, %1, %2, %3 offen offset:%4 sc1" :: "i"(R), "v"(voff), "s"(rs), "s"(soff), "i"(OFF) : "memory");
}
template <int R> DEV float rda1() { float t; asm volatile("v_accvgpr_read_b32 %0, a%c1" : "=v"(t) : "i"(R)); return t; }
DEV void ldst(v2u (&st)[4], rsrc_t rs, unsigned voff, unsigned soff) {
  asm volatile("buffer_load_dwordx2 %0, %4, %5, %6 offen sc1\n\tbuffer_load_dwordx2 %1, %4, %5, %6 offen offset:1024 sc1\n\t"
               "buffer_load_dwordx2 %2, %4, %5, %6 offen offset:2048 sc1\n\tbuffer_load_dwordx2 %3, %4, %5, %6 offen offset:3072 sc1"
               : "=&v"(st[0]), "=&v"(st[1]), "=&v"(st[2]), "=&v"(st[3]) : "v"(voff), "s"(rs), "s"(soff) : "memory");
}
template <int N> DEV unsigned st_wait(v2u (&st)[4]) {
  asm volatile("s_waitcnt vmcnt(%4)" : "+v"(st[0]), "+v"(st[1]), "+v"(st[2]), "+v"(st[3]) : "i"(N) : "memory");
  return max3u(max3u(st[0].x, st[0].y, st[1].x), max3u(st[1].y, st[2].x, st[2].y), umax(st[3].x, st[3].y));
}
DEV void sbump(unsigned& so) { asm volatile("s_add_u32 %0, %0, 0x1000" : "+s"(so) :: "scc"); }
template <bool Z, int R> DEV void mf32a(f32x16& c, const v4u& a) {
  if constexpr (Z) asm volatile("v_mfma_f32_32x32x16_bf16 %0, %1, a[%c2:%c3], 0" : "=&v"(c) : "v"(a), "i"(R), "i"(R + 3));
  else asm volatile("v_mfma_f32_32x32x16_bf16 %0, %1, a[%c2:%c3], %0" : "+v"(c) : "v"(a), "i"(R), "i"(R + 3));
}
template <bool Z> DEV void mf32v(f32x16& c, const v4u& a, const v4u& b) {
  if constexpr (Z) asm volatile("v_mfma_f32_32x32x16_bf16 %0, %1, %2, 0" : "=&v"(c) : "v"(a), "v"(b));
  else asm volatile("v_mfma_f32_32x32x16_bf16 %0, %1, %2, %0" : "+v"(c) : "v"(a), "v"(b));
}
template <bool Z, int R> DEV void mf16a(f32x4& c, const v4u& b) {
  if constexpr (Z) asm volatile("v_mfma_f32_16x16x32_bf16 %0, a[%c2:%c3], %1, 0" : "=&v"(c) : "v"(b), "i"(R), "i"(R + 3));
  else asm volatile("v_mfma_f32_16x16x32_bf16 %0, a[%c2:%c3], %1, %0" : "+v"(c) : "v"(b), "i"(R), "i"(R + 3));
}
template <bool Z> DEV void mf16v(f32x4& c, const v4u& a, const v4u& b) {
  if constexpr (Z) asm volatile("v_mfma_f32_16x16x32_bf16 %0, %1, %2, 0" : "=&v"(c) : "v"(a), "v"(b));
  else asm volatile("v_mfma_f32_16x16x32_bf16 %0, %1, %2, %0" : "+v"(c) : "v"(a), "v"(b));
}
template <int N> DEV void vmc() { asm volatile("s_waitcnt vmcnt(%0)" :: "i"(N) : "memory"); }
template <int I> DEV void dmaw(rsrc_t rw, unsigned l16, unsigned wb) {
  asm volatile("s_or_b32 m0, %0, %1\n\ts_nop 0\n\tbuffer_load_dwordx4 %2, %3, m0 offen" DSC1 " lds" :: "s"(wb), "i"(I * 4096), "v"(l16), "s"(rw) : "memory", "scc");
}
template <int N> DEV void lgkm() { asm volatile("s_waitcnt lgkmcnt(%0)" :: "i"(N)); }
#define MFMA_SETTLE() asm volatile("s_nop 7\n\ts_nop 3")
DEV bool any_nan(float x) { return __builtin_amdgcn_ballot_w64(x != x) != 0; }
#define PFD 3

template <int S, int NW, int VM, int VI> DEV void p2pair(f32x16& a0, v4u& w0, v4u& w1, unsigned bq, unsigned bcs, rsrc_t r, unsigned l16, unsigned so, const v4u& pv) {
#define P2_M0 "s_waitcnt lgkmcnt(%c[nw])\n\tv_mfma_f32_32x32x16_bf16 %[c], %[w0], a[%c[r0]:%c[r1]], "
#define P2_R0 "\n\tds_read_b128 %[w0], %[a0] offset:%c[o0]\n\t"
#define P2_M1 "v_mfma_f32_32x32x16_bf16 %[c], %[w1], a[%c[r2]:%c[r3]], %[c]"
#define P2_R1 "\n\tds_read_b128 %[w1], %[bq] offset:%c[o1]"
#define P2_IN [nw] "i"(NW), [r0] "i"(4 * S), [r1] "i"(4 * S + 3), [r2] "i"(4 * S + 4), [r3] "i"(4 * S + 7), [a0] "v"(S + 6 == 40 ? bcs : bq), [bq] "v"(bq), \
              [o0] "i"(S + 6 < 40 ? (S + 6) * 1024 : 0), [o1] "i"((S + 7) * 1024), [rs] "s"(r), [l16] "v"(l16), [so] "s"(so), [d0] "i"(VM == 1 ? 160 + 4 * VI : 0), [d1] "i"(VM == 1 ? 163 + 4 * VI : 3), \
              [vo] "i"((VI & 3) * 1024), [dm] "i"(VM == 2 ? VI * 4096 : VM == 3 ? VI : 0), [pv] "v"(pv), [p2] "v"(v2u{pv.x, pv.y})
#define P2_LD "buffer_load_dwordx4 a[%c[d0]:%c[d1]], %[l16], %[rs], %[so] offen offset:%c[vo] sc1\n\t"
#define P2_DMA "s_or_b32 m0, %[so], %[dm]\n\t"
#define P2_DMB "buffer_load_dwordx4 %[l16], %[rs], m0 offen" DSC1 " lds\n\t"
#define P2_ST "buffer_store_dwordx4 %[pv], %[l16], %[rs], %[so] offen offset:%c[dm] sc1\n\t"
#define P2_S2 "buffer_store_dwordx2 %[p2], %[l16], %[rs], %[so] offen sc1\n\t"
  if constexpr (S == 0) {
    static_assert(S != 0 || VM == 0, "");
    asm volatile(P2_M0 "0" P2_R0 P2_M1 P2_R1 : [c] "=&v"(a0), [w0] "+v"(w0), [w1] "+v"(w1) : P2_IN : "memory");
  } else if constexpr (S + 7 < 40) {
    if constexpr (VM == 0) asm volatile(P2_M0 "%[c]" P2_R0 P2_M1 P2_R1 : [c] "+v"(a0), [w0] "+v"(w0), [w1] "+v"(w1) : P2_IN : "memory");
    else if constexpr (VM == 1) asm volatile(P2_M0 "%[c]" P2_R0 P2_LD P2_M1 P2_R1 : [c] "+v"(a0), [w0] "+v"(w0), [w1] "+v"(w1) : P2_IN : "memory");
    else if constexpr (VM == 3) asm volatile(P2_M0 "%[c]" P2_R0 P2_ST P2_M1 P2_R1 : [c] "+v"(a0), [w0] "+v"(w0), [w1] "+v"(w1) : P2_IN : "memory");
    else if constexpr (VM == 4) asm volatile(P2_M0 "%[c]" P2_R0 P2_S2 P2_M1 P2_R1 : [c] "+v"(a0), [w0] "+v"(w0), [w1] "+v"(w1) : P2_IN : "memory");
    else asm volatile(P2_M0 "%[c]\n\t" P2_DMA "ds_read_b128 %[w0], %[a0] offset:%c[o0]\n\t" P2_DMB P2_M1 P2_R1 : [c] "+v"(a0), [w0] "+v"(w0), [w1] "+v"(w1) : P2_IN : "memory", "scc");
  } else if constexpr (S + 6 == 40) {
    static_assert(S + 6 != 40 || VM == 0, "");
    asm volatile(P2_M0 "%[c]" P2_R0 P2_M1 : [c] "+v"(a0), [w0] "+v"(w0), [w1] "+v"(w1) : P2_IN : "memory");
  } else {
    static_assert(VM == 0, "");
    asm volatile(P2_M0 "%[c]\n\t" P2_M1 : [c] "+v"(a0), [w0] "+v"(w0), [w1] "+v"(w1) : P2_IN : "memory");
  }
}
template <bool Z, int RA, int RB> DEV void mf16aa(f32x4& c) {
  if constexpr (Z) asm volatile("v_mfma_f32_16x16x32_bf16 %0, a[%c1:%c2], a[%c3:%c4], 0" : "=v"(c) : "i"(RA), "i"(RA + 3), "i"(RB), "i"(RB + 3));
  else asm volatile("v_mfma_f32_16x16x32_bf16 %0, a[%c1:%c2], a[%c3:%c4], %0" : "+v"(c) : "i"(RA), "i"(RA + 3), "i"(RB), "i"(RB + 3));
}
template <int RB> DEV void mf16va(f32x4& c, const v4u& a) { asm volatile("v_mfma_f32_16x16x32_bf16 %0, %1, a[%c2:%c3], %0" : "+v"(c) : "v"(a), "i"(RB), "i"(RB + 3)); }
template <bool Z, int RB> DEV void mf16vz(f32x4& c, const v4u& a) {
  if constexpr (Z) asm volatile("v_mfma_f32_16x16x32_bf16 %0, %1, a[%c2:%c3], 0" : "=&v"(c) : "v"(a), "i"(RB), "i"(RB + 3));
  else asm volatile("v_mfma_f32_16x16x32_bf16 %0, %1, a[%c2:%c3], %0" : "+v"(c) : "v"(a), "i"(RB), "i"(RB + 3));
}
#define DSW(addr, off, val) asm volatile("ds_write_b128 %0, %1 offset:%2" :: "v"(addr), "v"(val), "i"(off) : "memory")
#define ADM 12
#define ODM 0
#define GDM 10
#define DLY_O 24
#define DLY_X 13
#define HLP 17
#define DLY_OB 28
#define KFP 12
#define KCK 3
#define LW 8
#define DX1 8
#define DO1 12
#define PX 1
#define P2W 13
#define KPS 8

__global__ void __launch_bounds__(256, 1) k_main(Params p) {
  extern __shared__ __attribute__((aligned(16))) unsigned char smem[];
  LAS unsigned char* L = (LAS unsigned char*)smem;
  {
    const unsigned long long pc = __builtin_amdgcn_s_getpc();
    const unsigned t16 = __builtin_amdgcn_workitem_id_x() * 16;
    asm volatile("global_load_dwordx4 a[0:3], %0, %1\n\tglobal_load_dwordx4 a[4:7], %0, %1 offset:4080" :: "v"(t16), "s"(pc) : "memory");
    asm volatile("global_load_dwordx4 a[8:11], %0, %1" :: "v"(t16 + 8192 + ((__builtin_amdgcn_workgroup_id_x() >> 3) % 6) * 4096), "s"(pc) : "memory");
    SB();
  }
  const int b = __builtin_amdgcn_workgroup_id_x(), w = __builtin_amdgcn_readfirstlane(__builtin_amdgcn_workitem_id_x() >> 6);
  const int rg = map_rg(b), cg = map_cg(b), half = o_half(cg), c8 = o_c8(cg);
  const rsrc_t rs = __builtin_amdgcn_make_buffer_rsrc(p.ws, 0, WS_TOTAL, 0x00020000);
  asm volatile("" ::: "a255");
  unsigned vKa, vVa, vHv, vImg, vA0, vA2, vB, vS0, vS1, vK0, vK1, vF0, vF1, vKv, vKp, vOv, vOp, vXv, vSt, vB3, vK2, vK3, vO3, vX3, vSp, vA0x, vA2x, vBx;
  v4u vSel;
  auto addresses = [&]() __attribute__((always_inline)) {
    RELANE();
    const int g = lane >> 4, hh = w >> 1, dt = w & 1, q = (lane >> 2) & 3, p4 = lane & 3, r = lane & 15;
    const unsigned r_ = h * 144 + 2 + 32 * w + tok;
    vKa = L_K + r_ * 48; vVa = L_V + r_ * 80; vHv = (w == 3 && tok >= 16) ? (h * 16 + (tok - 16)) * 16 : 0x80000000u;
    const unsigned t_ = 32 * w + tok, key = (t_ & 7) ^ (((t_ >> 3) & 1) << 2);
    vImg = L_SCR + (h * 128 + t_) * 128 + (key << 4);
    const unsigned k0 = q ^ ((g & 1) << 2), rb0 = L_SCR + (hh * 128 + 8 * g + q) * 128, cB = dt ? 7 : 5 + (p4 >> 1);
    vA0 = rb0 + ((p4 ^ k0) << 4); vA2 = rb0 + ((4 ^ k0) << 4) + (p4 & 1) * 8; vB = rb0 + ((cB ^ k0) << 4) + (p4 & 1) * 8;
    vS0 = dt ? (r < 8 ? 2560 + ((hh * 5 + g) * 8 + r) * 16 : 0x80000000u) : ((hh * 5 + g) * 16 + r) * 16;
    vS1 = g ? 0x80000000u : dt ? (r < 8 ? 2560 + ((hh * 5 + 4) * 8 + r) * 16 : 0x80000000u) : ((hh * 5 + 4) * 16 + r) * 16;
    const unsigned ty0 = L_KVA + r * 16 + g * 8, ty1 = L_KVA + (r >> 3) * 400 + 256 + (r & 7) * 16 + g * 8;
    vK0 = w < 3 ? ty0 + w * 1600 : ty1 + 800;
    vK1 = w < 2 ? ty0 + w * 1600 + 800 : ty1 + (w == 2 ? 0 : 2400);
    if (h) vK0 = vK1 = L_RED + 8192 + tid * 8;
    vK2 = vK0 + (w < 3 ? 400 : 800); vK3 = vK1 + (w < 2 ? 400 : 800);
    vA0x = (vA0 ^ 64) + 512; vA2x = (vA2 ^ 64) + 512; vBx = (vB ^ 64) + 512;
#pragma unroll
    for (int j = 0; j < 4; ++j) vSel[j] = r == 2 * j ? 0x00003F80u : r == 2 * j + 1 ? 0x3F800000u : 0u;
    const bool match = ((tok >> 2) & 1) == h;
    const unsigned row = 4 * (tok >> 3) + (tok & 3);
    vF0 = L_KVA + h * 2000 + (match ? row * 16 : 384); vF1 = L_KVA + h * 2000 + (match && row < 8 ? 256 + row * 16 : 384);
    vKv = g * 4096 + r * 16;
    vKp = (lane >> 2) * 4096 + 3712 + (lane & 3) * 4;
    vOv = (tok >> 4) * 20480 + (tok & 15) * 16 + h * 2048; vOp = g * 20480 + q * 256 + p4 * 4;
    vXv = ((w & 1) * 16 + r) * 16 + g * 1024; vSt = h * 4096 + tok * 8; vB3 = 2048 + h * 384 + tok * 16;
    vO3 = h ? 0x80000000u : vOv; vX3 = (g & 1) ? 0x80000000u : (vXv & 0x1f0) + (h << 9); vSp = g ? 0x80000000u : r * 8;
    asm volatile("" : "+v"(vK2), "+v"(vK3), "+v"(vO3), "+v"(vX3), "+v"(vSp), "+v"(vA0x), "+v"(vA2x), "+v"(vBx), "+v"(vSel));
    asm volatile("" : "+v"(vKa), "+v"(vVa), "+v"(vHv), "+v"(vImg), "+v"(vA0), "+v"(vA2), "+v"(vB), "+v"(vS0), "+v"(vS1), "+v"(vK0));
    asm volatile("" : "+v"(vK1), "+v"(vF0), "+v"(vF1), "+v"(vKv), "+v"(vKp), "+v"(vOv), "+v"(vOp), "+v"(vXv), "+v"(vSt), "+v"(vB3));
    if (tid < 10) sts128(L, L_KVA + tid * 400 + 384, v4u{0u, 0u, 0u, 0u});
  };
  unsigned xr[10];
  Tmo tm;
  { RELANE();
    const int l = 0; (void)l;
    STAMP(0);
    constexpr unsigned SG_B = L_P0 + 2048, SG_O = SG_B + 10240, SG_P = SG_O + 10240, OOR = 0x80000000u;
    const int j = (tid >> 4) & 7, cc = lane & 15, seg = (cc * 13) >> 6, g = lane >> 4;
    v4u ra[3], rb[3], ro[3], rp;
    const unsigned wcol = (seg * 640 + 40 * cg + 8 * (cc - 5 * seg)) * 2;
    unsigned wrow[3];
#pragma unroll
    for (int i = 0; i < 3; ++i) {
      const int kap = 2 * i + (w >> 1) < 4 ? 2 * i + (w >> 1) : 4, lc = 5 * (rg & 1) + kap;
      const int t8 = lc >> 3, rb0 = 80 * (rg >> 1) + (1 - t8) * (20 * (lc >> 1) + 8 * (lc & 1)) + t8 * (40 * (lc - 8) + 16);
      wrow[i] = (rb0 + j + t8 * (16 * (j >> 2))) * 3840 + wcol;
    }
    { const rsrc_t rA = __builtin_amdgcn_make_buffer_rsrc((void*)p.wqA, 0, 640 * 1920 * 2, 0x00020000);
#pragma unroll
      for (int i = 0; i < 3; ++i) ra[i] = ldg(rA, wrow[i], 0); }
    load_x_in(p.x, rg, cg, w, lane, xr);
    const unsigned ep = __builtin_amdgcn_raw_buffer_load_b32(rs, 0, WS_CTRL + 128, SC1);
    FENCE(); SB();
    { const rsrc_t rB = __builtin_amdgcn_make_buffer_rsrc((void*)p.wqB, 0, 640 * 1920 * 2, 0x00020000);
      const rsrc_t rO = __builtin_amdgcn_make_buffer_rsrc((void*)(half ? p.woB : p.woA), 0, 640 * 640 * 2, 0x00020000);
      const rsrc_t rP = __builtin_amdgcn_make_buffer_rsrc((void*)(p.wphi + cg * 800), 0, 1600, 0x00020000);
#pragma unroll
      for (int i = 0; i < 3; ++i) rb[i] = ldg(rB, wrow[i], 0);
#pragma unroll
      for (int i = 0; i < 3; ++i) { const int so = 2 * i + (w >> 1) < 4 ? 2 * i + (w >> 1) : 4, t4 = so >> 2;
        ro[i] = ldg(rO, (40 * rg + (1 - t4) * (20 * (so >> 1) + 8 * (so & 1)) + t4 * 16 + j + t4 * (16 * (j >> 2))) * 1280 + (80 * c8 + 8 * (cc < 10 ? cc : 0)) * 2, 0); }
      rp = ldg(rP, tid * 16, 0);
    FENCE(); SB();
    STAMP(53);
    const unsigned sa = tid * 16;
#pragma unroll
    for (int i = 0; i < 3; ++i) if (i < 2 || w < 2) sts128(L, sa + i * 4096, ra[i]);
    BAR();
    STAMP(54);
    { v4u fr[3];
      build_wq_read(L, 0, w, lane, fr);
      publish_x(rs, WS_X, WS_ST0, rg, cg, w, lane, xr);
      build_wq_store(rs, 0, rg, cg, w, lane, 0, w < 3 ? 32 : 24, fr); }
    tm.ws = p.ws; tm.epoch = __builtin_amdgcn_readfirstlane(ep) + 0x9E3779B1u;
    STAMP(55);
#pragma unroll
    for (int i = 0; i < 3; ++i) if (i < 2 || w < 2) { sts128(L, SG_B + sa + i * 4096, rb[i]); sts128(L, SG_O + sa + i * 4096, ro[i]); }
    if (w < 2) sts128(L, SG_P + sa, rp);
    }
    DRAIN();
    BAR();
    if (tid == 0) __builtin_amdgcn_raw_buffer_store_b32(tm.epoch, rs, 0, WS_FLAG + (cg * 16 + rg) * 128, SC1);
    STAMP(60);
    v4u fv[4], fb[3], fo[2];
    {
      const int sg = w >> 1, hw = w & 1;
      v4u bf[2], af[4];
#pragma unroll
      for (int ct = 0; ct < 2; ++ct) {
        const int q = (lane >> 2) & 3, pp = lane & 3, fcol = ct ? 16 : 4 * pp;
        const int d0 = 8 * g + q < 19 ? 8 * g + q : 19, d1 = 8 * g + 4 + q < 19 ? 8 * g + 4 + q : 19;
        const v2u t0 = ldstr(L, SG_P + hw * 800 + d0 * 40 + fcol * 2), t1 = ldstr(L, SG_P + hw * 800 + d1 * 40 + fcol * 2);
        bf[ct] = v4u{g < 3 ? t0.x : 0u, g < 3 ? t0.y : 0u, g < 2 ? t1.x : 0u, g < 2 ? t1.y : 0u};
      }
#pragma unroll
      for (int i = 0; i < 4; ++i) { const int r = (i < 2 ? 8 * (cc >> 2) : 32) + 4 * (i & 1) + (cc & 3); const unsigned a = SG_B + r * 256 + sg * 80 + hw * 40 + g * 16;
        const v2u t0 = lds64(L, a), t1 = lds64(L, a + 8); af[i] = v4u{t0.x, t0.y, t1.x, t1.y}; }
      build_wq_read(L, SG_B, w, lane, fb);
      { const int q = (lane >> 2) & 3, pp = lane & 3;
#pragma unroll
        for (int it = 0; it < 2; ++it) {
          const int pc = it * 16 + (tid >> 4), pcc = pc < 25 ? pc : 24, so = (pcc * 13) >> 6, ft = pcc - 5 * so;
          const unsigned a = SG_O + (so * 8 + q) * 256 + 40 * pp + 8 * ft;
          const v2u t0 = ldstr(L, a), t1 = ldstr(L, a + 1024);
          fo[it] = v4u{t0.x, t0.y, t1.x, t1.y};
        } }
      f32x4 d[8];
      asm volatile("s_nop 1" : "+v"(bf[0]), "+v"(bf[1]), "+v"(af[0]), "+v"(af[1]), "+v"(af[2]), "+v"(af[3]));
      SFOR(i, 8) mf16v<true>(d[i], af[i & 3], bf[i >> 2]); SEND;
      asm volatile("s_nop 7");
      SB();
#pragma unroll
      for (int i = 0; i < 4; ++i) fv[i] = v4u{pk2(d[2 * i][0], d[2 * i][1]), pk2(d[2 * i][2], d[2 * i][3]), pk2(d[2 * i + 1][0], d[2 * i + 1][1]), pk2(d[2 * i + 1][2], d[2 * i + 1][3])};
    }
    STAMP(58);
    { const int sg = w >> 1, hw = w & 1;
#pragma unroll
      for (int ct = 0; ct < 2; ++ct) {
        const int row = ct ? (sg ? 0 : 16) + 4 * hw + (cc & 3) : 8 * (cc >> 2) + 4 * hw + (cc & 3);
        const unsigned tb = ct ? 2048 : (sg ? 0 : L_P0), ts = (ct || sg) ? KSTEP : 1024, th = ct ? 384 : 512;
        const bool in = ct == 0 || cc < 4;
#pragma unroll
        for (int u = 0; u < 2; ++u) { const int c = 5 * rg + (u ? 4 : g);
          stg_sc1(rs, (in && (u == 0 || g == 0)) ? (c >> 1) * ts + (c & 1) * th + row * 16 : OOR, WS_WQ + (16 + cg) * WQ_SZ + tb, fv[2 * ct + u]); }
        float cs = sum8(fv[2 * ct], 0.f);
        cs = g ? cs : sum8(fv[2 * ct + 1], cs);
        { auto t = __builtin_amdgcn_permlane16_swap(f2u(cs), f2u(cs), false, false); cs = u2f(t[0]) + u2f(t[1]); }
        { auto t = __builtin_amdgcn_permlane32_swap(f2u(cs), f2u(cs), false, false); cs = u2f(t[0]) + u2f(t[1]); }
        __builtin_amdgcn_raw_buffer_store_b32(f2u(cs), rs, (in && g == 0) ? row * 4 : OOR, WS_CSP + ((16 + cg) * 16 + rg) * 512 + (ct ? 3 : sg) * 128, SC1);
      }
      if (w >= 2) build_wq_store(rs, 1, rg, cg, w, lane, w == 2 ? 0 : 8, w == 2 ? 32 : 16, fb);
#pragma unroll
      for (int it = 0; it < 2; ++it) { const int pc = it * 16 + (tid >> 4);
        stg_sc1(rs, pc < 25 ? pc * 1024 + cc * 16 : OOR, WS_WO + (half * 8 + c8) * WO_SZ + (rg >> 2) * 25600 + (rg & 3) * 256, fo[it]); }
    }
    poison_o(rs, WS_O, rg, cg, w, lane);
    stg_sc1(rs, l16, WS_KV + (cg * 16 + rg) * 4096 + w * 1024, v4u{SENT, SENT, SENT, SENT});
    stg_sc1(rs, l16, WS_HALO + (rg * 16 + cg) * 4096 + w * 1024, v4u{SENT, SENT, SENT, SENT});
    addresses();
    STAMP(61);
    if (w == 1) wait_flags(rs, (h == 0 ? cc * 16 + rg : cg * 16 + cc) * 128, WS_FLAG, tm);
    BAR();
    STAMP(62);
    const rsrc_t rw = __builtin_amdgcn_make_buffer_rsrc(p.ws + WS_WQ + cg * WQ_SZ, 0, WQ_SZ, 0x00020000);
    SFOR(i, 22 + LW) dmaw<i>(rw, l16, w * 1024); SEND;
    float csv[16];
#pragma unroll
    for (int r = 0; r < 16; ++r) csv[r] = u2f(__builtin_amdgcn_raw_buffer_load_b32(rs, (tid & 127) * 4 + (r & 7) * 512, WS_CSP + cg * 8192 + (r >> 3) * 4096, SC1));
    FENCE(); SB();
    STAMP(63);
    FENCE(); SB();
    STAMP(57);
    { float cs = 0.f;
#pragma unroll
      for (int r = 0; r < 16; ++r) cs += csv[r];
      const unsigned ch = pk2(cs, 0.f) & 0xffffu, cl = pk2(cs - lo16(ch), 0.f) & 0xffffu;
      asm volatile("" :: "v"(cs));
      if (tid < 128) sts128(L, L_CS + tid * 16, v4u{ch | (cl << 16), ch, 0u, 0u}); }
    BAR();
    if (tid == 0) __builtin_amdgcn_raw_buffer_store_b32(tm.epoch, rs, 0, WS_FLAG + (256 + cg * 16 + rg) * 128, SC1);
  }

  unsigned sX = WS_X + (rg * 4 + w) * 40960, sSt = WS_ST0 + (rg * 1024 + w * 32) * 8, sWo = WS_WO + (c8 * 4 + w) * 25600;
  unsigned sOp = WS_O + ((rg * 8 + 2 * w) * 4 + (cg >> 2)) * 5120 + (cg & 3) * 256, sOr = WS_O + ((rg * 8 + half * 4) * 4 + w) * 5120;
  unsigned sXp = WS_X + XB + ((rg * 4 + half * 2 + (w >> 1)) * 40 + 5 * c8) * 1024, sSp = WS_ST1 + ((rg * 8 + c8) * 128 + half * 64 + w * 16) * 8;
  pin_s(sX); pin_s(sSt); pin_s(sWo); pin_s(sOp); pin_s(sOr); pin_s(sXp); pin_s(sSp);
  auto layer = [&](auto m_, const int ll) __attribute__((always_inline)) {
    constexpr int m = decltype(m_)::v;
    const int l = 2 * ll + m;
    const rsrc_t rw = __builtin_amdgcn_make_buffer_rsrc(p.ws + WS_WQ + ((m ^ 1) * 16 + cg) * WQ_SZ, 0, l < 3 ? WQ_SZ : 0u, 0x00020000);
    const unsigned wb1k = w * 1024;
#define DM(i) do { SB(); dmaw<(i)>(rw, l16, wb1k); SB(); } while (0)
    unsigned wos;
#define LDWO(i) do { lda<160 + 4 * (i), ((i) & 3) * 1024, true>(rs, l16, wos); if constexpr (((i) & 3) == 3) sbump(wos); } while (0)
#define P2VM(pp) ((pp) >= 2 && (pp) <= 16 && (pp) != 6 && (pp) != 11 ? 1 : 0)
#define P2VI(pp) (P2VM(pp) ? 7 + (pp) - 2 - ((pp) > 6) - ((pp) > 11) : 0)
#define P2CNT(pp) ((pp) < 2 ? 0 : (pp) - 1 - ((pp) >= 6) - ((pp) >= 11))
#define P2PAIR(pp, nw) do { p2pair<2 * (pp), nw, P2VM(pp), P2VI(pp)>(a0, wq[(2 * (pp)) % 6], wq[(2 * (pp) + 1) % 6], bq, bcq, rs, l16, wos, mfr); \
                            if constexpr (P2VM(pp) == 1 && (P2VI(pp) & 3) == 3) sbump(wos); } while (0)
    v4u wo24, mfr, wq[6];
    unsigned bq, bcq;
    f32x16 a0, a1, a2, a3;
    float rstd, sd, o[20];
    unsigned osb[4], ops, cXp, cSp, cOp, cWo;
    {
      RELANE();
      STAMP(1 + 12 * l);
      asm volatile("" : "+v"(vSt), "+v"(vB3));
      const unsigned xs = sX, sts = sSt;
#define LDX(s) do { lda<4 * (s), ((s) & 3) * 1024, true>(rs, l16, xso); if constexpr (((s) & 3) == 3) sbump(xso); } while (0)
      const unsigned stv = vSt;
      constexpr int NB = PFD + 1, HI = 23;
      constexpr int XR = 15 - XW;
      const unsigned b12 = l16, b3 = vB3;
      unsigned b12h, b3h, bcs;
      unsigned f1 = 0, f1v = 0;
      const rsrc_t rl = __builtin_amdgcn_make_buffer_rsrc(p.ws + WS_WQ + (m * 16 + cg) * WQ_SZ, 0, WQ_SZ, 0x00020000);
      auto pass1 = [&](auto hot_) __attribute__((always_inline)) {
        constexpr bool HOT = decltype(hot_)::v;
        v2u st[4];
        unsigned xso;
        auto window = [&](auto bar_) __attribute__((always_inline)) {
          xso = xs;
          ldst(st, rs, stv, sts);
          SFOR(s, XW) SB(); LDX(s); if constexpr (decltype(bar_)::v && (s == 1 || s == 5)) { SB(); BARE(); } SEND;
          FENCE(); SB();
        };
        wos = sWo;
        window(IC<HOT>{});
        v4u wb[NB][3];
        auto rd = [&](auto s_, auto t_) __attribute__((always_inline)) {
          constexpr int s = decltype(s_)::v, t = decltype(t_)::v;
          if constexpr (s < 40) dsr<(s < HI ? s : s - HI) * KSTEP + (t == 1 ? 1024 : 0)>(wb[s % NB][t], t == 2 ? (s < HI ? b3 : b3h) : (s < HI ? b12 : b12h));
          else if constexpr (s == 40) dsr<(t + 1) * 512>(wb[s % NB][t], bcs);
        };
        SFOR(j, PFD) rd(IC<j>{}, IC<2>{}); rd(IC<j>{}, IC<1>{}); rd(IC<j>{}, IC<0>{}); SEND;
        if (__builtin_expect(any_sent(st_wait<XW>(st)), 0)) {
          unsigned n = 0;
          do { CNT(0); poll_probe<200>(rs, (lane >> 3) * 1024 + (lane & 4) * 32 + 4, sts, tm); window(IC<0>{}); } while (any_sent(st_wait<XW>(st)) && ++n < 64);
        }
        if (HOT && m == 1 && l == 1 && w < 2) {
          float cs = 0.f;
          SFOR(r, 16) cs += rda1<210 + r>(); SEND;
          const unsigned ch = pk2(cs, 0.f) & 0xffffu, cl = pk2(cs - lo16(ch), 0.f) & 0xffffu;
          sts128(L, L_CS + 2048 + tid * 16, v4u{ch | (cl << 16), ch, 0u, 0u});
        }
        float sa, sq, nm;
        auto stat = [&](auto s_) __attribute__((always_inline)) {
          constexpr int s = decltype(s_)::v;
          if constexpr (s == 1) { sa = u2f(st[0].x) + u2f(st[1].x); asm volatile("" : "+v"(sa)); sq = u2f(st[0].y) + u2f(st[1].y); asm volatile("" : "+v"(sq)); }
          if constexpr (s == 2) { sa += u2f(st[2].x); asm volatile("" : "+v"(sa)); sq += u2f(st[2].y); asm volatile("" : "+v"(sq)); }
          if constexpr (s == 3) { sa += u2f(st[3].x); asm volatile("" : "+v"(sa)); sq += u2f(st[3].y); asm volatile("" : "+v"(sq)); }
          if constexpr (s == 5) { auto x0 = __builtin_amdgcn_permlane32_swap(f2u(sa), f2u(sa), false, false); nm = u2f(x0[0]) + u2f(x0[1]); asm volatile("" : "+v"(nm)); }
          if constexpr (s == 6) { auto x1 = __builtin_amdgcn_permlane32_swap(f2u(sq), f2u(sq), false, false); sq = u2f(x1[0]) + u2f(x1[1]); asm volatile("" : "+v"(sq)); }
          if constexpr (s == 7) { nm *= -1.f / 640.f; sq = sq * (1.f / 640.f) - nm * nm; asm volatile("" : "+v"(nm), "+v"(sq)); }
          if constexpr (s == 8) { sq = __builtin_fmaxf(sq, 0.f) + 1e-5f; rstd = __builtin_amdgcn_rsqf(sq); asm volatile("" : "+v"(rstd), "+v"(sq)); }
          if constexpr (s == 9) { sd = sq * rstd; mfr.x = pk2(nm, nm); asm volatile("" : "+v"(sd), "+v"(mfr.x)); }
          if constexpr (s == 10) { mfr.y = pk2(nm - lo16(mfr.x), 0.f); asm volatile("" : "+v"(mfr.y)); }
          if constexpr (s == 11) { mfr = v4u{h ? 0u : mfr.x, h ? 0u : mfr.y, 0u, 0u}; asm volatile("" : "+v"(mfr)); }
          if constexpr (s == 12) { b12h = b12 + HI * KSTEP; b3h = b3 + HI * KSTEP; asm volatile("" : "+v"(b12h), "+v"(b3h)); }
          if constexpr (s == 13) { bcs = L_CS + m * 2048 + (l16 & 0x1f0); bq = L_P0 + l16; asm volatile("" : "+v"(bcs), "+v"(bq)); bcq = bcs; }
        };
        SFOR(s, 40)
          SB();
          if constexpr (HOT && m == 0 && s == 27) {
            if (l == 0) { const int ln = fresh_lane(); f1v = (ln < 16 ? ln * 16 + rg : (ln < 32 ? cg : (c8 >> 2) * 8 + ((ln >> 4) & 1) * 4 + (c8 & 3)) * 16 + (ln & 15)) * 128; probe_issue(f1, rs, f1v, WS_FLAG + 256 * 128); }
            SB();
          }
          if constexpr (HOT && m == 0 && s == 33) {
            if (l == 0) {
              probe_wait<6>(f1);
              if (__builtin_amdgcn_ballot_w64(f1 != tm.epoch) != 0) wait_flags(rs, f1v, WS_FLAG + 256 * 128, tm);
            }
            SB();
          }
          if constexpr (HOT && s == 10) { BARE(); SB(); }
          constexpr int c = s % NB;
          constexpr int issued = s <= XR ? XW + 2 * s : (15 + s < 40 ? 15 + s : 40);
          asm volatile("s_waitcnt vmcnt(%0) lgkmcnt(%1)" :: "i"(issued - 1 - s + (s > 25 ? s - 25 : 0)), "i"(s == 39 ? 6 : 3 * ((s + PFD <= 41 ? PFD : 41 - s) - 1)) : "memory");
          mf32a<s == 0, 4 * s>(a1, wb[c][0]);
          if constexpr (HOT && s == 0) STAMP(2 + 12 * l);
          rd(IC<s + PFD>{}, IC<2>{});
          if constexpr (s < XR) LDX(issued);
          SB();
          mf32a<s == 0, 4 * s>(a2, wb[c][1]);
          rd(IC<s + PFD>{}, IC<1>{});
          stat(IC<s>{});
          SB();
          mf32a<s == 0, 4 * s>(a3, wb[c][2]);
          rd(IC<s + PFD>{}, IC<0>{});
                    if constexpr (s < 25) LDX(issued + (s < XR ? 1 : 0));
          else if constexpr (s < 32) dmaw<30 + (s >= 25 ? s - 25 : 0)>(rl, l16, wb1k);
          else if constexpr (s == 32) dmaw<0>(rl, l16, wb1k + (w < 2 ? 37 : 36) * 4096);
          else LDWO(s - 33);
          if constexpr (s >= 38) { dsr<(s - 38) * 3072>(wq[(s - 38) * 3], bq); dsr<(s - 38) * 3072 + 1024>(wq[(s - 38) * 3 + 1], bq); dsr<(s - 38) * 3072 + 2048>(wq[(s - 38) * 3 + 2], bq); }
        SEND;
        SB();
        lgkm<6>();
        mf32v<false>(a1, wb[40 % NB][0], mfr); mf32v<false>(a3, wb[40 % NB][2], mfr); mf32v<false>(a2, wb[40 % NB][1], mfr);
        P2PAIR(0, 4); P2PAIR(1, 4);
        asm volatile("s_nop 3");
        SB();
        if constexpr (!HOT) lgkm<0>();
      };
      pass1(IC<1>{});
      if (__builtin_expect(any_nan(a1[0]), 0)) {
        unsigned att = 0;
        lgkm<0>();
        do { CNT(1); pass1(IC<0>{}); } while (any_nan(a1[0]) && ++att < 32);
      }
      STAMP(3 + 12 * l);
    }
    vmc<7>();
    BARE();
    if (l == 0) {
      RELANE();
    v4u PV;
    asm volatile("v_mov_b32 %0, -1\n\tv_mov_b32 %1, -1\n\tv_mov_b32 %2, -1\n\tv_mov_b32 %3, -1" : "=v"(PV.x), "=v"(PV.y), "=v"(PV.z), "=v"(PV.w));
      stg_sc1(rs, vXv, sXp, PV); stg_sc1(rs, vXv + 512, sXp, PV); stg_sc1(rs, vX3, sXp + 4096, PV);
      __builtin_amdgcn_raw_buffer_store_b64(v2u{PV.x, PV.y}, rs, vSp, sSp, SC1);
    }
#pragma unroll
    for (int i = 0; i < 4; ++i) osb[i] = sOr + ((w + 1 + i) & 3) * 20480;
    ops = sOr + 4096; cWo = sWo + ((7 + P2W) >> 2) * 4096; cXp = sXp; cSp = sSp; cOp = sOp;
    sX += XB; sSt += STB; sWo += m ? -8 * WO_SZ : 8 * WO_SZ; sOp += XB; sOr += XB; sXp += XB; sSp += STB;
    constexpr int NDM2 = 0;
    if constexpr (m == 0) {
      RELANE();
      asm volatile("" : "+v"(vKa), "+v"(vVa), "+v"(vHv));
      v4u kc0, kc1, kc2, hd[4];
      const unsigned hsrc = WS_HALO + (((l >> 1) * 16 + rg - 1) * 16 + cg) * 4096;
#define VCH(i) ((i) < 4 ? v4u{f2u(a2[4 * (i)]), f2u(a2[4 * (i) + 1]), f2u(a2[4 * (i) + 2]), f2u(a2[4 * (i) + 3])} : v4u{f2u(a3[4]), f2u(a3[5]), f2u(a3[6]), f2u(a3[7])})
      {
        f32x2 t[10];
#pragma unroll
        for (int i = 0; i < 8; ++i) t[i] = pkmul(f32x2{a1[2 * i], a1[2 * i + 1]}, f32x2{rstd, rstd});
#pragma unroll
        for (int i = 0; i < 2; ++i) t[8 + i] = pkmul(f32x2{a3[2 * i], a3[2 * i + 1]}, f32x2{rstd, rstd});
        kc0 = v4u{pk2(t[0][0], t[0][1]), pk2(t[1][0], t[1][1]), pk2(t[2][0], t[2][1]), pk2(t[3][0], t[3][1])};
        kc1 = v4u{pk2(t[4][0], t[4][1]), pk2(t[5][0], t[5][1]), pk2(t[6][0], t[6][1]), pk2(t[7][0], t[7][1])};
        kc2 = v4u{pk2(t[8][0], t[8][1]), pk2(t[9][0], t[9][1]), 0u, 0u};
#pragma unroll
        for (int i = 0; i < 8; ++i) { const f32x2 u = pkmul(f32x2{a2[2 * i], a2[2 * i + 1]}, f32x2{rstd, rstd}); a2[2 * i] = u[0]; a2[2 * i + 1] = u[1]; }
#pragma unroll
        for (int i = 2; i < 4; ++i) { const f32x2 u = pkmul(f32x2{a3[2 * i], a3[2 * i + 1]}, f32x2{rstd, rstd}); a3[2 * i] = u[0]; a3[2 * i + 1] = u[1]; }
        asm volatile("" : "+v"(kc0), "+v"(kc1), "+v"(kc2), "+v"(a2), "+v"(a3)); }
      unsigned qp[10];
      const rsrc_t rh = __builtin_amdgcn_make_buffer_rsrc(p.ws + hsrc, 0, rg > 0 ? 4096u : 0u, 0x00020000);
      if (w == 3) {
        const unsigned hs = hsrc + 65536;
        stg_sc1(rs, vHv, hs, kc0); stg_sc1(rs, vHv + 512, hs, kc1); stg_sc1(rs, vHv + 1024, hs, kc2);
#pragma unroll
        for (int i = 0; i < 5; ++i) stg_sc1(rs, vHv + 1536 + i * 512, hs, VCH(i));
      }
      SFOR(p_, 18)
        constexpr int pp = p_ + 2;
        SB();
        constexpr int nwr = (pp - 1 >= 2 ? (pp - 1 > 16 ? 16 : pp - 1) / 2 : 0) - (pp - 4 >= 2 ? (pp - 4 > 16 ? 16 : pp - 4) / 2 : 0);
        P2PAIR(pp, (2 * pp + 6 <= 41 ? 4 : 39 - 2 * pp) + nwr);
        if constexpr (pp == 2) DSW(vKa, 14 * 48, kc0);
        if constexpr (pp == 4) DSW(vKa, 14 * 48 + 16, kc1);
        if constexpr (pp == 6) DSW(vKa, 14 * 48 + 32, kc2);
        if constexpr (pp >= 8 && pp <= 16 && !(pp & 1)) { constexpr int vi = pp >= 8 ? (pp - 8) / 2 : 0; DSW(vVa, 14 * 80 + vi * 16, VCH(vi)); }
        if constexpr (pp == 11) { qp[8] = pk2(a3[8], a3[9]); qp[9] = pk2(a3[10], a3[11]); asm volatile("" : "+v"(qp[8]), "+v"(qp[9])); }
        if constexpr (pp == HLP) {
          if (w == 0) {
#pragma unroll
            for (int i = 0; i < 4; ++i) hd[i] = ldg_sc1(rh, l16 + i * 1024, 0);
          }
        }
      SEND;
      SB();
      lgkm<0>();
      mf32v<false>(a0, wq[40 % 6], mfr);
      vmc<P2W>();
      BARE();
      const unsigned ka = vKa, va = vVa;
      const float fq = rstd * (LOG2E * 0.22360679774997896f);
      STAMP(5 + 12 * l);
      if (w == 0) {
        const unsigned q_ = ((lane >> 4) & 1) * 144 + (lane & 15);
        unsigned h0 = L_K + q_ * 48 + h * 16, h1 = h ? L_V + q_ * 80 : L_K + q_ * 48 + 32, h2 = L_V + q_ * 80 + 16 + h * 16;
        asm volatile("" : "+v"(h0), "+v"(h1), "+v"(h2));
        unsigned mm = 0;
#pragma unroll
        for (int i = 0; i < 4; ++i) mm = mx4(mm, hd[i]);
        if (__builtin_expect(any_sent(mm), 0)) {
          unsigned sp = 0;
          do {
            FENCE();
            mm = 0;
#pragma unroll
            for (int i = 0; i < 4; ++i) hd[i] = ldg_sc1(rh, l16 + i * 1024, 0);
#pragma unroll
            for (int i = 0; i < 4; ++i) mm = mx4(mm, hd[i]);
            CNT(2);
          } while (any_sent(mm) && !give_up(sp, tm));
        }
        sts128(L, h0, hd[0]);
        sts128(L, h1, hd[1]);
        sts128(L, h2, hd[2]);
        sts128(L, h2 + 32, hd[3]);
      }
      STAMP(4 + 12 * l);
      float lg[8];
      v4u kb[8][2], vb[8][5];
      v2u kt[8];
      auto rdk = [&](auto j_) __attribute__((always_inline)) { constexpr int j = decltype(j_)::v;
        kb[j][0] = lds128(L, ka + (7 - j) * 96); kb[j][1] = lds128(L, ka + (7 - j) * 96 + 16); kt[j] = lds64(L, ka + (7 - j) * 96 + 32); };
      auto rdv = [&](auto j_) __attribute__((always_inline)) { constexpr int j = decltype(j_)::v;
#pragma unroll
        for (int i = 0; i < 5; ++i) vb[j][i] = lds128(L, va + (7 - j) * 160 + i * 16); };
      SB();
      rdk(IC<0>{}); rdk(IC<1>{}); rdk(IC<2>{}); rdk(IC<3>{});
      SB();
#pragma unroll
      for (int i = 0; i < 8; ++i) qp[i] = pk2(a0[2 * i], a0[2 * i + 1]);
      SFOR(jj, 4)
        SB();
        if constexpr (jj < ADM) DM(NDM2 + jj);
        if constexpr (jj < 2) { rdk(IC<2 * jj + 4>{}); rdk(IC<2 * jj + 5>{}); } else rdv(IC<jj - 2>{});
        SB();
        float s0 = 0.f, s1 = 0.f;
#pragma unroll
        for (int i = 0; i < 8; ++i) { s0 = dot2(kb[2 * jj][i >> 2][i & 3], qp[i], s0); s1 = dot2(kb[2 * jj + 1][i >> 2][i & 3], qp[i], s1); }
#pragma unroll
        for (int i = 0; i < 2; ++i) { s0 = dot2(kt[2 * jj][i], qp[8 + i], s0); s1 = dot2(kt[2 * jj + 1][i], qp[8 + i], s1); }
        asm volatile("" : "+v"(s0), "+v"(s1));
        lg[2 * jj] = s0; lg[2 * jj + 1] = s1;
      SEND;
      SB();
      if constexpr (4 < ADM) DM(NDM2 + 4);
      rdv(IC<2>{});
      SB();
      if (rg == 0 && w == 0) {
#pragma unroll
        for (int j = 1; j < 8; ++j) lg[j] = tok >= 2 * j ? lg[j] : -__builtin_inff();
      }
      const float mx = max3f(max3f(lg[0], lg[1], lg[2]), max3f(lg[3], lg[4], lg[5]), max3f(lg[6], lg[7], lg[7]));
      const float nb = -fq * mx;
#pragma unroll
      for (int j = 0; j < 8; j += 2) { const f32x2 t = pkfma(f32x2{lg[j], lg[j + 1]}, f32x2{fq, fq}, f32x2{nb, nb}); lg[j] = t[0]; lg[j + 1] = t[1]; }
      SB();
#pragma unroll
      for (int j = 0; j < 8; ++j) lg[j] = __builtin_amdgcn_exp2f(lg[j]);
      SB();
      if constexpr (5 < ADM) DM(NDM2 + 5);
      SB();
      const float inv = __builtin_amdgcn_rcpf(((lg[0] + lg[1]) + (lg[2] + lg[3])) + ((lg[4] + lg[5]) + (lg[6] + lg[7])));
      f32x2 oa[10];
      SFOR(j, 8)
        SB();
        if constexpr (6 + j < ADM) DM(NDM2 + 6 + j);
        if constexpr (j < 5) rdv(IC<j + 3>{});
        SB();
        const float pj = lg[j];
        SFOR(i, 5)
          const f32x4 v = __builtin_bit_cast(f32x4, vb[j][i]);
          if constexpr (j == 0) { oa[2 * i] = pkmul(f32x2{pj, pj}, f32x2{v[0], v[1]}); oa[2 * i + 1] = pkmul(f32x2{pj, pj}, f32x2{v[2], v[3]}); }
          else { oa[2 * i] = pkfma(f32x2{pj, pj}, f32x2{v[0], v[1]}, oa[2 * i]); oa[2 * i + 1] = pkfma(f32x2{pj, pj}, f32x2{v[2], v[3]}, oa[2 * i + 1]); }
        SEND;
      SEND;
#pragma unroll
      for (int i = 0; i < 10; ++i) oa[i] = pkmul(oa[i], f32x2{inv, inv});
#pragma unroll
      for (int i = 0; i < 10; ++i) { o[2 * i] = oa[i][0]; o[2 * i + 1] = oa[i][1]; }
    } else {
      RELANE();
      asm volatile("" : "+v"(vImg), "+v"(vA0), "+v"(vA2), "+v"(vB), "+v"(vS0), "+v"(vS1), "+v"(vK0), "+v"(vK1), "+v"(vF0), "+v"(vF1), "+v"(vKv), "+v"(vKp));
      const float sl = rstd * LOG2E;
      {
        const unsigned rb = vImg;
        float kk[20];
#pragma unroll
        for (int i = 0; i < 16; ++i) kk[i] = a1[i];
#pragma unroll
        for (int i = 0; i < 4; ++i) kk[16 + i] = a3[i];
        unsigned ph[20];
        phi_map<true>(kk, sl, rstd, ph, [&](auto i_) __attribute__((always_inline)) { constexpr int i = decltype(i_)::v; SB();
          if constexpr (i < ADM) dmaw<NDM2 + i>(rw, l16, wb1k);
          if (i == 0) sts128(L, rb ^ (5 << 4), v4u{pk2(a2[0], a2[1]), pk2(a2[2], a2[3]), pk2(a2[4], a2[5]), pk2(a2[6], a2[7])});
          if (i == 1) sts128(L, rb ^ (6 << 4), v4u{pk2(a2[8], a2[9]), pk2(a2[10], a2[11]), pk2(a2[12], a2[13]), pk2(a2[14], a2[15])});
          if (i == 2) sts128(L, rb ^ (7 << 4), v4u{pk2(a3[4], a3[5]), pk2(a3[6], a3[7]), pk2(sd, 0.f), 0u});
          if (i == 5) sts128(L, rb ^ (0 << 4), v4u{ph[0], ph[1], ph[2], ph[3]});
          if (i == 6) sts128(L, rb ^ (1 << 4), v4u{ph[4], ph[5], ph[6], ph[7]});
          SB(); });
#pragma unroll
        for (int c = 2; c < 5; ++c) sts128(L, rb ^ (c << 4), v4u{ph[4 * c], ph[4 * c + 1], ph[4 * c + 2], ph[4 * c + 3]});
      }
      {
        const unsigned aA0 = vA0, aA2 = vA2, aB = vB;
        asm volatile("" : "+v"(vA0x), "+v"(vA2x), "+v"(vBx), "+v"(vK2), "+v"(vK3), "+v"(vSel));
        const unsigned aA0x = vA0x, aA2x = vA2x, aBx = vBx;
        BAR();
        f32x4 d0, d1, d2;
        v2u t[4][8];
        auto trd = [&](auto k_) __attribute__((always_inline)) {
          constexpr int ks = decltype(k_)::v; constexpr unsigned o_ = ks * 4096;
          t[ks][0] = ldstr(L, aB + o_); t[ks][1] = ldstr(L, aBx + o_); t[ks][6] = ldstr(L, aA2 + o_); t[ks][7] = ldstr(L, aA2x + o_);
          t[ks][2] = ldstr(L, aA0 + o_); t[ks][3] = ldstr(L, aA0x + o_); t[ks][4] = ldstr(L, aA0 + 8 + o_); t[ks][5] = ldstr(L, aA0x + 8 + o_);
          SB();
        };
        trd(IC<0>{});
        SFOR(ks, 4)
          if constexpr (ks < 3) trd(IC<ks + 1>{});
          const v4u bb = {t[ks][0].x, t[ks][0].y, t[ks][1].x, t[ks][1].y};
          mf16v<ks == 0>(d2, v4u{t[ks][6].x, t[ks][6].y, t[ks][7].x, t[ks][7].y}, bb);
          mf16v<ks == 0>(d0, v4u{t[ks][2].x, t[ks][2].y, t[ks][3].x, t[ks][3].y}, bb);
          mf16v<ks == 0>(d1, v4u{t[ks][4].x, t[ks][4].y, t[ks][5].x, t[ks][5].y}, bb);
          SB();
        SEND;
        asm volatile("s_nop 7");
        SB();
        const unsigned e0 = pk2(d2[0], d2[1]), e1 = pk2(d2[2], d2[3]);
        auto s0 = __builtin_amdgcn_permlane16_swap(e0, e0, false, false);
        auto s1 = __builtin_amdgcn_permlane16_swap(e1, e1, false, false);
        const unsigned ks_ = WS_KV + (((l >> 1) * 16 + cg) * 16 + rg) * 4096;
        stg_sc1(rs, vS0, ks_, v4u{pk2(d0[0], d0[1]), pk2(d0[2], d0[3]), pk2(d1[0], d1[1]), pk2(d1[2], d1[3])});
        stg_sc1(rs, vS1, ks_, v4u{e0, e1, s0[1], s1[1]});
      }
      STAMP(4 + 12 * l);
      if (l == 1) {
    v4u PV;
    asm volatile("v_mov_b32 %0, -1\n\tv_mov_b32 %1, -1\n\tv_mov_b32 %2, -1\n\tv_mov_b32 %3, -1" : "=v"(PV.x), "=v"(PV.y), "=v"(PV.z), "=v"(PV.w));
#pragma unroll
        for (int u = 0; u < 2; ++u) {
          stg_sc1(rs, vOv, sOp + u * XB, PV); stg_sc1(rs, vOv + 1024, sOp + u * XB, PV); stg_sc1(rs, vO3, sOp + u * XB + 4096, PV);
          stg_sc1(rs, vXv, cXp + u * XB, PV); stg_sc1(rs, vXv + 512, cXp + u * XB, PV); stg_sc1(rs, vX3, cXp + u * XB + 4096, PV);
          __builtin_amdgcn_raw_buffer_store_b64(v2u{PV.x, PV.y}, rs, vSp, cSp + u * STB, SC1);
        }
      }
      const unsigned ks_ = WS_KV + ((l >> 1) * 16 + cg) * 65536 + (w < 3 ? w * 1024 : 11 * 256);
      const unsigned kvv = vKv;
      unsigned kpr, kso[4];
#pragma unroll
      for (int i = 0; i < 4; ++i) kso[i] = ks_ + i * 16384;
#define LDKP(r) lda<4 * (r), ((r) & 3) * 256, true>(rs, kvv, kso[(r) >> 2])
      const unsigned kps = WS_KV + ((l >> 1) * 16 + cg) * 65536, kpv = vKp;
      SFOR(p_, 18) constexpr int pp = p_ + 2; SB(); P2PAIR(pp, (2 * pp + 6 <= 41 ? 4 : 39 - 2 * pp));
        if constexpr (pp == 17) probe_issue(kpr, rs, kpv, kps);
        if constexpr (pp >= 18 && 2 * (pp - 18) < KPS) { constexpr int r = pp >= 18 ? 2 * (pp - 18) : 0; LDKP(r); LDKP(r + 1); }
      SEND;
      SB();
      lgkm<0>();
      mf32v<false>(a0, wq[40 % 6], mfr);
      SFOR(r, 16) if constexpr (r >= (KPS > 4 ? 4 : KPS)) LDKP(r); SEND;
      FENCE(); SB();
      STAMP(5 + 12 * l);
      unsigned ph[20];
      float eps;
      { float qq[20];
#pragma unroll
        for (int i = 0; i < 4; ++i) qq[16 + i] = a3[8 + i];
#pragma unroll
        for (int i = 0; i < 16; ++i) qq[i] = a0[i];
        eps = 1e-6f * phi_map<false>(qq, sl, 1.f, ph, [&](auto i_) __attribute__((always_inline)) {
          if constexpr (decltype(i_)::v == 4) {
            SB();
            probe_wait<16>(kpr);
            if (__builtin_expect(any_sent(kpr), 0)) {
              CNT(3);
              poll_probe<100>(rs, kpv, kps, tm);
              SFOR(r, 16) LDKP(r); SEND;
            }
            FENCE(); SB();
          } });
      }
      v4u f[10];
#pragma unroll
      for (int i = 0; i < 20; i += 4) asm volatile("" : "+v"(ph[i]), "+v"(ph[i + 1]), "+v"(ph[i + 2]), "+v"(ph[i + 3]));
      SB();
      f32x4 sm[4];
      auto ksum = [&]() __attribute__((always_inline)) {
        SFOR(r, 16) if constexpr ((r & 3) == 0) vmc<12 - r>(); mf16vz<(r < 4), 4 * r>(sm[r & 3], vSel); SEND;
        asm volatile("s_nop 7");
        SB();
        return any_nan(((sm[0][0] + sm[1][0]) + sm[2][0]) + sm[3][0]);
      };
      if (__builtin_expect(ksum(), 0)) {
        unsigned sp = 0;
        do { CNT(1); SFOR(r, 16) LDKP(r); SEND; } while (ksum() && !give_up(sp, tm));
      }
      {
        v2u t2[4];
#pragma unroll
        for (int j = 0; j < 4; ++j) t2[j] = v2u{pk2(sm[j][0], sm[j][1]), pk2(sm[j][2], sm[j][3])};
        asm volatile("ds_write_b64 %0, %4\n\tds_write_b64 %1, %5\n\tds_write_b64 %2, %6\n\tds_write_b64 %3, %7" :: "v"(vK0), "v"(vK2), "v"(vK1), "v"(vK3), "v"(t2[0]), "v"(t2[1]), "v"(t2[2]), "v"(t2[3]) : "memory");
      }
      SFOR(i, (ADM > 10 ? ADM - 10 : 0)) DM(10 + i); SEND;
      BAR();
#pragma unroll
      for (int s = 0; s < 5; ++s) { f[2 * s + 1] = lds128(L, vF1 + s * 400); f[2 * s] = lds128(L, vF0 + s * 400); }
      f32x16 n0, n1;
      SFOR(s, 5)
        const v4u bb = {ph[4 * s], ph[4 * s + 1], ph[4 * s + 2], ph[4 * s + 3]};
        mf32v<s == 0>(n1, f[2 * s + 1], bb); mf32v<s == 0>(n0, f[2 * s], bb);
      SEND;
      MFMA_SETTLE();
      SB();
      const float inv = __builtin_amdgcn_rcpf(n1[4] + eps);
#pragma unroll
      for (int i = 0; i < 2; ++i) { const f32x2 t = pkmul(f32x2{n1[2 * i], n1[2 * i + 1]}, f32x2{inv, inv}); o[16 + 2 * i] = t[0]; o[17 + 2 * i] = t[1]; }
#pragma unroll
      for (int i = 0; i < 8; ++i) { const f32x2 t = pkmul(f32x2{n0[2 * i], n0[2 * i + 1]}, f32x2{inv, inv}); o[2 * i] = t[0]; o[2 * i + 1] = t[1]; }
    }
    {
      RELANE();
      asm volatile("" : "+v"(vOv), "+v"(vOp), "+v"(vXv), "+v"(vO3), "+v"(vX3), "+v"(vSp));
      const unsigned os = cOp;
      stg_sc1(rs, vOv, os, v4u{pk2(o[0], o[1]), pk2(o[2], o[3]), pk2(o[4], o[5]), pk2(o[6], o[7])});
      stg_sc1(rs, vOv + 1024, os, v4u{pk2(o[8], o[9]), pk2(o[10], o[11]), pk2(o[12], o[13]), pk2(o[14], o[15])});
      const unsigned e0 = pk2(o[16], o[17]), e1 = pk2(o[18], o[19]);
      auto s0 = __builtin_amdgcn_permlane32_swap(e0, e0, false, false);
      auto s1 = __builtin_amdgcn_permlane32_swap(e1, e1, false, false);
      stg_sc1(rs, vO3, os + 4096, v4u{e0, e1, s0[1], s1[1]});
      STAMP(6 + 12 * l);
      if (m == 0 && l == 0) {
        v4u PV;
        asm volatile("v_mov_b32 %0, -1\n\tv_mov_b32 %1, -1\n\tv_mov_b32 %2, -1\n\tv_mov_b32 %3, -1" : "=v"(PV.x), "=v"(PV.y), "=v"(PV.z), "=v"(PV.w));
        stg_sc1(rs, vOv, sOp, PV); stg_sc1(rs, vOv + 1024, sOp, PV); stg_sc1(rs, vO3, sOp + 4096, PV);
        stg_sc1(rs, l16, WS_KV + (256 + cg * 16 + rg) * 4096 + w * 1024, PV);
        stg_sc1(rs, l16, WS_HALO + (256 + rg * 16 + cg) * 4096 + w * 1024, PV);
        __builtin_amdgcn_s_sleep(DO1 - 4);
      } else __builtin_amdgcn_s_sleep(DO1);
      wos = cWo;
      SFOR(i, 17 - P2W) LDWO(7 + P2W + i); SEND;
      asm volatile("buffer_load_dwordx4 %0, %1, %2, %3 offen sc1" : "=v"(wo24) : "v"(l16), "s"(rs), "s"(wos) : "memory");
      SFOR(i, ODM) DM(NDM2 + ADM + i); SEND;
      __builtin_amdgcn_s_sleep(DLY_O);
      STAMP(7 + 12 * l);
      const unsigned l16b = l16 + 1024;
#define LDO(i, s) do { if constexpr ((s) == 0) lda<20 * (i), 0, true>(rs, l16, osb[i]); else lda<20 * (i) + 4 * (s), ((s) - 1) * 1024, true>(rs, l16b, osb[i]); } while (0)
      unsigned opr;
      const unsigned opv = vOp;
      probe_issue(opr, rs, opv, ops);
      SFOR(s, 5) LDO(0, s); if constexpr (s == 1) BARE(); SEND;
      BARE();
      SFOR(s, 5) LDO(1, s); SEND;
      SB();
      unsigned rwa[3], rra = L_RED + w * 15360 + l16;
      f32x2 rsum[10];
#pragma unroll
      for (int i = 0; i < 3; ++i) rwa[i] = L_RED + l16 + (((w + 1 + i) & 3) * 3 + i) * 5120;
#pragma unroll
      for (int i = 0; i < 10; ++i) rsum[i] = f32x2{lo16(xr[i]), hi16(xr[i])};
      asm volatile("" : "+v"(rsum[0]), "+v"(rsum[1]), "+v"(rsum[2]), "+v"(rsum[3]), "+v"(rsum[4]), "+v"(rsum[5]), "+v"(rsum[6]), "+v"(rsum[7]), "+v"(rsum[8]), "+v"(rsum[9]));
      asm volatile("" : "+v"(rwa[0]), "+v"(rwa[1]), "+v"(rwa[2]), "+v"(rra));
      f32x4 acc[2][5], part[15];
      probe_wait<10>(opr);
      asm volatile("" : "+v"(wo24));
      if (__builtin_expect(any_sent(opr), 0)) {
        CNT(4);
        poll_probe<100>(rs, opv, ops, tm);
        SFOR(s, 5) LDO(0, s); SEND;
        SFOR(s, 5) LDO(1, s); SEND;
      }
      auto redo = [&](auto i_) __attribute__((always_inline)) {
        constexpr int i = decltype(i_)::v;
        unsigned sp = 0;
        do {
          CNT(1);
          SFOR(s, 5) LDO(i, s); SEND;
          vmc<0>();
          SFOR(j, 25) if constexpr (j < 24) mf16aa<(j < 5), 160 + 4 * j, 20 * i + 4 * (j / 5)>(acc[i & 1][j % 5]); else mf16va<20 * i + 16>(acc[i & 1][4], wo24); SEND;
          asm volatile("s_nop 7");
          SB();
        } while (any_nan(acc[i & 1][4][0]) && !give_up(sp, tm));
      };
      constexpr int NDO = NDM2 + ADM + ODM;
      static_assert(NDO + GDM <= 22 && NDM2 + ADM + ODM <= 17 && GDM <= 10 && ADM <= 14, "");
      SFOR(i, 4)
        SFOR(j, 25)
          constexpr int s = j / 5, ft = j % 5, WS0 = 4, wstep = 4;
          SB();
          constexpr int gi = 5 * i + s;
          if constexpr (ft == 0) vmc<(gi < 10 ? 9 : 19 - gi) + (gi > 20 - GDM ? gi - (20 - GDM) : 0)>();
          if constexpr (j < 24) mf16aa<s == 0, 160 + 4 * j, 20 * i + 4 * s>(acc[i & 1][ft]); else mf16va<20 * i + 16>(acc[i & 1][ft], wo24);
          if constexpr (i == 0 && j == 0) STAMP(8 + 12 * l);
          if constexpr (ft == 2) {
            if constexpr (i < 2) LDO(i + 2, s);
            else if constexpr (gi >= 20 - GDM) dmaw<NDO + gi - (20 - GDM)>(rw, l16, wb1k);
          }
          if constexpr (i > 0 && j == WS0 - 1) { asm volatile("s_nop 1"); SB(); if (__builtin_expect(any_nan(acc[(i - 1) & 1][4][0]), 0)) redo(IC<i - 1>{}); }
          if constexpr (i > 0 && j >= WS0 && j <= WS0 + 4 * wstep && (j - WS0) % wstep == 0)
            DSW(rwa[i > 0 ? i - 1 : 0], ((j - WS0) / wstep) * 1024, acc[(i - 1) & 1][(j - WS0) / wstep]);
          if constexpr (i == 3 && j == 23) { SB(); BAR(); }
          if constexpr (i == 3 && j >= 23) {
#pragma unroll
            for (int q = (j - 23) * 8; q < (j - 23) * 8 + 8 && q < 15; ++q) part[q] = __builtin_bit_cast(f32x4, lds128(L, rra + (q % 3) * 5120 + (q / 3) * 1024));
          }
        SEND;
      SEND;
      SB();
      STAMP(9 + 12 * l);
#pragma unroll
      for (int ft = 0; ft < 5; ++ft)
#pragma unroll
        for (int q = 0; q < 3; ++q) { rsum[2 * ft] = pkadd(rsum[2 * ft], f32x2{part[ft * 3 + q][0], part[ft * 3 + q][1]}); rsum[2 * ft + 1] = pkadd(rsum[2 * ft + 1], f32x2{part[ft * 3 + q][2], part[ft * 3 + q][3]}); }
      asm volatile("" : "+v"(rsum[8]), "+v"(rsum[9]));
      SB();
      if (__builtin_expect(any_nan(acc[1][4][0]), 0)) redo(IC<3>{});
      auto fin = [&](int ft) __attribute__((always_inline)) {
        const f32x2 lo = pkadd(rsum[2 * ft], f32x2{acc[1][ft][0], acc[1][ft][1]}), hi = pkadd(rsum[2 * ft + 1], f32x2{acc[1][ft][2], acc[1][ft][3]});
        xr[2 * ft] = pk2(lo[0], lo[1]); xr[2 * ft + 1] = pk2(hi[0], hi[1]);
      };
      fin(0); fin(1);
      if (l < 3) {
        stg_sc1(rs, vXv, cXp, v4u{xr[0], xr[1], xr[2], xr[3]});
        SB();
        fin(2); fin(3);
        stg_sc1(rs, vXv + 512, cXp, v4u{xr[4], xr[5], xr[6], xr[7]});
        SB();
        fin(4);
        { auto s0 = __builtin_amdgcn_permlane16_swap(xr[8], xr[8], false, false);
          auto s1 = __builtin_amdgcn_permlane16_swap(xr[9], xr[9], false, false);
          stg_sc1(rs, vX3, cXp + 4096, v4u{xr[8], xr[9], s0[1], s1[1]}); }
        SB();
        { float sa = 0.f, sq = 0.f;
#pragma unroll
          for (int i = 0; i < 10; ++i) { sa = dot2(xr[i], 0x3F803F80u, sa); sq = dot2(xr[i], xr[i], sq); }
          { auto a = __builtin_amdgcn_permlane16_swap(f2u(sa), f2u(sa), false, false); sa = u2f(a[0]) + u2f(a[1]);
            auto c = __builtin_amdgcn_permlane16_swap(f2u(sq), f2u(sq), false, false); sq = u2f(c[0]) + u2f(c[1]); }
          { auto a = __builtin_amdgcn_permlane32_swap(f2u(sa), f2u(sa), false, false); sa = u2f(a[0]) + u2f(a[1]);
            auto c = __builtin_amdgcn_permlane32_swap(f2u(sq), f2u(sq), false, false); sq = u2f(c[0]) + u2f(c[1]); }
          __builtin_amdgcn_raw_buffer_store_b64(v2u{f2u(sa), f2u(sq)}, rs, vSp, cSp, SC1); }
        STAMP(10 + 12 * l);
        if (m == 0 && l == 0 && w < 2) {
          const unsigned cv = tid * 4, cv2 = cv + 4096;
          SFOR(r, 16) lda1<210 + r, (r & 7) * 512>(rs, r < 8 ? cv : cv2, WS_CSP + (16 + cg) * 8192); SEND;
        }
        BARE();
        __builtin_amdgcn_s_sleep(DX1);
        SFOR(i, 22 - NDO - GDM + LW) DM(NDO + GDM + i); SEND;
        __builtin_amdgcn_s_sleep(DLY_X);
      } else {
        fin(2); fin(3); fin(4);
        GAS v2u* dst = (GAS v2u*)(p.out + (size_t)(rg * 128 + half * 64 + w * 16 + (lane & 15)) * 640 + c8 * 80 + (lane >> 4) * 20);
#pragma unroll
        for (int i = 0; i < 5; ++i) dst[i] = v2u{xr[2 * i], xr[2 * i + 1]};
        if (b == 0 && w == 0) __builtin_amdgcn_raw_buffer_store_b32(tm.epoch, rs, lane ? 0x80000000u : 0u, WS_CTRL + 128, SC1);
      }
      STAMP(11 + 12 * l);
    }
  };
#pragma nounroll
  for (int ll = 0; ll < 2; ++ll) { layer(IC<0>{}, ll); layer(IC<1>{}, ll); }
}

extern "C" void kernel_launch(void* const* d_in, const int* in_sizes, int n_in, void* d_out, int out_size, void* d_ws, size_t ws_size, hipStream_t stream) {
  Params p;
  p.x = (const u16*)d_in[0]; p.wqA = (const u16*)d_in[1]; p.woA = (const u16*)d_in[2];
  p.wqB = (const u16*)d_in[4]; p.wphi = (const u16*)d_in[5]; p.woB = (const u16*)d_in[6];
  p.out = (u16*)d_out; p.ws = (unsigned char*)d_ws;
  (void)hipFuncSetAttribute((const void*)k_main, hipFuncAttributeMaxDynamicSharedMemorySize, LDS_ALLOC);
  k_main<<<256, 256, LDS_ALLOC, stream>>>(p);
}
```

```cpp
#define JIT
#include <hip/hip_runtime.h>
#include <stdint.h>

typedef unsigned short u16;
typedef unsigned v4u __attribute__((ext_vector_type(4)));
typedef unsigned v2u __attribute__((ext_vector_type(2)));
typedef float f32x2 __attribute__((ext_vector_type(2)));
typedef float f32x4 __attribute__((ext_vector_type(4)));
typedef float f32x16 __attribute__((ext_vector_type(16)));
typedef __bf16 bf16x8 __attribute__((ext_vector_type(8)));
typedef __bf16 bf16x2 __attribute__((ext_vector_type(2)));
typedef short s16x4 __attribute__((ext_vector_type(4)));
#define LAS __attribute__((address_space(3)))
#define GAS __attribute__((address_space(1)))
typedef __amdgpu_buffer_rsrc_t rsrc_t;
#define DEV __device__ __forceinline__

#define DSC1 " sc1"
constexpr unsigned WQ_SZ = 153600, WO_SZ = 102400, XB = 2621440, STB = 131072;
constexpr unsigned WS_CTRL = 0;
constexpr unsigned WS_FLAG = 4096;
constexpr unsigned WS_WQ = WS_FLAG + 4 * 256 * 128;
constexpr unsigned WS_WO = WS_WQ + 32 * WQ_SZ;
constexpr unsigned WS_CSP = WS_WO + 16 * WO_SZ;
constexpr unsigned WS_ST0 = WS_CSP + 2 * 16 * 16 * 512;
constexpr unsigned WS_ST1 = WS_ST0 + STB;
constexpr unsigned WS_X = WS_ST0 + 4 * STB;
constexpr unsigned WS_O = WS_X + 4 * XB;
constexpr unsigned WS_KV = WS_O + 4 * XB;
constexpr unsigned WS_HALO = WS_KV + 2 * 256 * 4096;
constexpr unsigned WS_END = WS_HALO + 2 * 256 * 4096;
constexpr unsigned WS_STAMP = 40u << 20;
constexpr unsigned WS_TOTAL = WS_STAMP + 256 * 4 * 64 * 8;
constexpr unsigned KSTEP = 2816, L_P0 = 112640, L_KVA = 153600, L_CS = L_KVA + 4096;
constexpr unsigned L_SCR = 73216;
constexpr unsigned L_K = L_SCR, L_V = L_SCR + 2 * 144 * 48;
constexpr unsigned L_RED = 90112;
constexpr unsigned LDS_BYTES = L_CS + 4096;
constexpr unsigned SENT = 0xFFFFFFFFu;
constexpr unsigned CODE_BYTES = 24576;
constexpr int SC1 = 16;
constexpr float LOG2E = 1.4426950408889634f;

struct Params { const u16 *x, *wqA, *woA, *wqB, *wphi, *woB; u16* out; unsigned char* ws; };

DEV unsigned pk2(float a, float b) { bf16x2 v = {(__bf16)a, (__bf16)b}; return __builtin_bit_cast(unsigned, v); }
DEV float lo16(unsigned u) { return __builtin_bit_cast(float, u << 16); }
DEV float hi16(unsigned u) { return __builtin_bit_cast(float, u & 0xffff0000u); }
DEV unsigned f2u(float f) { return __builtin_bit_cast(unsigned, f); }
DEV float u2f(unsigned u) { return __builtin_bit_cast(float, u); }
DEV unsigned umax(unsigned a, unsigned b) { return __builtin_elementwise_max(a, b); }
DEV unsigned umin(unsigned a, unsigned b) { return __builtin_elementwise_min(a, b); }
DEV float max3f(float a, float b, float c) { float d; asm("v_max3_f32 %0, %1, %2, %3" : "=v"(d) : "v"(a), "v"(b), "v"(c)); return d; }
DEV float min3f(float a, float b, float c) { float d; asm("v_min3_f32 %0, %1, %2, %3" : "=v"(d) : "v"(a), "v"(b), "v"(c)); return d; }
DEV float dot2(unsigned a, unsigned b, float c) { return __builtin_amdgcn_fdot2_f32_bf16(__builtin_bit_cast(bf16x2, a), __builtin_bit_cast(bf16x2, b), c, false); }
DEV f32x2 pkfma(f32x2 a, f32x2 b, f32x2 c) { return __builtin_elementwise_fma(a, b, c); }
DEV f32x2 pkmul(f32x2 a, f32x2 b) { return a * b; }
DEV f32x2 pkadd(f32x2 a, f32x2 b) { return a + b; }
DEV f32x16 mfma32(v4u a, v4u b, f32x16 c) { return __builtin_amdgcn_mfma_f32_32x32x16_bf16(__builtin_bit_cast(bf16x8, a), __builtin_bit_cast(bf16x8, b), c, 0, 0, 0); }
DEV f32x4 mfma16(v4u a, v4u b, f32x4 c) { return __builtin_amdgcn_mfma_f32_16x16x32_bf16(__builtin_bit_cast(bf16x8, a), __builtin_bit_cast(bf16x8, b), c, 0, 0, 0); }
DEV v4u lds128(LAS unsigned char* L, unsigned a) { return *(LAS v4u*)(L + a); }
DEV v2u lds64(LAS unsigned char* L, unsigned a) { return *(LAS v2u*)(L + a); }
DEV void sts128(LAS unsigned char* L, unsigned a, v4u v) { *(LAS v4u*)(L + a) = v; }
DEV v2u ldstr(LAS unsigned char* L, unsigned a) { return __builtin_bit_cast(v2u, __builtin_amdgcn_ds_read_tr16_b64_v4i16((LAS s16x4*)(L + a))); }
DEV v4u ldg(rsrc_t rs, unsigned voff, unsigned soff) { return __builtin_amdgcn_raw_buffer_load_b128(rs, voff, soff, 0); }
DEV v4u ldg_sc1(rsrc_t rs, unsigned voff, unsigned soff) { return __builtin_amdgcn_raw_buffer_load_b128(rs, voff, soff, SC1); }
DEV v2u ldg64_sc1(rsrc_t rs, unsigned voff, unsigned soff) { return __builtin_amdgcn_raw_buffer_load_b64(rs, voff, soff, SC1); }
DEV void stg_sc1(rsrc_t rs, unsigned voff, unsigned soff, v4u v) { __builtin_amdgcn_raw_buffer_store_b128(v, rs, voff, soff, SC1); }
DEV unsigned mx4(unsigned m, v4u d) { return umax(umax(umax(m, d.x), d.y), umax(d.z, d.w)); }
DEV unsigned max3u(unsigned a, unsigned b, unsigned c) { return umax(umax(a, b), c); }
DEV bool any_sent(unsigned m) { return __builtin_amdgcn_ballot_w64(m == SENT) != 0; }
#define BAR() asm volatile("s_waitcnt lgkmcnt(0)\n\ts_barrier" ::: "memory")
#define DRAIN() asm volatile("s_waitcnt vmcnt(0)" ::: "memory")
#define FENCE() asm volatile("" ::: "memory")
#define SB() __builtin_amdgcn_sched_barrier(0)
DEV void dma4(rsrc_t rs, unsigned voff, unsigned soff, unsigned ldsa) {
  asm volatile("s_mov_b32 m0, %0\n\ts_nop 0\n\tbuffer_load_dwordx4 %1, %2, %3 offen sc1 lds\n\tbuffer_load_dwordx4 %1, %2, %3 offen offset:1024 sc1 lds\n\t"
               "buffer_load_dwordx4 %1, %2, %3 offen offset:2048 sc1 lds\n\tbuffer_load_dwordx4 %1, %2, %3 offen offset:3072 sc1 lds"
               :: "s"(__builtin_amdgcn_readfirstlane(ldsa)), "v"(voff), "s"(rs), "s"(__builtin_amdgcn_readfirstlane(soff)) : "memory");
}
DEV void dma1(rsrc_t rs, unsigned voff, unsigned soff, unsigned ldsa) {
  asm volatile("s_mov_b32 m0, %0\n\ts_nop 0\n\tbuffer_load_dwordx4 %1, %2, %3 offen sc1 lds"
               :: "s"(__builtin_amdgcn_readfirstlane(ldsa)), "v"(voff), "s"(rs), "s"(__builtin_amdgcn_readfirstlane(soff)) : "memory");
}
DEV void dma_range(rsrc_t rs, unsigned l16, unsigned wsrc, int w, int k0, int k1) {
  int k = k0 + 4 * w;
  for (; k + 4 <= k1; k += 16) dma4(rs, l16, wsrc + k * 1024, k * 1024);
  for (; k < k1; ++k) dma1(rs, l16, wsrc + k * 1024, k * 1024);
}
struct Tmo { unsigned char* ws; unsigned epoch; };
DEV bool give_up(unsigned& sp, const Tmo& tm) {
  ++sp;
  if (sp > 32) __builtin_amdgcn_s_sleep(8);
  if ((sp & 63u) == 0u) {
    GAS unsigned* t = (GAS unsigned*)(tm.ws + WS_CTRL);
    if (sp >= (1u << 16)) { __hip_atomic_store(t, tm.epoch, __ATOMIC_RELAXED, __HIP_MEMORY_SCOPE_AGENT); return true; }
    if (__builtin_amdgcn_readfirstlane(__hip_atomic_load(t, __ATOMIC_RELAXED, __HIP_MEMORY_SCOPE_AGENT)) == tm.epoch) return true;
  }
  return false;
}
#define MAPV 1
#define XW 10
DEV int map_rg(int b) { return ((b & 7) >> 1) * 4 + (b >> 6); }
DEV int map_cg(int b) { return (b & 1) * 8 + ((b >> 3) & 7); }
DEV int o_half(int cg) { return (cg >> 2) & 1; }
DEV int o_c8(int cg) { return (cg >> 3) * 4 + (cg & 3); }
DEV int xfeat(int c, int j) { int c8 = c / 10, l = c % 10; return 80 * c8 + (l < 8 ? 20 * (l >> 1) + 8 * (l & 1) + j : 40 * (l - 8) + 20 * (j >> 2) + 16 + (j & 3)); }
DEV int ocol(int cgp, int s, int j) { return 40 * cgp + (s < 4 ? 20 * (s >> 1) + 8 * (s & 1) + j : 20 * (j >> 2) + 16 + (j & 3)); }
DEV void pin_s(unsigned& v) { asm volatile("" : "+s"(v)); }
DEV int fresh_lane() { int l; asm volatile("v_mbcnt_lo_u32_b32 %0, -1, 0\n\tv_mbcnt_hi_u32_b32 %0, -1, %0" : "=v"(l)); return l; }
#define RELANE() const int lane = fresh_lane(), h = lane >> 5, tok = lane & 31, tid = w * 64 + lane; const unsigned l16 = lane * 16; (void)h; (void)tok; (void)tid; (void)l16
#define STAMP(i) do {} while (0)
#define LDS_ALLOC LDS_BYTES
#define CNT(i) do {} while (0)

DEV void publish_x(rsrc_t rs, unsigned xbuf, unsigned stbuf, int rg, int cg, int w, int lane, const unsigned (&xr)[10]) {
  const int g = lane >> 4, tok = lane & 15, half = o_half(cg), c8 = o_c8(cg);
  const unsigned sb = xbuf + ((rg * 4 + half * 2 + (w >> 1)) * 40 + 5 * c8) * 1024;
  const unsigned vb = ((w & 1) * 16 + tok) * 16;
  stg_sc1(rs, vb + g * 1024, sb, v4u{xr[0], xr[1], xr[2], xr[3]});
  stg_sc1(rs, vb + g * 1024 + 512, sb, v4u{xr[4], xr[5], xr[6], xr[7]});
  auto s0 = __builtin_amdgcn_permlane16_swap(xr[8], xr[8], false, false);
  auto s1 = __builtin_amdgcn_permlane16_swap(xr[9], xr[9], false, false);
  if (!(g & 1)) stg_sc1(rs, vb + 4096 + (g >> 1) * 512, sb, v4u{xr[8], xr[9], s0[1], s1[1]});
  SB();
  float sa = 0.f, sq = 0.f;
#pragma unroll
  for (int i = 0; i < 10; ++i) { sa = dot2(xr[i], 0x3F803F80u, sa); sq = dot2(xr[i], xr[i], sq); }
  { auto a = __builtin_amdgcn_permlane16_swap(f2u(sa), f2u(sa), false, false); sa = u2f(a[0]) + u2f(a[1]);
    auto b = __builtin_amdgcn_permlane16_swap(f2u(sq), f2u(sq), false, false); sq = u2f(b[0]) + u2f(b[1]); }
  { auto a = __builtin_amdgcn_permlane32_swap(f2u(sa), f2u(sa), false, false); sa = u2f(a[0]) + u2f(a[1]);
    auto b = __builtin_amdgcn_permlane32_swap(f2u(sq), f2u(sq), false, false); sq = u2f(b[0]) + u2f(b[1]); }
  if (g == 0) __builtin_amdgcn_raw_buffer_store_b64(v2u{f2u(sa), f2u(sq)}, rs, tok * 8, stbuf + ((rg * 8 + c8) * 128 + half * 64 + w * 16) * 8, SC1);
}
DEV void load_x_in(const u16* x, int rg, int cg, int w, int lane, unsigned (&xr)[10]) {
  const int g = lane >> 4, tok = lane & 15;
  const GAS v2u* src = (const GAS v2u*)(x + (size_t)(rg * 128 + o_half(cg) * 64 + w * 16 + tok) * 640 + o_c8(cg) * 80 + g * 20);
#pragma unroll
  for (int i = 0; i < 5; ++i) { v2u t = src[i]; xr[2 * i] = t.x; xr[2 * i + 1] = t.y; }
}

DEV void poison_x(rsrc_t rs, unsigned xbuf, unsigned stbuf, int rg, int cg, int w, int lane) {
  const int g = lane >> 4, tok = lane & 15, half = o_half(cg), c8 = o_c8(cg);
  const unsigned sb = xbuf + ((rg * 4 + half * 2 + (w >> 1)) * 40 + 5 * c8) * 1024, vb = ((w & 1) * 16 + tok) * 16;
  const v4u P = {SENT, SENT, SENT, SENT};
  stg_sc1(rs, vb + g * 1024, sb, P); stg_sc1(rs, vb + g * 1024 + 512, sb, P); stg_sc1(rs, vb + 4096 + (g >> 1) * 512, sb, P);
  __builtin_amdgcn_raw_buffer_store_b64(v2u{SENT, SENT}, rs, tok * 8, stbuf + ((rg * 8 + c8) * 128 + half * 64 + w * 16) * 8, SC1);
}
DEV void poison_o(rsrc_t rs, unsigned obuf, int rg, int cg, int w, int lane) {
  const int h = lane >> 5, tok = lane & 31;
  const unsigned os = obuf + ((rg * 8 + 2 * w) * 4 + (cg >> 2)) * 5120 + (cg & 3) * 256, ov = (tok >> 4) * 20480 + (tok & 15) * 16;
  const v4u P = {SENT, SENT, SENT, SENT};
  stg_sc1(rs, ov + h * 2048, os, P); stg_sc1(rs, ov + h * 2048 + 1024, os, P); stg_sc1(rs, ov + 4096, os, P);
}
DEV void wait_flags(rsrc_t rs, unsigned voff, unsigned soff, const Tmo& tm) {
  unsigned sp = 0, v;
  do { FENCE(); v = __builtin_amdgcn_raw_buffer_load_b32(rs, voff, soff, SC1); } while (__builtin_amdgcn_ballot_w64(v != tm.epoch) != 0 && !give_up(sp, tm));
}
DEV void build_wq_read(LAS unsigned char* L, unsigned stage, int w, int lane, v4u (&fr)[3]) {
  const int gam = lane >> 4, q = (lane >> 2) & 3, pp = lane & 3, a = 2 * (gam & 1) + (pp >> 1), h = pp & 1;
  const int eoff = (w < 3) ? 40 * w + 20 * h + 4 * a : (a == 0 ? 40 : a == 1 ? 80 : 0) + 20 * h + 16;
#pragma unroll
  for (int it = 0; it < 3; ++it) {
    const int kap = 2 * it + (gam >> 1), kc = kap < 4 ? kap : 4;
    const unsigned ad = stage + (kc * 8 + q) * 256 + eoff * 2;
    const v2u t0 = ldstr(L, ad), t1 = ldstr(L, ad + 1024);
    fr[it] = v4u{t0.x, t0.y, t1.x, t1.y};
  }
}
DEV float sum8(v4u v, float c) { c = dot2(v.x, 0x3F803F80u, c); c = dot2(v.y, 0x3F803F80u, c); c = dot2(v.z, 0x3F803F80u, c); return dot2(v.w, 0x3F803F80u, c); }
DEV void build_wq_store(rsrc_t rs, int m, int rg, int cg, int w, int lane, int r0, int r1, const v4u (&fr)[3]) {
  const int row = lane & 31, hi = lane >> 5;
  const unsigned tb = w == 0 ? L_P0 : (w - 1) * 1024, ts = w == 0 ? 1024 : KSTEP, th = w == 3 ? 384 : 512;
  const bool in = row >= r0 && row < r1;
#pragma unroll
  for (int it = 0; it < 3; ++it) {
    const int c = 5 * rg + 2 * it + hi;
    stg_sc1(rs, (in && (it < 2 || !hi)) ? (c >> 1) * ts + (c & 1) * th + row * 16 : 0x80000000u, WS_WQ + (m * 16 + cg) * WQ_SZ + tb, fr[it]);
  }
  float cs = sum8(fr[1], sum8(fr[0], 0.f));
  cs = hi ? cs : sum8(fr[2], cs);
  auto t = __builtin_amdgcn_permlane32_swap(f2u(cs), f2u(cs), false, false);
  __builtin_amdgcn_raw_buffer_store_b32(f2u(u2f(t[0]) + u2f(t[1])), rs, (in && !hi) ? row * 4 : 0x80000000u, WS_CSP + ((m * 16 + cg) * 16 + rg) * 512 + w * 128, SC1);
}
DEV void probe_issue(unsigned& d, rsrc_t rs, unsigned voff, unsigned soff) {
  asm volatile("buffer_load_dword %0, %1, %2, %3 offen sc1" : "=v"(d) : "v"(voff), "s"(rs), "s"(soff) : "memory");
}
template <int N> DEV void probe_wait(unsigned& d) { asm volatile("s_waitcnt vmcnt(%1)" : "+v"(d) : "i"(N) : "memory"); }
#define PSL 2
template <int R> DEV unsigned probe_turn(rsrc_t rs, unsigned voff, unsigned soff) {
  unsigned v;
  asm volatile("buffer_load_dword a%c4, %1, %2, %3 offen sc1\n\ts_waitcnt vmcnt(1)\n\tv_accvgpr_read_b32 %0, a%c5" : "=v"(v) : "v"(voff), "s"(rs), "s"(soff), "i"(R), "i"(R ^ 1) : "memory");
  return v;
}
template <int R> DEV void poll_flag(rsrc_t rs, unsigned voff, unsigned soff, const Tmo& tm) {
  unsigned sp = 0;
  asm volatile("buffer_load_dword a%c3, %0, %1, %2 offen sc1" :: "v"(voff), "s"(rs), "s"(soff), "i"(R) : "memory");
  for (;;) {
    __builtin_amdgcn_s_sleep(PSL);
    if (__builtin_amdgcn_ballot_w64(probe_turn<R + 1>(rs, voff, soff) != tm.epoch) == 0) break;
    __builtin_amdgcn_s_sleep(PSL);
    if (__builtin_amdgcn_ballot_w64(probe_turn<R>(rs, voff, soff) != tm.epoch) == 0) break;
    if (give_up(sp, tm)) break;
  }
}
template <int R> DEV void poll_probe(rsrc_t rs, unsigned voff, unsigned soff, const Tmo& tm) {
  unsigned sp = 0;
  asm volatile("buffer_load_dword a%c3, %0, %1, %2 offen sc1" :: "v"(voff), "s"(rs), "s"(soff), "i"(R) : "memory");
  for (;;) {
    __builtin_amdgcn_s_sleep(PSL);
    if (!any_sent(probe_turn<R + 1>(rs, voff, soff))) break;
    __builtin_amdgcn_s_sleep(PSL);
    if (!any_sent(probe_turn<R>(rs, voff, soff))) break;
    if (give_up(sp, tm)) break;
  }
}
template <int I> struct IC { static constexpr int v = I; };
template <int I, int N, class F> DEV void sfor(F&& f) { if constexpr (I < N) { f(IC<I>{}); sfor<I + 1, N>(f); } }
#define SFOR(var, n) sfor<0, n>([&](auto var##_) __attribute__((always_inline)) { constexpr int var = decltype(var##_)::v;
#define SEND })
template <bool NORM, class F> DEV float phi_map(const float (&a)[20], float s, float post, unsigned (&ph)[20], F slot) {
  float mx = max3f(a[0], a[1], a[2]), mn = min3f(a[0], a[1], a[2]), mx2 = max3f(a[3], a[4], a[5]), mn2 = min3f(a[3], a[4], a[5]);
#pragma unroll
  for (int i = 6; i < 18; i += 4) { mx = max3f(mx, a[i], a[i + 1]); mn = min3f(mn, a[i], a[i + 1]); mx2 = max3f(mx2, a[i + 2], a[i + 3]); mn2 = min3f(mn2, a[i + 2], a[i + 3]); }
  mx = max3f(mx, a[18], a[19]); mn = min3f(mn, a[18], a[19]); mx = max3f(mx, mx2, mx2); mn = min3f(mn, mn2, mn2);
  const float bp = -s * mx, bn = s * mn, ns = -s;
  float sp = 0.f;
  SFOR(sg, 2)
    const f32x2 sc = sg ? f32x2{ns, ns} : f32x2{s, s}, bi = sg ? f32x2{bn, bn} : f32x2{bp, bp};
    f32x2 e[10];
    slot(IC<5 * sg>{});
#pragma unroll
    for (int i = 0; i < 10; ++i) e[i] = pkfma(f32x2{a[2 * i], a[2 * i + 1]}, sc, bi);
    SB();
    SFOR(i, 10)
      if constexpr (i == 3 || i == 7) slot(IC<5 * sg + 1 + (i >> 2)>{});
      e[i] = f32x2{__builtin_amdgcn_exp2f(e[i][0]), __builtin_amdgcn_exp2f(e[i][1])};
    SEND;
    f32x2 s0 = pkadd(e[0], e[1]), s1 = pkadd(e[2], e[3]), s2 = pkadd(e[4], e[5]);
    s0 = pkadd(s0, e[6]); s1 = pkadd(s1, e[7]); s2 = pkadd(s2, e[8]); s0 = pkadd(s0, e[9]);
    slot(IC<5 * sg + 3>{});
    if constexpr (!NORM && sg == 0) {
#pragma unroll
      for (int i = 0; i < 10; ++i) ph[i] = pk2(e[i][0], e[i][1]);
    }
    s0 = pkadd(pkadd(s0, s1), s2);
    const float sm = s0[0] + s0[1];
    if constexpr (NORM || sg == 1) {
      const float r = __builtin_amdgcn_rcpf(sm) * (NORM ? post : sp);
#pragma unroll
      for (int i = 0; i < 10; ++i) e[i] = pkmul(e[i], f32x2{r, r});
      SB();
#pragma unroll
      for (int i = 0; i < 10; ++i) ph[10 * sg + i] = pk2(e[i][0], e[i][1]);
    }
    sp = sg ? sp : sm;
    slot(IC<5 * sg + 4>{});
  SEND;
  return sp;
}

#define BARE() asm volatile("s_barrier" ::: "memory")
template <int OFF> DEV void dsr(v4u& d, unsigned addr) { asm volatile("ds_read_b128 %0, %1 offset:%2" : "=v"(d) : "v"(addr), "i"(OFF)); }
template <int R, int OFF, bool COH> DEV void lda(rsrc_t rs, unsigned voff, unsigned soff) {
  if constexpr (COH) asm volatile("buffer_load_dwordx4 a[%c0:%c1], %2, %3, %4 offen offset:%5 sc1" :: "i"(R), "i"(R + 3), "v"(voff), "s"(rs), "s"(soff), "i"(OFF) : "memory");
  else asm volatile("buffer_load_dwordx4 a[%c0:%c1], %2, %3, %4 offen offset:%5" :: "i"(R), "i"(R + 3), "v"(voff), "s"(rs), "s"(soff), "i"(OFF) : "memory");
}
template <int R, int OFF> DEV void lda1(rsrc_t rs, unsigned voff, unsigned soff) {
  asm volatile("buffer_load_dword a%c0, %1, %2, %3 offen offset:%4 sc1" :: "i"(R), "v"(voff), "s"(rs), "s"(soff), "i"(OFF) : "memory");
}
template <int R> DEV float rda1() { float t; asm volatile("v_accvgpr_read_b32 %0, a%c1" : "=v"(t) : "i"(R)); return t; }
DEV void ldst(v2u (&st)[4], rsrc_t rs, unsigned voff, unsigned soff) {
  asm volatile("buffer_load_dwordx2 %0, %4, %5, %6 offen sc1\n\tbuffer_load_dwordx2 %1, %4, %5, %6 offen offset:1024 sc1\n\t"
               "buffer_load_dwordx2 %2, %4, %5, %6 offen offset:2048 sc1\n\tbuffer_load_dwordx2 %3, %4, %5, %6 offen offset:3072 sc1"
               : "=&v"(st[0]), "=&v"(st[1]), "=&v"(st[2]), "=&v"(st[3]) : "v"(voff), "s"(rs), "s"(soff) : "memory");
}
template <int N> DEV unsigned st_wait(v2u (&st)[4]) {
  asm volatile("s_waitcnt vmcnt(%4)" : "+v"(st[0]), "+v"(st[1]), "+v"(st[2]), "+v"(st[3]) : "i"(N) : "memory");
  return max3u(max3u(st[0].x, st[0].y, st[1].x), max3u(st[1].y, st[2].x, st[2].y), umax(st[3].x, st[3].y));
}
template <int R, int SKIP> DEV void code_ahead(unsigned l16, int piece) {
  unsigned long long pc;
  asm volatile("s_getpc_b64 %0" : "=s"(pc));
  asm volatile("global_load_dwordx4 a[%c2:%c3], %0, %1" :: "v"(l16 + SKIP + (piece & 15) * 1024), "s"(pc), "i"(R), "i"(R + 3) : "memory");
}
DEV void sbump(unsigned& so) { asm volatile("s_add_u32 %0, %0, 0x1000" : "+s"(so) :: "scc"); }
template <bool Z, int R> DEV void mf32a(f32x16& c, const v4u& a) {
  if constexpr (Z) asm volatile("v_mfma_f32_32x32x16_bf16 %0, %1, a[%c2:%c3], 0" : "=&v"(c) : "v"(a), "i"(R), "i"(R + 3));
  else asm volatile("v_mfma_f32_32x32x16_bf16 %0, %1, a[%c2:%c3], %0" : "+v"(c) : "v"(a), "i"(R), "i"(R + 3));
}
template <bool Z> DEV void mf32v(f32x16& c, const v4u& a, const v4u& b) {
  if constexpr (Z) asm volatile("v_mfma_f32_32x32x16_bf16 %0, %1, %2, 0" : "=&v"(c) : "v"(a), "v"(b));
  else asm volatile("v_mfma_f32_32x32x16_bf16 %0, %1, %2, %0" : "+v"(c) : "v"(a), "v"(b));
}
template <bool Z, int R> DEV void mf16a(f32x4& c, const v4u& b) {
  if constexpr (Z) asm volatile("v_mfma_f32_16x16x32_bf16 %0, a[%c2:%c3], %1, 0" : "=&v"(c) : "v"(b), "i"(R), "i"(R + 3));
  else asm volatile("v_mfma_f32_16x16x32_bf16 %0, a[%c2:%c3], %1, %0" : "+v"(c) : "v"(b), "i"(R), "i"(R + 3));
}
template <bool Z> DEV void mf16v(f32x4& c, const v4u& a, const v4u& b) {
  if constexpr (Z) asm volatile("v_mfma_f32_16x16x32_bf16 %0, %1, %2, 0" : "=&v"(c) : "v"(a), "v"(b));
  else asm volatile("v_mfma_f32_16x16x32_bf16 %0, %1, %2, %0" : "+v"(c) : "v"(a), "v"(b));
}
template <int N> DEV void vmc() { asm volatile("s_waitcnt vmcnt(%0)" :: "i"(N) : "memory"); }
template <int I> DEV void dmaw(rsrc_t rw, unsigned l16, unsigned wb) {
  asm volatile("s_or_b32 m0, %0, %1\n\ts_nop 0\n\tbuffer_load_dwordx4 %2, %3, m0 offen" DSC1 " lds" :: "s"(wb), "i"(I * 4096), "v"(l16), "s"(rw) : "memory", "scc");
}
template <int N> DEV void lgkm() { asm volatile("s_waitcnt lgkmcnt(%0)" :: "i"(N)); }
#define MFMA_SETTLE() asm volatile("s_nop 7\n\ts_nop 3")
DEV bool any_nan(float x) { return __builtin_amdgcn_ballot_w64(x != x) != 0; }
#define PFD 3

template <int S, int NW, int VM, int VI> DEV void p2pair(f32x16& a0, v4u& w0, v4u& w1, unsigned bq, unsigned bcs, rsrc_t r, unsigned l16, unsigned so, const v4u& pv) {
#define P2_M0 "s_waitcnt lgkmcnt(%c[nw])\n\tv_mfma_f32_32x32x16_bf16 %[c], %[w0], a[%c[r0]:%c[r1]], "
#define P2_R0 "\n\tds_read_b128 %[w0], %[a0] offset:%c[o0]\n\t"
#define P2_M1 "v_mfma_f32_32x32x16_bf16 %[c], %[w1], a[%c[r2]:%c[r3]], %[c]"
#define P2_R1 "\n\tds_read_b128 %[w1], %[bq] offset:%c[o1]"
#define P2_IN [nw] "i"(NW), [r0] "i"(4 * S), [r1] "i"(4 * S + 3), [r2] "i"(4 * S + 4), [r3] "i"(4 * S + 7), [a0] "v"(S + 6 == 40 ? bcs : bq), [bq] "v"(bq), \
              [o0] "i"(S + 6 < 40 ? (S + 6) * 1024 : 0), [o1] "i"((S + 7) * 1024), [rs] "s"(r), [l16] "v"(l16), [so] "s"(so), [d0] "i"(VM == 1 ? 160 + 4 * VI : 0), [d1] "i"(VM == 1 ? 163 + 4 * VI : 3), \
              [vo] "i"((VI & 3) * 1024), [dm] "i"(VM == 2 ? VI * 4096 : VM == 3 ? VI : 0), [pv] "v"(pv), [p2] "v"(v2u{pv.x, pv.y})
#define P2_LD "buffer_load_dwordx4 a[%c[d0]:%c[d1]], %[l16], %[rs], %[so] offen offset:%c[vo] sc1\n\t"
#define P2_DMA "s_or_b32 m0, %[so], %[dm]\n\t"
#define P2_DMB "buffer_load_dwordx4 %[l16], %[rs], m0 offen" DSC1 " lds\n\t"
#define P2_ST "buffer_store_dwordx4 %[pv], %[l16], %[rs], %[so] offen offset:%c[dm] sc1\n\t"
#define P2_S2 "buffer_store_dwordx2 %[p2], %[l16], %[rs], %[so] offen sc1\n\t"
  if constexpr (S == 0) {
    static_assert(S != 0 || VM == 0, "");
    asm volatile(P2_M0 "0" P2_R0 P2_M1 P2_R1 : [c] "=&v"(a0), [w0] "+v"(w0), [w1] "+v"(w1) : P2_IN : "memory");
  } else if constexpr (S + 7 < 40) {
    if constexpr (VM == 0) asm volatile(P2_M0 "%[c]" P2_R0 P2_M1 P2_R1 : [c] "+v"(a0), [w0] "+v"(w0), [w1] "+v"(w1) : P2_IN : "memory");
    else if constexpr (VM == 1) asm volatile(P2_M0 "%[c]" P2_R0 P2_LD P2_M1 P2_R1 : [c] "+v"(a0), [w0] "+v"(w0), [w1] "+v"(w1) : P2_IN : "memory");
    else if constexpr (VM == 3) asm volatile(P2_M0 "%[c]" P2_R0 P2_ST P2_M1 P2_R1 : [c] "+v"(a0), [w0] "+v"(w0), [w1] "+v"(w1) : P2_IN : "memory");
    else if constexpr (VM == 4) asm volatile(P2_M0 "%[c]" P2_R0 P2_S2 P2_M1 P2_R1 : [c] "+v"(a0), [w0] "+v"(w0), [w1] "+v"(w1) : P2_IN : "memory");
    else asm volatile(P2_M0 "%[c]\n\t" P2_DMA "ds_read_b128 %[w0], %[a0] offset:%c[o0]\n\t" P2_DMB P2_M1 P2_R1 : [c] "+v"(a0), [w0] "+v"(w0), [w1] "+v"(w1) : P2_IN : "memory", "scc");
  } else if constexpr (S + 6 == 40) {
    static_assert(S + 6 != 40 || VM == 0, "");
    asm volatile(P2_M0 "%[c]" P2_R0 P2_M1 : [c] "+v"(a0), [w0] "+v"(w0), [w1] "+v"(w1) : P2_IN : "memory");
  } else {
    static_assert(VM == 0, "");
    asm volatile(P2_M0 "%[c]\n\t" P2_M1 : [c] "+v"(a0), [w0] "+v"(w0), [w1] "+v"(w1) : P2_IN : "memory");
  }
}
template <bool Z, int RA, int RB> DEV void mf16aa(f32x4& c) {
  if constexpr (Z) asm volatile("v_mfma_f32_16x16x32_bf16 %0, a[%c1:%c2], a[%c3:%c4], 0" : "=v"(c) : "i"(RA), "i"(RA + 3), "i"(RB), "i"(RB + 3));
  else asm volatile("v_mfma_f32_16x16x32_bf16 %0, a[%c1:%c2], a[%c3:%c4], %0" : "+v"(c) : "i"(RA), "i"(RA + 3), "i"(RB), "i"(RB + 3));
}
template <int RB> DEV void mf16va(f32x4& c, const v4u& a) { asm volatile("v_mfma_f32_16x16x32_bf16 %0, %1, a[%c2:%c3], %0" : "+v"(c) : "v"(a), "i"(RB), "i"(RB + 3)); }
template <bool Z, int RB> DEV void mf16vz(f32x4& c, const v4u& a) {
  if constexpr (Z) asm volatile("v_mfma_f32_16x16x32_bf16 %0, %1, a[%c2:%c3], 0" : "=&v"(c) : "v"(a), "i"(RB), "i"(RB + 3));
  else asm volatile("v_mfma_f32_16x16x32_bf16 %0, %1, a[%c2:%c3], %0" : "+v"(c) : "v"(a), "i"(RB), "i"(RB + 3));
}
#define DSW(addr, off, val) asm volatile("ds_write_b128 %0, %1 offset:%2" :: "v"(addr), "v"(val), "i"(off) : "memory")
#define ADM 12
#define ODM 0
#define GDM 10
#define DLY_O 24
#define DLY_X 13
#define HLP 17
#define DLY_OB 28
#define KFP 12
#define KCK 3
#define LW 8
#define DX1 8
#define DO1 12
#define PX 1
#define P2W 13
#define KQS 4
#define DO_0 36
#define DO_1 24
#define DO_2 19
#define DO_3 18
#define DX_0 20
#define DX_1 10
#define DX_2 12
#define KPS 8

__global__ void __launch_bounds__(256, 1) k_main(Params p) {
  extern __shared__ __attribute__((aligned(16))) unsigned char smem[];
  LAS unsigned char* L = (LAS unsigned char*)smem;
  {
    const unsigned long long pc = __builtin_amdgcn_s_getpc();
    const unsigned t16 = __builtin_amdgcn_workitem_id_x() * 16;
    asm volatile("global_load_dwordx4 a[0:3], %0, %1\n\tglobal_load_dwordx4 a[4:7], %0, %1 offset:4080" :: "v"(t16), "s"(pc) : "memory");
    asm volatile("global_load_dwordx4 a[8:11], %0, %1" :: "v"(t16 + 8192 + ((__builtin_amdgcn_workgroup_id_x() >> 3) % 6) * 4096), "s"(pc) : "memory");
    SB();
  }
  const int b = __builtin_amdgcn_workgroup_id_x(), w = __builtin_amdgcn_readfirstlane(__builtin_amdgcn_workitem_id_x() >> 6);
  const int rg = map_rg(b), cg = map_cg(b), half = o_half(cg), c8 = o_c8(cg);
  const rsrc_t rs = __builtin_amdgcn_make_buffer_rsrc(p.ws, 0, WS_TOTAL, 0x00020000);
  asm volatile("" ::: "a255");
  unsigned vKa, vVa, vHv, vImg, vA0, vA2, vB, vS0, vS1, vK0, vK1, vF0, vF1, vKv, vKp, vOv, vOp, vXv, vSt, vB3, vK2, vK3, vO3, vX3, vSp, vA0x, vA2x, vBx;
  v4u vSel;
  auto addresses = [&]() __attribute__((always_inline)) {
    RELANE();
    const int g = lane >> 4, hh = w >> 1, dt = w & 1, q = (lane >> 2) & 3, p4 = lane & 3, r = lane & 15;
    const unsigned r_ = h * 144 + 2 + 32 * w + tok;
    vKa = L_K + r_ * 48; vVa = L_V + r_ * 80; vHv = (w == 3 && tok >= 16) ? (h * 16 + (tok - 16)) * 16 : 0x80000000u;
    const unsigned t_ = 32 * w + tok, key = (t_ & 7) ^ (((t_ >> 3) & 1) << 2);
    vImg = L_SCR + (h * 128 + t_) * 128 + (key << 4);
    const unsigned k0 = q ^ ((g & 1) << 2), rb0 = L_SCR + (hh * 128 + 8 * g + q) * 128, cB = dt ? 7 : 5 + (p4 >> 1);
    vA0 = rb0 + ((p4 ^ k0) << 4); vA2 = rb0 + ((4 ^ k0) << 4) + (p4 & 1) * 8; vB = rb0 + ((cB ^ k0) << 4) + (p4 & 1) * 8;
    vS0 = dt ? (r < 8 ? 2560 + ((hh * 5 + g) * 8 + r) * 16 : 0x80000000u) : ((hh * 5 + g) * 16 + r) * 16;
    vS1 = g ? 0x80000000u : dt ? (r < 8 ? 2560 + ((hh * 5 + 4) * 8 + r) * 16 : 0x80000000u) : ((hh * 5 + 4) * 16 + r) * 16;
    const unsigned ty0 = L_KVA + r * 16 + g * 8, ty1 = L_KVA + (r >> 3) * 400 + 256 + (r & 7) * 16 + g * 8;
    vK0 = w < 3 ? ty0 + w * 1600 : ty1 + 800;
    vK1 = w < 2 ? ty0 + w * 1600 + 800 : ty1 + (w == 2 ? 0 : 2400);
    if (h) vK0 = vK1 = L_RED + 8192 + tid * 8;
    vK2 = vK0 + (w < 3 ? 400 : 800); vK3 = vK1 + (w < 2 ? 400 : 800);
    vA0x = (vA0 ^ 64) + 512; vA2x = (vA2 ^ 64) + 512; vBx = (vB ^ 64) + 512;
#pragma unroll
    for (int j = 0; j < 4; ++j) vSel[j] = r == 2 * j ? 0x00003F80u : r == 2 * j + 1 ? 0x3F800000u : 0u;
    const bool match = ((tok >> 2) & 1) == h;
    const unsigned row = 4 * (tok >> 3) + (tok & 3);
    vF0 = L_KVA + h * 2000 + (match ? row * 16 : 384); vF1 = L_KVA + h * 2000 + (match && row < 8 ? 256 + row * 16 : 384);
    vKv = g * 4096 + r * 16;
    vKp = (lane >> 2) * 4096 + 3712 + (lane & 3) * 4;
    vOv = (tok >> 4) * 20480 + (tok & 15) * 16 + h * 2048; vOp = g * 20480 + q * 256 + p4 * 4;
    vXv = ((w & 1) * 16 + r) * 16 + g * 1024; vSt = h * 4096 + tok * 8; vB3 = 2048 + h * 384 + tok * 16;
    vO3 = h ? 0x80000000u : vOv; vX3 = (g & 1) ? 0x80000000u : (vXv & 0x1f0) + (h << 9); vSp = g ? 0x80000000u : r * 8;
    asm volatile("" : "+v"(vK2), "+v"(vK3), "+v"(vO3), "+v"(vX3), "+v"(vSp), "+v"(vA0x), "+v"(vA2x), "+v"(vBx), "+v"(vSel));
    asm volatile("" : "+v"(vKa), "+v"(vVa), "+v"(vHv), "+v"(vImg), "+v"(vA0), "+v"(vA2), "+v"(vB), "+v"(vS0), "+v"(vS1), "+v"(vK0));
    asm volatile("" : "+v"(vK1), "+v"(vF0), "+v"(vF1), "+v"(vKv), "+v"(vKp), "+v"(vOv), "+v"(vOp), "+v"(vXv), "+v"(vSt), "+v"(vB3));
    if (tid < 10) sts128(L, L_KVA + tid * 400 + 384, v4u{0u, 0u, 0u, 0u});
  };
  unsigned xr[10];
  Tmo tm;
  { RELANE();
    const int l = 0; (void)l;
    STAMP(0);
    constexpr unsigned SG_B = L_P0 + 2048, SG_O = SG_B + 10240, SG_P = SG_O + 10240, OOR = 0x80000000u;
    const int j = (tid >> 4) & 7, cc = lane & 15, seg = (cc * 13) >> 6, g = lane >> 4;
    v4u ra[3], rb[3], ro[3], rp;
    const unsigned wcol = (seg * 640 + 40 * cg + 8 * (cc - 5 * seg)) * 2;
    unsigned wrow[3];
#pragma unroll
    for (int i = 0; i < 3; ++i) {
      const int kap = 2 * i + (w >> 1) < 4 ? 2 * i + (w >> 1) : 4, lc = 5 * (rg & 1) + kap;
      const int t8 = lc >> 3, rb0 = 80 * (rg >> 1) + (1 - t8) * (20 * (lc >> 1) + 8 * (lc & 1)) + t8 * (40 * (lc - 8) + 16);
      wrow[i] = (rb0 + j + t8 * (16 * (j >> 2))) * 3840 + wcol;
    }
    { const rsrc_t rA = __builtin_amdgcn_make_buffer_rsrc((void*)p.wqA, 0, 640 * 1920 * 2, 0x00020000);
#pragma unroll
      for (int i = 0; i < 3; ++i) ra[i] = ldg(rA, wrow[i], 0); }
    load_x_in(p.x, rg, cg, w, lane, xr);
    const unsigned ep = __builtin_amdgcn_raw_buffer_load_b32(rs, 0, WS_CTRL + 128, SC1);
    FENCE(); SB();
    { const rsrc_t rB = __builtin_amdgcn_make_buffer_rsrc((void*)p.wqB, 0, 640 * 1920 * 2, 0x00020000);
      const rsrc_t rO = __builtin_amdgcn_make_buffer_rsrc((void*)(half ? p.woB : p.woA), 0, 640 * 640 * 2, 0x00020000);
      const rsrc_t rP = __builtin_amdgcn_make_buffer_rsrc((void*)(p.wphi + cg * 800), 0, 1600, 0x00020000);
#pragma unroll
      for (int i = 0; i < 3; ++i) rb[i] = ldg(rB, wrow[i], 0);
#pragma unroll
      for (int i = 0; i < 3; ++i) { const int so = 2 * i + (w >> 1) < 4 ? 2 * i + (w >> 1) : 4, t4 = so >> 2;
        ro[i] = ldg(rO, (40 * rg + (1 - t4) * (20 * (so >> 1) + 8 * (so & 1)) + t4 * 16 + j + t4 * (16 * (j >> 2))) * 1280 + (80 * c8 + 8 * (cc < 10 ? cc : 0)) * 2, 0); }
      rp = ldg(rP, tid * 16, 0);
    FENCE(); SB();
    STAMP(53);
    const unsigned sa = tid * 16;
#pragma unroll
    for (int i = 0; i < 3; ++i) if (i < 2 || w < 2) sts128(L, sa + i * 4096, ra[i]);
    BAR();
    STAMP(54);
    { v4u fr[3];
      build_wq_read(L, 0, w, lane, fr);
      publish_x(rs, WS_X, WS_ST0, rg, cg, w, lane, xr);
      build_wq_store(rs, 0, rg, cg, w, lane, 0, w < 3 ? 32 : 24, fr); }
    tm.ws = p.ws; tm.epoch = __builtin_amdgcn_readfirstlane(ep) + 0x9E3779B1u;
    STAMP(55);
#pragma unroll
    for (int i = 0; i < 3; ++i) if (i < 2 || w < 2) { sts128(L, SG_B + sa + i * 4096, rb[i]); sts128(L, SG_O + sa + i * 4096, ro[i]); }
    if (w < 2) sts128(L, SG_P + sa, rp);
    }
    DRAIN();
    BAR();
    if (tid == 0) __builtin_amdgcn_raw_buffer_store_b32(tm.epoch, rs, 0, WS_FLAG + (cg * 16 + rg) * 128, SC1);
    STAMP(60);
    v4u fv[4], fb[3], fo[2];
    {
      const int sg = w >> 1, hw = w & 1;
      v4u bf[2], af[4];
#pragma unroll
      for (int ct = 0; ct < 2; ++ct) {
        const int q = (lane >> 2) & 3, pp = lane & 3, fcol = ct ? 16 : 4 * pp;
        const int d0 = 8 * g + q < 19 ? 8 * g + q : 19, d1 = 8 * g + 4 + q < 19 ? 8 * g + 4 + q : 19;
        const v2u t0 = ldstr(L, SG_P + hw * 800 + d0 * 40 + fcol * 2), t1 = ldstr(L, SG_P + hw * 800 + d1 * 40 + fcol * 2);
        bf[ct] = v4u{g < 3 ? t0.x : 0u, g < 3 ? t0.y : 0u, g < 2 ? t1.x : 0u, g < 2 ? t1.y : 0u};
      }
#pragma unroll
      for (int i = 0; i < 4; ++i) { const int r = (i < 2 ? 8 * (cc >> 2) : 32) + 4 * (i & 1) + (cc & 3); const unsigned a = SG_B + r * 256 + sg * 80 + hw * 40 + g * 16;
        const v2u t0 = lds64(L, a), t1 = lds64(L, a + 8); af[i] = v4u{t0.x, t0.y, t1.x, t1.y}; }
      build_wq_read(L, SG_B, w, lane, fb);
      { const int q = (lane >> 2) & 3, pp = lane & 3;
#pragma unroll
        for (int it = 0; it < 2; ++it) {
          const int pc = it * 16 + (tid >> 4), pcc = pc < 25 ? pc : 24, so = (pcc * 13) >> 6, ft = pcc - 5 * so;
          const unsigned a = SG_O + (so * 8 + q) * 256 + 40 * pp + 8 * ft;
          const v2u t0 = ldstr(L, a), t1 = ldstr(L, a + 1024);
          fo[it] = v4u{t0.x, t0.y, t1.x, t1.y};
        } }
      f32x4 d[8];
      asm volatile("s_nop 1" : "+v"(bf[0]), "+v"(bf[1]), "+v"(af[0]), "+v"(af[1]), "+v"(af[2]), "+v"(af[3]));
      SFOR(i, 8) mf16v<true>(d[i], af[i & 3], bf[i >> 2]); SEND;
      asm volatile("s_nop 7");
      SB();
#pragma unroll
      for (int i = 0; i < 4; ++i) fv[i] = v4u{pk2(d[2 * i][0], d[2 * i][1]), pk2(d[2 * i][2], d[2 * i][3]), pk2(d[2 * i + 1][0], d[2 * i + 1][1]), pk2(d[2 * i + 1][2], d[2 * i + 1][3])};
    }
    STAMP(58);
    { const int sg = w >> 1, hw = w & 1;
#pragma unroll
      for (int ct = 0; ct < 2; ++ct) {
        const int row = ct ? (sg ? 0 : 16) + 4 * hw + (cc & 3) : 8 * (cc >> 2) + 4 * hw + (cc & 3);
        const unsigned tb = ct ? 2048 : (sg ? 0 : L_P0), ts = (ct || sg) ? KSTEP : 1024, th = ct ? 384 : 512;
        const bool in = ct == 0 || cc < 4;
#pragma unroll
        for (int u = 0; u < 2; ++u) { const int c = 5 * rg + (u ? 4 : g);
          stg_sc1(rs, (in && (u == 0 || g == 0)) ? (c >> 1) * ts + (c & 1) * th + row * 16 : OOR, WS_WQ + (16 + cg) * WQ_SZ + tb, fv[2 * ct + u]); }
        float cs = sum8(fv[2 * ct], 0.f);
        cs = g ? cs : sum8(fv[2 * ct + 1], cs);
        { auto t = __builtin_amdgcn_permlane16_swap(f2u(cs), f2u(cs), false, false); cs = u2f(t[0]) + u2f(t[1]); }
        { auto t = __builtin_amdgcn_permlane32_swap(f2u(cs), f2u(cs), false, false); cs = u2f(t[0]) + u2f(t[1]); }
        __builtin_amdgcn_raw_buffer_store_b32(f2u(cs), rs, (in && g == 0) ? row * 4 : OOR, WS_CSP + ((16 + cg) * 16 + rg) * 512 + (ct ? 3 : sg) * 128, SC1);
      }
      if (w >= 2) build_wq_store(rs, 1, rg, cg, w, lane, w == 2 ? 0 : 8, w == 2 ? 32 : 16, fb);
#pragma unroll
      for (int it = 0; it < 2; ++it) { const int pc = it * 16 + (tid >> 4);
        stg_sc1(rs, pc < 25 ? pc * 1024 + cc * 16 : OOR, WS_WO + (half * 8 + c8) * WO_SZ + (rg >> 2) * 25600 + (rg & 3) * 256, fo[it]); }
    }
    poison_o(rs, WS_O, rg, cg, w, lane);
    stg_sc1(rs, l16, WS_KV + (cg * 16 + rg) * 4096 + w * 1024, v4u{SENT, SENT, SENT, SENT});
    stg_sc1(rs, l16, WS_HALO + (rg * 16 + cg) * 4096 + w * 1024, v4u{SENT, SENT, SENT, SENT});
    addresses();
    STAMP(61);
    if (w == 1) wait_flags(rs, (h == 0 ? cc * 16 + rg : cg * 16 + cc) * 128, WS_FLAG, tm);
    BAR();
    STAMP(62);
    code_ahead<100, 0>(l16, (b >> 3) * 4 + w);
    const rsrc_t rw = __builtin_amdgcn_make_buffer_rsrc(p.ws + WS_WQ + cg * WQ_SZ, 0, WQ_SZ, 0x00020000);
    SFOR(i, 22 + LW) dmaw<i>(rw, l16, w * 1024); SEND;
    float csv[16];
#pragma unroll
    for (int r = 0; r < 16; ++r) csv[r] = u2f(__builtin_amdgcn_raw_buffer_load_b32(rs, (tid & 127) * 4 + (r & 7) * 512, WS_CSP + cg * 8192 + (r >> 3) * 4096, SC1));
    FENCE(); SB();
    STAMP(63);
    FENCE(); SB();
    STAMP(57);
    { float cs = 0.f;
#pragma unroll
      for (int r = 0; r < 16; ++r) cs += csv[r];
      const unsigned ch = pk2(cs, 0.f) & 0xffffu, cl = pk2(cs - lo16(ch), 0.f) & 0xffffu;
      asm volatile("" :: "v"(cs));
      if (tid < 128) sts128(L, L_CS + tid * 16, v4u{ch | (cl << 16), ch, 0u, 0u}); }
    BAR();
    if (tid == 0) __builtin_amdgcn_raw_buffer_store_b32(tm.epoch, rs, 0, WS_FLAG + (256 + cg * 16 + rg) * 128, SC1);
  }

  unsigned sX = WS_X + (rg * 4 + w) * 40960, sSt = WS_ST0 + (rg * 1024 + w * 32) * 8, sWo = WS_WO + (c8 * 4 + w) * 25600;
  unsigned sOp = WS_O + ((rg * 8 + 2 * w) * 4 + (cg >> 2)) * 5120 + (cg & 3) * 256, sOr = WS_O + ((rg * 8 + half * 4) * 4 + w) * 5120;
  unsigned sXp = WS_X + XB + ((rg * 4 + half * 2 + (w >> 1)) * 40 + 5 * c8) * 1024, sSp = WS_ST1 + ((rg * 8 + c8) * 128 + half * 64 + w * 16) * 8;
  pin_s(sX); pin_s(sSt); pin_s(sWo); pin_s(sOp); pin_s(sOr); pin_s(sXp); pin_s(sSp);
  auto layer = [&](auto m_, const int ll) __attribute__((always_inline)) {
    constexpr int m = decltype(m_)::v;
    const int l = 2 * ll + m;
    const rsrc_t rw = __builtin_amdgcn_make_buffer_rsrc(p.ws + WS_WQ + ((m ^ 1) * 16 + cg) * WQ_SZ, 0, l < 3 ? WQ_SZ : 0u, 0x00020000);
    const unsigned wb1k = w * 1024;
#define DM(i) do { SB(); dmaw<(i)>(rw, l16, wb1k); SB(); } while (0)
    unsigned wos;
#define LDWO(i) do { lda<160 + 4 * (i), ((i) & 3) * 1024, true>(rs, l16, wos); if constexpr (((i) & 3) == 3) sbump(wos); } while (0)
#define P2VM(pp) ((pp) >= 2 && (pp) <= 16 && (pp) != 6 && (pp) != 11 ? 1 : 0)
#define P2VI(pp) (P2VM(pp) ? 7 + (pp) - 2 - ((pp) > 6) - ((pp) > 11) : 0)
#define P2CNT(pp) ((pp) < 2 ? 0 : (pp) - 1 - ((pp) >= 6) - ((pp) >= 11))
#define P2PAIR(pp, nw) do { p2pair<2 * (pp), nw, P2VM(pp), P2VI(pp)>(a0, wq[(2 * (pp)) % 6], wq[(2 * (pp) + 1) % 6], bq, bcq, rs, l16, wos, mfr); \
                            if constexpr (P2VM(pp) == 1 && (P2VI(pp) & 3) == 3) sbump(wos); } while (0)
    v4u wo24, mfr, wq[6];
    unsigned bq, bcq;
    f32x16 a0, a1, a2, a3;
    float rstd, sd, o[20];
    unsigned osb[4], ops, cXp, cSp, cOp, cWo;
    {
      RELANE();
      STAMP(1 + 12 * l);
      asm volatile("" : "+v"(vSt), "+v"(vB3));
      const unsigned xs = sX, sts = sSt;
#define LDX(s) do { lda<4 * (s), ((s) & 3) * 1024, true>(rs, l16, xso); if constexpr (((s) & 3) == 3) sbump(xso); } while (0)
      const unsigned stv = vSt;
      constexpr int NB = PFD + 1, HI = 23;
      constexpr int XR = 15 - XW;
      const unsigned b12 = l16, b3 = vB3;
      unsigned b12h, b3h, bcs;
      unsigned f1 = 0, f1v = 0;
      const rsrc_t rl = __builtin_amdgcn_make_buffer_rsrc(p.ws + WS_WQ + (m * 16 + cg) * WQ_SZ, 0, WQ_SZ, 0x00020000);
      auto pass1 = [&](auto hot_) __attribute__((always_inline)) {
        constexpr bool HOT = decltype(hot_)::v;
        v2u st[4];
        unsigned xso;
        auto window = [&](auto bar_) __attribute__((always_inline)) {
          xso = xs;
          ldst(st, rs, stv, sts);
          SFOR(s, XW) SB(); LDX(s); if constexpr (decltype(bar_)::v && (s == 1 || s == 5)) { SB(); BARE(); } SEND;
          FENCE(); SB();
        };
        wos = sWo;
        window(IC<HOT>{});
        v4u wb[NB][3];
        auto rd = [&](auto s_, auto t_) __attribute__((always_inline)) {
          constexpr int s = decltype(s_)::v, t = decltype(t_)::v;
          if constexpr (s < 40) dsr<(s < HI ? s : s - HI) * KSTEP + (t == 1 ? 1024 : 0)>(wb[s % NB][t], t == 2 ? (s < HI ? b3 : b3h) : (s < HI ? b12 : b12h));
          else if constexpr (s == 40) dsr<(t + 1) * 512>(wb[s % NB][t], bcs);
        };
        SFOR(j, PFD) rd(IC<j>{}, IC<2>{}); rd(IC<j>{}, IC<1>{}); rd(IC<j>{}, IC<0>{}); SEND;
        if (__builtin_expect(any_sent(st_wait<XW>(st)), 0)) {
          unsigned n = 0;
          do { CNT(0); poll_probe<200>(rs, (lane >> 3) * 1024 + (lane & 4) * 32 + 4, sts, tm); window(IC<0>{}); } while (any_sent(st_wait<XW>(st)) && ++n < 64);
        }
        if (HOT && m == 1 && l == 1 && w < 2) {
          float cs = 0.f;
          SFOR(r, 16) cs += rda1<210 + r>(); SEND;
          const unsigned ch = pk2(cs, 0.f) & 0xffffu, cl = pk2(cs - lo16(ch), 0.f) & 0xffffu;
          sts128(L, L_CS + 2048 + tid * 16, v4u{ch | (cl << 16), ch, 0u, 0u});
        }
        float sa, sq, nm;
        auto stat = [&](auto s_) __attribute__((always_inline)) {
          constexpr int s = decltype(s_)::v;
          if constexpr (s == 1) { sa = u2f(st[0].x) + u2f(st[1].x); asm volatile("" : "+v"(sa)); sq = u2f(st[0].y) + u2f(st[1].y); asm volatile("" : "+v"(sq)); }
          if constexpr (s == 2) { sa += u2f(st[2].x); asm volatile("" : "+v"(sa)); sq += u2f(st[2].y); asm volatile("" : "+v"(sq)); }
          if constexpr (s == 3) { sa += u2f(st[3].x); asm volatile("" : "+v"(sa)); sq += u2f(st[3].y); asm volatile("" : "+v"(sq)); }
          if constexpr (s == 5) { auto x0 = __builtin_amdgcn_permlane32_swap(f2u(sa), f2u(sa), false, false); nm = u2f(x0[0]) + u2f(x0[1]); asm volatile("" : "+v"(nm)); }
          if constexpr (s == 6) { auto x1 = __builtin_amdgcn_permlane32_swap(f2u(sq), f2u(sq), false, false); sq = u2f(x1[0]) + u2f(x1[1]); asm volatile("" : "+v"(sq)); }
          if constexpr (s == 7) { nm *= -1.f / 640.f; sq = sq * (1.f / 640.f) - nm * nm; asm volatile("" : "+v"(nm), "+v"(sq)); }
          if constexpr (s == 8) { sq = __builtin_fmaxf(sq, 0.f) + 1e-5f; rstd = __builtin_amdgcn_rsqf(sq); asm volatile("" : "+v"(rstd), "+v"(sq)); }
          if constexpr (s == 9) { sd = sq * rstd; mfr.x = pk2(nm, nm); asm volatile("" : "+v"(sd), "+v"(mfr.x)); }
          if constexpr (s == 10) { mfr.y = pk2(nm - lo16(mfr.x), 0.f); asm volatile("" : "+v"(mfr.y)); }
          if constexpr (s == 11) { mfr = v4u{h ? 0u : mfr.x, h ? 0u : mfr.y, 0u, 0u}; asm volatile("" : "+v"(mfr)); }
          if constexpr (s == 12) { b12h = b12 + HI * KSTEP; b3h = b3 + HI * KSTEP; asm volatile("" : "+v"(b12h), "+v"(b3h)); }
          if constexpr (s == 13) { bcs = L_CS + m * 2048 + (l16 & 0x1f0); bq = L_P0 + l16; asm volatile("" : "+v"(bcs), "+v"(bq)); bcq = bcs; }
        };
        SFOR(s, 40)
          SB();
          if constexpr (HOT && m == 0 && s == 27) {
            if (l == 0) { const int ln = fresh_lane(); f1v = (ln < 16 ? ln * 16 + rg : (ln < 32 ? cg : (c8 >> 2) * 8 + ((ln >> 4) & 1) * 4 + (c8 & 3)) * 16 + (ln & 15)) * 128; probe_issue(f1, rs, f1v, WS_FLAG + 256 * 128); }
            SB();
          }
          if constexpr (HOT && m == 0 && s == 33) {
            if (l == 0) {
              probe_wait<6>(f1);
              if (__builtin_amdgcn_ballot_w64(f1 != tm.epoch) != 0) wait_flags(rs, f1v, WS_FLAG + 256 * 128, tm);
            }
            SB();
          }
          if constexpr (HOT && s == 10) { BARE(); SB(); }
          constexpr int c = s % NB;
          constexpr int issued = s <= XR ? XW + 2 * s : (15 + s < 40 ? 15 + s : 40);
          asm volatile("s_waitcnt vmcnt(%0) lgkmcnt(%1)" :: "i"(issued - 1 - s + (s > 25 ? s - 25 : 0)), "i"(s == 39 ? 6 : 3 * ((s + PFD <= 41 ? PFD : 41 - s) - 1)) : "memory");
          mf32a<s == 0, 4 * s>(a1, wb[c][0]);
          if constexpr (HOT && s == 0) STAMP(2 + 12 * l);
          rd(IC<s + PFD>{}, IC<2>{});
          if constexpr (s < XR) LDX(issued);
          SB();
          mf32a<s == 0, 4 * s>(a2, wb[c][1]);
          rd(IC<s + PFD>{}, IC<1>{});
          stat(IC<s>{});
          SB();
          mf32a<s == 0, 4 * s>(a3, wb[c][2]);
          rd(IC<s + PFD>{}, IC<0>{});
                    if constexpr (s < 25) LDX(issued + (s < XR ? 1 : 0));
          else if constexpr (s < 32) dmaw<30 + (s >= 25 ? s - 25 : 0)>(rl, l16, wb1k);
          else if constexpr (s == 32) dmaw<0>(rl, l16, wb1k + (w < 2 ? 37 : 36) * 4096);
          else LDWO(s - 33);
          if constexpr (s >= 38) { dsr<(s - 38) * 3072>(wq[(s - 38) * 3], bq); dsr<(s - 38) * 3072 + 1024>(wq[(s - 38) * 3 + 1], bq); dsr<(s - 38) * 3072 + 2048>(wq[(s - 38) * 3 + 2], bq); }
        SEND;
        SB();
        lgkm<6>();
        mf32v<false>(a1, wb[40 % NB][0], mfr); mf32v<false>(a3, wb[40 % NB][2], mfr); mf32v<false>(a2, wb[40 % NB][1], mfr);
        P2PAIR(0, 4); P2PAIR(1, 4);
        asm volatile("s_nop 3");
        SB();
        if constexpr (!HOT) lgkm<0>();
      };
      pass1(IC<1>{});
      if (__builtin_expect(any_nan(a1[0]), 0)) {
        unsigned att = 0;
        lgkm<0>();
        do { CNT(1); pass1(IC<0>{}); } while (any_nan(a1[0]) && ++att < 32);
      }
      STAMP(3 + 12 * l);
    }
    vmc<7>();
    BARE();
    if (l == 0) {
      RELANE();
    v4u PV;
    asm volatile("v_mov_b32 %0, -1\n\tv_mov_b32 %1, -1\n\tv_mov_b32 %2, -1\n\tv_mov_b32 %3, -1" : "=v"(PV.x), "=v"(PV.y), "=v"(PV.z), "=v"(PV.w));
      stg_sc1(rs, vXv, sXp, PV); stg_sc1(rs, vXv + 512, sXp, PV); stg_sc1(rs, vX3, sXp + 4096, PV);
      __builtin_amdgcn_raw_buffer_store_b64(v2u{PV.x, PV.y}, rs, vSp, sSp, SC1);
    }
#pragma unroll
    for (int i = 0; i < 4; ++i) osb[i] = sOr + ((w + 1 + i) & 3) * 20480;
    ops = sOr + 4096; cWo = sWo + ((7 + P2W) >> 2) * 4096; cXp = sXp; cSp = sSp; cOp = sOp;
    sX += XB; sSt += STB; sWo += m ? -8 * WO_SZ : 8 * WO_SZ; sOp += XB; sOr += XB; sXp += XB; sSp += STB;
    constexpr int NDM2 = 0;
    if constexpr (m == 0) {
      RELANE();
      asm volatile("" : "+v"(vKa), "+v"(vVa), "+v"(vHv));
      v4u kc0, kc1, kc2, hd[4];
      const unsigned hsrc = WS_HALO + (((l >> 1) * 16 + rg - 1) * 16 + cg) * 4096;
#define VCH(i) ((i) < 4 ? v4u{f2u(a2[4 * (i)]), f2u(a2[4 * (i) + 1]), f2u(a2[4 * (i) + 2]), f2u(a2[4 * (i) + 3])} : v4u{f2u(a3[4]), f2u(a3[5]), f2u(a3[6]), f2u(a3[7])})
      {
        f32x2 t[10];
#pragma unroll
        for (int i = 0; i < 8; ++i) t[i] = pkmul(f32x2{a1[2 * i], a1[2 * i + 1]}, f32x2{rstd, rstd});
#pragma unroll
        for (int i = 0; i < 2; ++i) t[8 + i] = pkmul(f32x2{a3[2 * i], a3[2 * i + 1]}, f32x2{rstd, rstd});
        kc0 = v4u{pk2(t[0][0], t[0][1]), pk2(t[1][0], t[1][1]), pk2(t[2][0], t[2][1]), pk2(t[3][0], t[3][1])};
        kc1 = v4u{pk2(t[4][0], t[4][1]), pk2(t[5][0], t[5][1]), pk2(t[6][0], t[6][1]), pk2(t[7][0], t[7][1])};
        kc2 = v4u{pk2(t[8][0], t[8][1]), pk2(t[9][0], t[9][1]), 0u, 0u};
#pragma unroll
        for (int i = 0; i < 8; ++i) { const f32x2 u = pkmul(f32x2{a2[2 * i], a2[2 * i + 1]}, f32x2{rstd, rstd}); a2[2 * i] = u[0]; a2[2 * i + 1] = u[1]; }
#pragma unroll
        for (int i = 2; i < 4; ++i) { const f32x2 u = pkmul(f32x2{a3[2 * i], a3[2 * i + 1]}, f32x2{rstd, rstd}); a3[2 * i] = u[0]; a3[2 * i + 1] = u[1]; }
        asm volatile("" : "+v"(kc0), "+v"(kc1), "+v"(kc2), "+v"(a2), "+v"(a3)); }
      unsigned qp[10];
      const rsrc_t rh = __builtin_amdgcn_make_buffer_rsrc(p.ws + hsrc, 0, rg > 0 ? 4096u : 0u, 0x00020000);
      if (w == 3) {
        const unsigned hs = hsrc + 65536;
        stg_sc1(rs, vHv, hs, kc0); stg_sc1(rs, vHv + 512, hs, kc1); stg_sc1(rs, vHv + 1024, hs, kc2);
#pragma unroll
        for (int i = 0; i < 5; ++i) stg_sc1(rs, vHv + 1536 + i * 512, hs, VCH(i));
      }
      SFOR(p_, 18)
        constexpr int pp = p_ + 2;
        SB();
        constexpr int nwr = (pp - 1 >= 2 ? (pp - 1 > 16 ? 16 : pp - 1) / 2 : 0) - (pp - 4 >= 2 ? (pp - 4 > 16 ? 16 : pp - 4) / 2 : 0);
        P2PAIR(pp, (2 * pp + 6 <= 41 ? 4 : 39 - 2 * pp) + nwr);
        if constexpr (pp == 2) DSW(vKa, 14 * 48, kc0);
        if constexpr (pp == 4) DSW(vKa, 14 * 48 + 16, kc1);
        if constexpr (pp == 6) DSW(vKa, 14 * 48 + 32, kc2);
        if constexpr (pp >= 8 && pp <= 16 && !(pp & 1)) { constexpr int vi = pp >= 8 ? (pp - 8) / 2 : 0; DSW(vVa, 14 * 80 + vi * 16, VCH(vi)); }
        if constexpr (pp == 11) { qp[8] = pk2(a3[8], a3[9]); qp[9] = pk2(a3[10], a3[11]); asm volatile("" : "+v"(qp[8]), "+v"(qp[9])); }
        if constexpr (pp == HLP) {
          if (w == 0) {
#pragma unroll
            for (int i = 0; i < 4; ++i) hd[i] = ldg_sc1(rh, l16 + i * 1024, 0);
          }
        }
      SEND;
      SB();
      lgkm<0>();
      mf32v<false>(a0, wq[40 % 6], mfr);
      vmc<P2W>();
      BARE();
      const unsigned ka = vKa, va = vVa;
      const float fq = rstd * (LOG2E * 0.22360679774997896f);
      STAMP(5 + 12 * l);
      if (w == 0) {
        const unsigned q_ = ((lane >> 4) & 1) * 144 + (lane & 15);
        unsigned h0 = L_K + q_ * 48 + h * 16, h1 = h ? L_V + q_ * 80 : L_K + q_ * 48 + 32, h2 = L_V + q_ * 80 + 16 + h * 16;
        asm volatile("" : "+v"(h0), "+v"(h1), "+v"(h2));
        unsigned mm = 0;
#pragma unroll
        for (int i = 0; i < 4; ++i) mm = mx4(mm, hd[i]);
        if (__builtin_expect(any_sent(mm), 0)) {
          unsigned sp = 0;
          do {
            FENCE();
            mm = 0;
#pragma unroll
            for (int i = 0; i < 4; ++i) hd[i] = ldg_sc1(rh, l16 + i * 1024, 0);
#pragma unroll
            for (int i = 0; i < 4; ++i) mm = mx4(mm, hd[i]);
            CNT(2);
          } while (any_sent(mm) && !give_up(sp, tm));
        }
        sts128(L, h0, hd[0]);
        sts128(L, h1, hd[1]);
        sts128(L, h2, hd[2]);
        sts128(L, h2 + 32, hd[3]);
      }
      STAMP(4 + 12 * l);
      float lg[8];
      v4u kb[8][2], vb[8][5];
      v2u kt[8];
      auto rdk = [&](auto j_) __attribute__((always_inline)) { constexpr int j = decltype(j_)::v;
        kb[j][0] = lds128(L, ka + (7 - j) * 96); kb[j][1] = lds128(L, ka + (7 - j) * 96 + 16); kt[j] = lds64(L, ka + (7 - j) * 96 + 32); };
      auto rdv = [&](auto j_) __attribute__((always_inline)) { constexpr int j = decltype(j_)::v;
#pragma unroll
        for (int i = 0; i < 5; ++i) vb[j][i] = lds128(L, va + (7 - j) * 160 + i * 16); };
      SB();
      rdk(IC<0>{}); rdk(IC<1>{}); rdk(IC<2>{}); rdk(IC<3>{});
      SB();
#pragma unroll
      for (int i = 0; i < 8; ++i) qp[i] = pk2(a0[2 * i], a0[2 * i + 1]);
      SFOR(jj, 4)
        SB();
        if constexpr (jj < ADM) DM(NDM2 + jj);
        if constexpr (jj < 2) { rdk(IC<2 * jj + 4>{}); rdk(IC<2 * jj + 5>{}); } else rdv(IC<jj - 2>{});
        SB();
        float s0 = 0.f, s1 = 0.f;
#pragma unroll
        for (int i = 0; i < 8; ++i) { s0 = dot2(kb[2 * jj][i >> 2][i & 3], qp[i], s0); s1 = dot2(kb[2 * jj + 1][i >> 2][i & 3], qp[i], s1); }
#pragma unroll
        for (int i = 0; i < 2; ++i) { s0 = dot2(kt[2 * jj][i], qp[8 + i], s0); s1 = dot2(kt[2 * jj + 1][i], qp[8 + i], s1); }
        asm volatile("" : "+v"(s0), "+v"(s1));
        lg[2 * jj] = s0; lg[2 * jj + 1] = s1;
      SEND;
      SB();
      if constexpr (4 < ADM) DM(NDM2 + 4);
      rdv(IC<2>{});
      SB();
      if (rg == 0 && w == 0) {
#pragma unroll
        for (int j = 1; j < 8; ++j) lg[j] = tok >= 2 * j ? lg[j] : -__builtin_inff();
      }
      const float mx = max3f(max3f(lg[0], lg[1], lg[2]), max3f(lg[3], lg[4], lg[5]), max3f(lg[6], lg[7], lg[7]));
      const float nb = -fq * mx;
#pragma unroll
      for (int j = 0; j < 8; j += 2) { const f32x2 t = pkfma(f32x2{lg[j], lg[j + 1]}, f32x2{fq, fq}, f32x2{nb, nb}); lg[j] = t[0]; lg[j + 1] = t[1]; }
      SB();
#pragma unroll
      for (int j = 0; j < 8; ++j) lg[j] = __builtin_amdgcn_exp2f(lg[j]);
      SB();
      if constexpr (5 < ADM) DM(NDM2 + 5);
      SB();
      const float inv = __builtin_amdgcn_rcpf(((lg[0] + lg[1]) + (lg[2] + lg[3])) + ((lg[4] + lg[5]) + (lg[6] + lg[7])));
      f32x2 oa[10];
      SFOR(j, 8)
        SB();
        if constexpr (6 + j < ADM) DM(NDM2 + 6 + j);
        if constexpr (j < 5) rdv(IC<j + 3>{});
        SB();
        const float pj = lg[j];
        SFOR(i, 5)
          const f32x4 v = __builtin_bit_cast(f32x4, vb[j][i]);
          if constexpr (j == 0) { oa[2 * i] = pkmul(f32x2{pj, pj}, f32x2{v[0], v[1]}); oa[2 * i + 1] = pkmul(f32x2{pj, pj}, f32x2{v[2], v[3]}); }
          else { oa[2 * i] = pkfma(f32x2{pj, pj}, f32x2{v[0], v[1]}, oa[2 * i]); oa[2 * i + 1] = pkfma(f32x2{pj, pj}, f32x2{v[2], v[3]}, oa[2 * i + 1]); }
        SEND;
      SEND;
#pragma unroll
      for (int i = 0; i < 10; ++i) oa[i] = pkmul(oa[i], f32x2{inv, inv});
#pragma unroll
      for (int i = 0; i < 10; ++i) { o[2 * i] = oa[i][0]; o[2 * i + 1] = oa[i][1]; }
    } else {
      RELANE();
      asm volatile("" : "+v"(vImg), "+v"(vA0), "+v"(vA2), "+v"(vB), "+v"(vS0), "+v"(vS1), "+v"(vK0), "+v"(vK1), "+v"(vF0), "+v"(vF1), "+v"(vKv), "+v"(vKp));
      const float sl = rstd * LOG2E;
      {
        const unsigned rb = vImg;
        float kk[20];
#pragma unroll
        for (int i = 0; i < 16; ++i) kk[i] = a1[i];
#pragma unroll
        for (int i = 0; i < 4; ++i) kk[16 + i] = a3[i];
        unsigned ph[20];
        phi_map<true>(kk, sl, rstd, ph, [&](auto i_) __attribute__((always_inline)) { constexpr int i = decltype(i_)::v; SB();
          if constexpr (i < ADM) dmaw<NDM2 + i>(rw, l16, wb1k);
          if (i == 0) sts128(L, rb ^ (5 << 4), v4u{pk2(a2[0], a2[1]), pk2(a2[2], a2[3]), pk2(a2[4], a2[5]), pk2(a2[6], a2[7])});
          if (i == 1) sts128(L, rb ^ (6 << 4), v4u{pk2(a2[8], a2[9]), pk2(a2[10], a2[11]), pk2(a2[12], a2[13]), pk2(a2[14], a2[15])});
          if (i == 2) sts128(L, rb ^ (7 << 4), v4u{pk2(a3[4], a3[5]), pk2(a3[6], a3[7]), pk2(sd, 0.f), 0u});
          if (i == 5) sts128(L, rb ^ (0 << 4), v4u{ph[0], ph[1], ph[2], ph[3]});
          if (i == 6) sts128(L, rb ^ (1 << 4), v4u{ph[4], ph[5], ph[6], ph[7]});
          SB(); });
#pragma unroll
        for (int c = 2; c < 5; ++c) sts128(L, rb ^ (c << 4), v4u{ph[4 * c], ph[4 * c + 1], ph[4 * c + 2], ph[4 * c + 3]});
      }
      {
        const unsigned aA0 = vA0, aA2 = vA2, aB = vB;
        asm volatile("" : "+v"(vA0x), "+v"(vA2x), "+v"(vBx), "+v"(vK2), "+v"(vK3), "+v"(vSel));
        const unsigned aA0x = vA0x, aA2x = vA2x, aBx = vBx;
        BAR();
        f32x4 d0, d1, d2;
        v2u t[4][8];
        auto trd = [&](auto k_) __attribute__((always_inline)) {
          constexpr int ks = decltype(k_)::v; constexpr unsigned o_ = ks * 4096;
          t[ks][0] = ldstr(L, aB + o_); t[ks][1] = ldstr(L, aBx + o_); t[ks][6] = ldstr(L, aA2 + o_); t[ks][7] = ldstr(L, aA2x + o_);
          t[ks][2] = ldstr(L, aA0 + o_); t[ks][3] = ldstr(L, aA0x + o_); t[ks][4] = ldstr(L, aA0 + 8 + o_); t[ks][5] = ldstr(L, aA0x + 8 + o_);
          SB();
        };
        trd(IC<0>{});
        SFOR(ks, 4)
          if constexpr (ks < 3) trd(IC<ks + 1>{});
          const v4u bb = {t[ks][0].x, t[ks][0].y, t[ks][1].x, t[ks][1].y};
          mf16v<ks == 0>(d2, v4u{t[ks][6].x, t[ks][6].y, t[ks][7].x, t[ks][7].y}, bb);
          mf16v<ks == 0>(d0, v4u{t[ks][2].x, t[ks][2].y, t[ks][3].x, t[ks][3].y}, bb);
          mf16v<ks == 0>(d1, v4u{t[ks][4].x, t[ks][4].y, t[ks][5].x, t[ks][5].y}, bb);
          SB();
        SEND;
        asm volatile("s_nop 7");
        SB();
        const unsigned e0 = pk2(d2[0], d2[1]), e1 = pk2(d2[2], d2[3]);
        auto s0 = __builtin_amdgcn_permlane16_swap(e0, e0, false, false);
        auto s1 = __builtin_amdgcn_permlane16_swap(e1, e1, false, false);
        const unsigned ks_ = WS_KV + (((l >> 1) * 16 + cg) * 16 + rg) * 4096;
        stg_sc1(rs, vS0, ks_, v4u{pk2(d0[0], d0[1]), pk2(d0[2], d0[3]), pk2(d1[0], d1[1]), pk2(d1[2], d1[3])});
        stg_sc1(rs, vS1, ks_, v4u{e0, e1, s0[1], s1[1]});
      }
      STAMP(4 + 12 * l);
      if (l == 1) {
    v4u PV;
    asm volatile("v_mov_b32 %0, -1\n\tv_mov_b32 %1, -1\n\tv_mov_b32 %2, -1\n\tv_mov_b32 %3, -1" : "=v"(PV.x), "=v"(PV.y), "=v"(PV.z), "=v"(PV.w));
#pragma unroll
        for (int u = 0; u < 2; ++u) {
          stg_sc1(rs, vOv, sOp + u * XB, PV); stg_sc1(rs, vOv + 1024, sOp + u * XB, PV); stg_sc1(rs, vO3, sOp + u * XB + 4096, PV);
          stg_sc1(rs, vXv, cXp + u * XB, PV); stg_sc1(rs, vXv + 512, cXp + u * XB, PV); stg_sc1(rs, vX3, cXp + u * XB + 4096, PV);
          __builtin_amdgcn_raw_buffer_store_b64(v2u{PV.x, PV.y}, rs, vSp, cSp + u * STB, SC1);
        }
      }
      const unsigned ks_ = WS_KV + ((l >> 1) * 16 + cg) * 65536 + (w < 3 ? w * 1024 : 11 * 256);
      const unsigned kvv = vKv;
      unsigned kpr, kso[4];
#pragma unroll
      for (int i = 0; i < 4; ++i) kso[i] = ks_ + i * 16384;
#define LDKP(r) lda<4 * (r), ((r) & 3) * 256, true>(rs, kvv, kso[(r) >> 2])
      const unsigned kps = WS_KV + ((l >> 1) * 16 + cg) * 65536, kpv = vKp;
      SFOR(p_, 18) constexpr int pp = p_ + 2; SB(); P2PAIR(pp, (2 * pp + 6 <= 41 ? 4 : 39 - 2 * pp));
      SEND;
      SB();
      lgkm<0>();
      mf32v<false>(a0, wq[40 % 6], mfr);
      MFMA_SETTLE();
      FENCE(); SB();
      STAMP(5 + 12 * l);
      unsigned ph[20];
      float eps;
      { float qq[20];
#pragma unroll
        for (int i = 0; i < 4; ++i) qq[16 + i] = a3[8 + i];
#pragma unroll
        for (int i = 0; i < 16; ++i) qq[i] = a0[i];
        eps = 1e-6f * phi_map<false>(qq, sl, 1.f, ph, [&](auto i_) __attribute__((always_inline)) {
          if constexpr (decltype(i_)::v == KQS) { SB(); probe_issue(kpr, rs, kpv, kps); SFOR(r, 16) LDKP(r); SEND; SB(); } });
      }
      v4u f[10];
#pragma unroll
      for (int i = 0; i < 20; i += 4) asm volatile("" : "+v"(ph[i]), "+v"(ph[i + 1]), "+v"(ph[i + 2]), "+v"(ph[i + 3]));
      SB();
      probe_wait<16>(kpr);
      if (__builtin_expect(any_sent(kpr), 0)) { CNT(3); poll_probe<100>(rs, kpv, kps, tm); SFOR(r, 16) LDKP(r); SEND; }
      f32x4 sm[4];
      auto ksum = [&]() __attribute__((always_inline)) {
        SFOR(r, 16) if constexpr ((r & 3) == 0) vmc<12 - r>(); mf16vz<(r < 4), 4 * r>(sm[r & 3], vSel); SEND;
        asm volatile("s_nop 7");
        SB();
        return any_nan(((sm[0][0] + sm[1][0]) + sm[2][0]) + sm[3][0]);
      };
      if (__builtin_expect(ksum(), 0)) {
        unsigned sp = 0;
        do { CNT(1); SFOR(r, 16) LDKP(r); SEND; } while (ksum() && !give_up(sp, tm));
      }
      {
        v2u t2[4];
#pragma unroll
        for (int j = 0; j < 4; ++j) t2[j] = v2u{pk2(sm[j][0], sm[j][1]), pk2(sm[j][2], sm[j][3])};
        asm volatile("ds_write_b64 %0, %4\n\tds_write_b64 %1, %5\n\tds_write_b64 %2, %6\n\tds_write_b64 %3, %7" :: "v"(vK0), "v"(vK2), "v"(vK1), "v"(vK3), "v"(t2[0]), "v"(t2[1]), "v"(t2[2]), "v"(t2[3]) : "memory");
      }
      SFOR(i, (ADM > 10 ? ADM - 10 : 0)) DM(10 + i); SEND;
      BAR();
#pragma unroll
      for (int s = 0; s < 5; ++s) { f[2 * s + 1] = lds128(L, vF1 + s * 400); f[2 * s] = lds128(L, vF0 + s * 400); }
      f32x16 n0, n1;
      SFOR(s, 5)
        const v4u bb = {ph[4 * s], ph[4 * s + 1], ph[4 * s + 2], ph[4 * s + 3]};
        mf32v<s == 0>(n1, f[2 * s + 1], bb); mf32v<s == 0>(n0, f[2 * s], bb);
      SEND;
      MFMA_SETTLE();
      SB();
      const float inv = __builtin_amdgcn_rcpf(n1[4] + eps);
#pragma unroll
      for (int i = 0; i < 2; ++i) { const f32x2 t = pkmul(f32x2{n1[2 * i], n1[2 * i + 1]}, f32x2{inv, inv}); o[16 + 2 * i] = t[0]; o[17 + 2 * i] = t[1]; }
#pragma unroll
      for (int i = 0; i < 8; ++i) { const f32x2 t = pkmul(f32x2{n0[2 * i], n0[2 * i + 1]}, f32x2{inv, inv}); o[2 * i] = t[0]; o[2 * i + 1] = t[1]; }
    }
    {
      RELANE();
      asm volatile("" : "+v"(vOv), "+v"(vOp), "+v"(vXv), "+v"(vO3), "+v"(vX3), "+v"(vSp));
      const unsigned os = cOp;
      stg_sc1(rs, vOv, os, v4u{pk2(o[0], o[1]), pk2(o[2], o[3]), pk2(o[4], o[5]), pk2(o[6], o[7])});
      stg_sc1(rs, vOv + 1024, os, v4u{pk2(o[8], o[9]), pk2(o[10], o[11]), pk2(o[12], o[13]), pk2(o[14], o[15])});
      const unsigned e0 = pk2(o[16], o[17]), e1 = pk2(o[18], o[19]);
      auto s0 = __builtin_amdgcn_permlane32_swap(e0, e0, false, false);
      auto s1 = __builtin_amdgcn_permlane32_swap(e1, e1, false, false);
      stg_sc1(rs, vO3, os + 4096, v4u{e0, e1, s0[1], s1[1]});
      STAMP(6 + 12 * l);
      if (m == 0 && l == 0) {
        v4u PV;
        asm volatile("v_mov_b32 %0, -1\n\tv_mov_b32 %1, -1\n\tv_mov_b32 %2, -1\n\tv_mov_b32 %3, -1" : "=v"(PV.x), "=v"(PV.y), "=v"(PV.z), "=v"(PV.w));
        stg_sc1(rs, vOv, sOp, PV); stg_sc1(rs, vOv + 1024, sOp, PV); stg_sc1(rs, vO3, sOp + 4096, PV);
        stg_sc1(rs, l16, WS_KV + (256 + cg * 16 + rg) * 4096 + w * 1024, PV);
        stg_sc1(rs, l16, WS_HALO + (256 + rg * 16 + cg) * 4096 + w * 1024, PV);
        code_ahead<100, 0>(l16, (b >> 3) * 4 + w);
        __builtin_amdgcn_s_sleep(DO1 - 4);
      } else __builtin_amdgcn_s_sleep(DO1);
      wos = cWo;
      SFOR(i, 17 - P2W) LDWO(7 + P2W + i); SEND;
      asm volatile("buffer_load_dwordx4 %0, %1, %2, %3 offen sc1" : "=v"(wo24) : "v"(l16), "s"(rs), "s"(wos) : "memory");
      SFOR(i, ODM) DM(NDM2 + ADM + i); SEND;
      __builtin_amdgcn_s_sleep(m ? DO_3 : DO_2);
      if (!ll) __builtin_amdgcn_s_sleep(m ? DO_1 - DO_3 : DO_0 - DO_2);
      STAMP(7 + 12 * l);
      const unsigned l16b = l16 + 1024;
#define LDO(i, s) do { if constexpr ((s) == 0) lda<20 * (i), 0, true>(rs, l16, osb[i]); else lda<20 * (i) + 4 * (s), ((s) - 1) * 1024, true>(rs, l16b, osb[i]); } while (0)
      unsigned opr;
      const unsigned opv = vOp;
      probe_issue(opr, rs, opv, ops);
      SFOR(s, 5) LDO(0, s); if constexpr (s == 1) BARE(); SEND;
      BARE();
      SFOR(s, 5) LDO(1, s); SEND;
      SB();
      unsigned rwa[3], rra = L_RED + w * 15360 + l16;
      f32x2 rsum[10];
#pragma unroll
      for (int i = 0; i < 3; ++i) rwa[i] = L_RED + l16 + (((w + 1 + i) & 3) * 3 + i) * 5120;
#pragma unroll
      for (int i = 0; i < 10; ++i) rsum[i] = f32x2{lo16(xr[i]), hi16(xr[i])};
      asm volatile("" : "+v"(rsum[0]), "+v"(rsum[1]), "+v"(rsum[2]), "+v"(rsum[3]), "+v"(rsum[4]), "+v"(rsum[5]), "+v"(rsum[6]), "+v"(rsum[7]), "+v"(rsum[8]), "+v"(rsum[9]));
      asm volatile("" : "+v"(rwa[0]), "+v"(rwa[1]), "+v"(rwa[2]), "+v"(rra));
      f32x4 acc[2][5], part[15];
      probe_wait<10>(opr);
      asm volatile("" : "+v"(wo24));
      if (__builtin_expect(any_sent(opr), 0)) {
        CNT(4);
        poll_probe<100>(rs, opv, ops, tm);
        SFOR(s, 5) LDO(0, s); SEND;
        SFOR(s, 5) LDO(1, s); SEND;
      }
      auto redo = [&](auto i_) __attribute__((always_inline)) {
        constexpr int i = decltype(i_)::v;
        unsigned sp = 0;
        do {
          CNT(1);
          SFOR(s, 5) LDO(i, s); SEND;
          vmc<0>();
          SFOR(j, 25) if constexpr (j < 24) mf16aa<(j < 5), 160 + 4 * j, 20 * i + 4 * (j / 5)>(acc[i & 1][j % 5]); else mf16va<20 * i + 16>(acc[i & 1][4], wo24); SEND;
          asm volatile("s_nop 7");
          SB();
        } while (any_nan(acc[i & 1][4][0]) && !give_up(sp, tm));
      };
      constexpr int NDO = NDM2 + ADM + ODM;
      static_assert(NDO + GDM <= 22 && NDM2 + ADM + ODM <= 17 && GDM <= 10 && ADM <= 14, "");
      SFOR(i, 4)
        SFOR(j, 25)
          constexpr int s = j / 5, ft = j % 5, WS0 = 4, wstep = 4;
          SB();
          constexpr int gi = 5 * i + s;
          if constexpr (ft == 0) vmc<(gi < 10 ? 9 : 19 - gi) + (gi > 20 - GDM ? gi - (20 - GDM) : 0)>();
          if constexpr (j < 24) mf16aa<s == 0, 160 + 4 * j, 20 * i + 4 * s>(acc[i & 1][ft]); else mf16va<20 * i + 16>(acc[i & 1][ft], wo24);
          if constexpr (i == 0 && j == 0) STAMP(8 + 12 * l);
          if constexpr (ft == 2) {
            if constexpr (i < 2) LDO(i + 2, s);
            else if constexpr (gi >= 20 - GDM) dmaw<NDO + gi - (20 - GDM)>(rw, l16, wb1k);
          }
          if constexpr (i > 0 && j == WS0 - 1) { asm volatile("s_nop 1"); SB(); if (__builtin_expect(any_nan(acc[(i - 1) & 1][4][0]), 0)) redo(IC<i - 1>{}); }
          if constexpr (i > 0 && j >= WS0 && j <= WS0 + 4 * wstep && (j - WS0) % wstep == 0)
            DSW(rwa[i > 0 ? i - 1 : 0], ((j - WS0) / wstep) * 1024, acc[(i - 1) & 1][(j - WS0) / wstep]);
          if constexpr (i == 3 && j == 23) { SB(); BAR(); }
          if constexpr (i == 3 && j >= 23) {
#pragma unroll
            for (int q = (j - 23) * 8; q < (j - 23) * 8 + 8 && q < 15; ++q) part[q] = __builtin_bit_cast(f32x4, lds128(L, rra + (q % 3) * 5120 + (q / 3) * 1024));
          }
        SEND;
      SEND;
      SB();
      STAMP(9 + 12 * l);
#pragma unroll
      for (int ft = 0; ft < 5; ++ft)
#pragma unroll
        for (int q = 0; q < 3; ++q) { rsum[2 * ft] = pkadd(rsum[2 * ft], f32x2{part[ft * 3 + q][0], part[ft * 3 + q][1]}); rsum[2 * ft + 1] = pkadd(rsum[2 * ft + 1], f32x2{part[ft * 3 + q][2], part[ft * 3 + q][3]}); }
      asm volatile("" : "+v"(rsum[8]), "+v"(rsum[9]));
      SB();
      if (__builtin_expect(any_nan(acc[1][4][0]), 0)) redo(IC<3>{});
      auto fin = [&](int ft) __attribute__((always_inline)) {
        const f32x2 lo = pkadd(rsum[2 * ft], f32x2{acc[1][ft][0], acc[1][ft][1]}), hi = pkadd(rsum[2 * ft + 1], f32x2{acc[1][ft][2], acc[1][ft][3]});
        xr[2 * ft] = pk2(lo[0], lo[1]); xr[2 * ft + 1] = pk2(hi[0], hi[1]);
      };
      fin(0); fin(1);
      if (l < 3) {
        stg_sc1(rs, vXv, cXp, v4u{xr[0], xr[1], xr[2], xr[3]});
        SB();
        fin(2); fin(3);
        stg_sc1(rs, vXv + 512, cXp, v4u{xr[4], xr[5], xr[6], xr[7]});
        SB();
        fin(4);
        { auto s0 = __builtin_amdgcn_permlane16_swap(xr[8], xr[8], false, false);
          auto s1 = __builtin_amdgcn_permlane16_swap(xr[9], xr[9], false, false);
          stg_sc1(rs, vX3, cXp + 4096, v4u{xr[8], xr[9], s0[1], s1[1]}); }
        SB();
        { float sa = 0.f, sq = 0.f;
#pragma unroll
          for (int i = 0; i < 10; ++i) { sa = dot2(xr[i], 0x3F803F80u, sa); sq = dot2(xr[i], xr[i], sq); }
          { auto a = __builtin_amdgcn_permlane16_swap(f2u(sa), f2u(sa), false, false); sa = u2f(a[0]) + u2f(a[1]);
            auto c = __builtin_amdgcn_permlane16_swap(f2u(sq), f2u(sq), false, false); sq = u2f(c[0]) + u2f(c[1]); }
          { auto a = __builtin_amdgcn_permlane32_swap(f2u(sa), f2u(sa), false, false); sa = u2f(a[0]) + u2f(a[1]);
            auto c = __builtin_amdgcn_permlane32_swap(f2u(sq), f2u(sq), false, false); sq = u2f(c[0]) + u2f(c[1]); }
          __builtin_amdgcn_raw_buffer_store_b64(v2u{f2u(sa), f2u(sq)}, rs, vSp, cSp, SC1); }
        STAMP(10 + 12 * l);
        if (m == 0 && l == 0 && w < 2) {
          const unsigned cv = tid * 4, cv2 = cv + 4096;
          SFOR(r, 16) lda1<210 + r, (r & 7) * 512>(rs, r < 8 ? cv : cv2, WS_CSP + (16 + cg) * 8192); SEND;
        }
        if (m == 0 && l == 0) code_ahead<230, 2048>(l16, (b >> 3) * 4 + w);
        BARE();
        __builtin_amdgcn_s_sleep(DX1);
        SFOR(i, 22 - NDO - GDM + LW) DM(NDO + GDM + i); SEND;
        __builtin_amdgcn_s_sleep(m ? DX_1 : DX_2);
        if (!m && !ll) __builtin_amdgcn_s_sleep(DX_0 - DX_2);
      } else {
        fin(2); fin(3); fin(4);
        GAS v2u* dst = (GAS v2u*)(p.out + (size_t)(rg * 128 + half * 64 + w * 16 + (lane & 15)) * 640 + c8 * 80 + (lane >> 4) * 20);
#pragma unroll
        for (int i = 0; i < 5; ++i) dst[i] = v2u{xr[2 * i], xr[2 * i + 1]};
        if (b == 0 && w == 0) __builtin_amdgcn_raw_buffer_store_b32(tm.epoch, rs, lane ? 0x80000000u : 0u, WS_CTRL + 128, SC1);
      }
      STAMP(11 + 12 * l);
    }
  };
#pragma nounroll
  for (int ll = 0; ll < 2; ++ll) { layer(IC<0>{}, ll); layer(IC<1>{}, ll); }
}

extern "C" void kernel_launch(void* const* d_in, const int* in_sizes, int n_in, void* d_out, int out_size, void* d_ws, size_t ws_size, hipStream_t stream) {
  Params p;
  p.x = (const u16*)d_in[0]; p.wqA = (const u16*)d_in[1]; p.woA = (const u16*)d_in[2];
  p.wqB = (const u16*)d_in[4]; p.wphi = (const u16*)d_in[5]; p.woB = (const u16*)d_in[6];
  p.out = (u16*)d_out; p.ws = (unsigned char*)d_ws;
  (void)hipFuncSetAttribute((const void*)k_main, hipFuncAttributeMaxDynamicSharedMemorySize, LDS_ALLOC);
  k_main<<<256, 256, LDS_ALLOC, stream>>>(p);
}
```

```cpp
#include <hip/hip_runtime.h>
#include <stdint.h>

typedef unsigned short u16;
typedef unsigned v4u __attribute__((ext_vector_type(4)));
typedef unsigned v2u __attribute__((ext_vector_type(2)));
typedef float f32x2 __attribute__((ext_vector_type(2)));
typedef float f32x4 __attribute__((ext_vector_type(4)));
typedef float f32x16 __attribute__((ext_vector_type(16)));
typedef __bf16 bf16x8 __attribute__((ext_vector_type(8)));
typedef __bf16 bf16x2 __attribute__((ext_vector_type(2)));
typedef short s16x4 __attribute__((ext_vector_type(4)));
#define LAS __attribute__((address_space(3)))
#define GAS __attribute__((address_space(1)))
typedef __amdgpu_buffer_rsrc_t rsrc_t;
#define DEV __device__ __forceinline__

#define DSC1 " sc1"
constexpr unsigned WQ_SZ = 153600, WO_SZ = 102400, XB = 2621440, STB = 131072;
constexpr unsigned WS_CTRL = 0;
constexpr unsigned WS_FLAG = 4096;
constexpr unsigned WS_WQ = WS_FLAG + 4 * 256 * 128;
constexpr unsigned WS_WO = WS_WQ + 32 * WQ_SZ;
constexpr unsigned WS_CSP = WS_WO + 16 * WO_SZ;
constexpr unsigned WS_ST0 = WS_CSP + 2 * 16 * 16 * 512;
constexpr unsigned WS_ST1 = WS_ST0 + STB;
constexpr unsigned WS_X = WS_ST0 + 4 * STB;
constexpr unsigned WS_O = WS_X + 4 * XB;
constexpr unsigned WS_KV = WS_O + 4 * XB;
constexpr unsigned WS_HALO = WS_KV + 2 * 256 * 4096;
constexpr unsigned WS_END = WS_HALO + 2 * 256 * 4096;
constexpr unsigned WS_STAMP = 40u << 20;
constexpr unsigned WS_TOTAL = WS_STAMP + 256 * 4 * 64 * 8;
constexpr unsigned KSTEP = 2816, L_P0 = 112640, L_KVA = 153600, L_CS = L_KVA + 4096;
constexpr unsigned L_SCR = 73216;
constexpr unsigned L_K = L_SCR, L_V = L_SCR + 2 * 144 * 48;
constexpr unsigned L_RED = 90112;
constexpr unsigned LDS_BYTES = L_CS + 4096;
constexpr unsigned SENT = 0xFFFFFFFFu;
constexpr unsigned CODE_BYTES = 24576;
constexpr int SC1 = 16;
constexpr float LOG2E = 1.4426950408889634f;

struct Params { const u16 *x, *wqA, *woA, *wqB, *wphi, *woB; u16* out; unsigned char* ws; };

DEV unsigned pk2(float a, float b) { bf16x2 v = {(__bf16)a, (__bf16)b}; return __builtin_bit_cast(unsigned, v); }
DEV float lo16(unsigned u) { return __builtin_bit_cast(float, u << 16); }
DEV float hi16(unsigned u) { return __builtin_bit_cast(float, u & 0xffff0000u); }
DEV unsigned f2u(float f) { return __builtin_bit_cast(unsigned, f); }
DEV float u2f(unsigned u) { return __builtin_bit_cast(float, u); }
DEV unsigned umax(unsigned a, unsigned b) { return __builtin_elementwise_max(a, b); }
DEV unsigned umin(unsigned a, unsigned b) { return __builtin_elementwise_min(a, b); }
DEV float max3f(float a, float b, float c) { float d; asm("v_max3_f32 %0, %1, %2, %3" : "=v"(d) : "v"(a), "v"(b), "v"(c)); return d; }
DEV float min3f(float a, float b, float c) { float d; asm("v_min3_f32 %0, %1, %2, %3" : "=v"(d) : "v"(a), "v"(b), "v"(c)); return d; }
DEV float dot2(unsigned a, unsigned b, float c) { return __builtin_amdgcn_fdot2_f32_bf16(__builtin_bit_cast(bf16x2, a), __builtin_bit_cast(bf16x2, b), c, false); }
DEV f32x2 pkfma(f32x2 a, f32x2 b, f32x2 c) { return __builtin_elementwise_fma(a, b, c); }
DEV f32x2 pkmul(f32x2 a, f32x2 b) { return a * b; }
DEV f32x2 pkadd(f32x2 a, f32x2 b) { return a + b; }
DEV f32x16 mfma32(v4u a, v4u b, f32x16 c) { return __builtin_amdgcn_mfma_f32_32x32x16_bf16(__builtin_bit_cast(bf16x8, a), __builtin_bit_cast(bf16x8, b), c, 0, 0, 0); }
DEV f32x4 mfma16(v4u a, v4u b, f32x4 c) { return __builtin_amdgcn_mfma_f32_16x16x32_bf16(__builtin_bit_cast(bf16x8, a), __builtin_bit_cast(bf16x8, b), c, 0, 0, 0); }
DEV v4u lds128(LAS unsigned char* L, unsigned a) { return *(LAS v4u*)(L + a); }
DEV v2u lds64(LAS unsigned char* L, unsigned a) { return *(LAS v2u*)(L + a); }
DEV void sts128(LAS unsigned char* L, unsigned a, v4u v) { *(LAS v4u*)(L + a) = v; }
DEV v2u ldstr(LAS unsigned char* L, unsigned a) { return __builtin_bit_cast(v2u, __builtin_amdgcn_ds_read_tr16_b64_v4i16((LAS s16x4*)(L + a))); }
DEV v4u ldg(rsrc_t rs, unsigned voff, unsigned soff) { return __builtin_amdgcn_raw_buffer_load_b128(rs, voff, soff, 0); }
DEV v4u ldg_sc1(rsrc_t rs, unsigned voff, unsigned soff) { return __builtin_amdgcn_raw_buffer_load_b128(rs, voff, soff, SC1); }
DEV v2u ldg64_sc1(rsrc_t rs, unsigned voff, unsigned soff) { return __builtin_amdgcn_raw_buffer_load_b64(rs, voff, soff, SC1); }
DEV void stg_sc1(rsrc_t rs, unsigned voff, unsigned soff, v4u v) { __builtin_amdgcn_raw_buffer_store_b128(v, rs, voff, soff, SC1); }
DEV unsigned mx4(unsigned m, v4u d) { return umax(umax(umax(m, d.x), d.y), umax(d.z, d.w)); }
DEV unsigned max3u(unsigned a, unsigned b, unsigned c) { return umax(umax(a, b), c); }
DEV bool any_sent(unsigned m) { return __builtin_amdgcn_ballot_w64(m == SENT) != 0; }
#define BAR() asm volatile("s_waitcnt lgkmcnt(0)\n\ts_barrier" ::: "memory")
#define DRAIN() asm volatile("s_waitcnt vmcnt(0)" ::: "memory")
#define FENCE() asm volatile("" ::: "memory")
#define SB() __builtin_amdgcn_sched_barrier(0)
DEV void dma4(rsrc_t rs, unsigned voff, unsigned soff, unsigned ldsa) {
  asm volatile("s_mov_b32 m0, %0\n\ts_nop 0\n\tbuffer_load_dwordx4 %1, %2, %3 offen sc1 lds\n\tbuffer_load_dwordx4 %1, %2, %3 offen offset:1024 sc1 lds\n\t"
               "buffer_load_dwordx4 %1, %2, %3 offen offset:2048 sc1 lds\n\tbuffer_load_dwordx4 %1, %2, %3 offen offset:3072 sc1 lds"
               :: "s"(__builtin_amdgcn_readfirstlane(ldsa)), "v"(voff), "s"(rs), "s"(__builtin_amdgcn_readfirstlane(soff)) : "memory");
}
DEV void dma1(rsrc_t rs, unsigned voff, unsigned soff, unsigned ldsa) {
  asm volatile("s_mov_b32 m0, %0\n\ts_nop 0\n\tbuffer_load_dwordx4 %1, %2, %3 offen sc1 lds"
               :: "s"(__builtin_amdgcn_readfirstlane(ldsa)), "v"(voff), "s"(rs), "s"(__builtin_amdgcn_readfirstlane(soff)) : "memory");
}
DEV void dma_range(rsrc_t rs, unsigned l16, unsigned wsrc, int w, int k0, int k1) {
  int k = k0 + 4 * w;
  for (; k + 4 <= k1; k += 16) dma4(rs, l16, wsrc + k * 1024, k * 1024);
  for (; k < k1; ++k) dma1(rs, l16, wsrc + k * 1024, k * 1024);
}
struct Tmo { unsigned char* ws; unsigned epoch; };
DEV bool give_up(unsigned& sp, const Tmo& tm) {
  ++sp;
  if (sp > 32) __builtin_amdgcn_s_sleep(8);
  if ((sp & 63u) == 0u) {
    GAS unsigned* t = (GAS unsigned*)(tm.ws + WS_CTRL);
    if (sp >= (1u << 16)) { __hip_atomic_store(t, tm.epoch, __ATOMIC_RELAXED, __HIP_MEMORY_SCOPE_AGENT); return true; }
    if (__builtin_amdgcn_readfirstlane(__hip_atomic_load(t, __ATOMIC_RELAXED, __HIP_MEMORY_SCOPE_AGENT)) == tm.epoch) return true;
  }
  return false;
}
#define MAPV 1
#define XW 10
DEV int map_rg(int b) { return ((b & 7) >> 1) * 4 + (b >> 6); }
DEV int map_cg(int b) { return (b & 1) * 8 + ((b >> 3) & 7); }
DEV int o_half(int cg) { return (cg >> 2) & 1; }
DEV int o_c8(int cg) { return (cg >> 3) * 4 + (cg & 3); }
DEV int xfeat(int c, int j) { int c8 = c / 10, l = c % 10; return 80 * c8 + (l < 8 ? 20 * (l >> 1) + 8 * (l & 1) + j : 40 * (l - 8) + 20 * (j >> 2) + 16 + (j & 3)); }
DEV int ocol(int cgp, int s, int j) { return 40 * cgp + (s < 4 ? 20 * (s >> 1) + 8 * (s & 1) + j : 20 * (j >> 2) + 16 + (j & 3)); }
DEV void pin_s(unsigned& v) { asm volatile("" : "+s"(v)); }
DEV int fresh_lane() { int l; asm volatile("v_mbcnt_lo_u32_b32 %0, -1, 0\n\tv_mbcnt_hi_u32_b32 %0, -1, %0" : "=v"(l)); return l; }
#define RELANE() const int lane = fresh_lane(), h = lane >> 5, tok = lane & 31, tid = w * 64 + lane; const unsigned l16 = lane * 16; (void)h; (void)tok; (void)tid; (void)l16
#define STAMP(i) do {} while (0)
#define LDS_ALLOC LDS_BYTES
#define CNT(i) do {} while (0)

DEV void publish_x(rsrc_t rs, unsigned xbuf, unsigned stbuf, int rg, int cg, int w, int lane, const unsigned (&xr)[10]) {
  const int g = lane >> 4, tok = lane & 15, half = o_half(cg), c8 = o_c8(cg);
  const unsigned sb = xbuf + ((rg * 4 + half * 2 + (w >> 1)) * 40 + 5 * c8) * 1024;
  const unsigned vb = ((w & 1) * 16 + tok) * 16;
  stg_sc1(rs, vb + g * 1024, sb, v4u{xr[0], xr[1], xr[2], xr[3]});
  stg_sc1(rs, vb + g * 1024 + 512, sb, v4u{xr[4], xr[5], xr[6], xr[7]});
  auto s0 = __builtin_amdgcn_permlane16_swap(xr[8], xr[8], false, false);
  auto s1 = __builtin_amdgcn_permlane16_swap(xr[9], xr[9], false, false);
  if (!(g & 1)) stg_sc1(rs, vb + 4096 + (g >> 1) * 512, sb, v4u{xr[8], xr[9], s0[1], s1[1]});
  SB();
  float sa = 0.f, sq = 0.f;
#pragma unroll
  for (int i = 0; i < 10; ++i) { sa = dot2(xr[i], 0x3F803F80u, sa); sq = dot2(xr[i], xr[i], sq); }
  { auto a = __builtin_amdgcn_permlane16_swap(f2u(sa), f2u(sa), false, false); sa = u2f(a[0]) + u2f(a[1]);
    auto b = __builtin_amdgcn_permlane16_swap(f2u(sq), f2u(sq), false, false); sq = u2f(b[0]) + u2f(b[1]); }
  { auto a = __builtin_amdgcn_permlane32_swap(f2u(sa), f2u(sa), false, false); sa = u2f(a[0]) + u2f(a[1]);
    auto b = __builtin_amdgcn_permlane32_swap(f2u(sq), f2u(sq), false, false); sq = u2f(b[0]) + u2f(b[1]); }
  if (g == 0) __builtin_amdgcn_raw_buffer_store_b64(v2u{f2u(sa), f2u(sq)}, rs, tok * 8, stbuf + ((rg * 8 + c8) * 128 + half * 64 + w * 16) * 8, SC1);
}
DEV void load_x_in(const u16* x, int rg, int cg, int w, int lane, unsigned (&xr)[10]) {
  const int g = lane >> 4, tok = lane & 15;
  const GAS v2u* src = (const GAS v2u*)(x + (size_t)(rg * 128 + o_half(cg) * 64 + w * 16 + tok) * 640 + o_c8(cg) * 80 + g * 20);
#pragma unroll
  for (int i = 0; i < 5; ++i) { v2u t = src[i]; xr[2 * i] = t.x; xr[2 * i + 1] = t.y; }
}

DEV void poison_x(rsrc_t rs, unsigned xbuf, unsigned stbuf, int rg, int cg, int w, int lane) {
  const int g = lane >> 4, tok = lane & 15, half = o_half(cg), c8 = o_c8(cg);
  const unsigned sb = xbuf + ((rg * 4 + half * 2 + (w >> 1)) * 40 + 5 * c8) * 1024, vb = ((w & 1) * 16 + tok) * 16;
  const v4u P = {SENT, SENT, SENT, SENT};
  stg_sc1(rs, vb + g * 1024, sb, P); stg_sc1(rs, vb + g * 1024 + 512, sb, P); stg_sc1(rs, vb + 4096 + (g >> 1) * 512, sb, P);
  __builtin_amdgcn_raw_buffer_store_b64(v2u{SENT, SENT}, rs, tok * 8, stbuf + ((rg * 8 + c8) * 128 + half * 64 + w * 16) * 8, SC1);
}
DEV void poison_o(rsrc_t rs, unsigned obuf, int rg, int cg, int w, int lane) {
  const int h = lane >> 5, tok = lane & 31;
  const unsigned os = obuf + ((rg * 8 + 2 * w) * 4 + (cg >> 2)) * 5120 + (cg & 3) * 256, ov = (tok >> 4) * 20480 + (tok & 15) * 16;
  const v4u P = {SENT, SENT, SENT, SENT};
  stg_sc1(rs, ov + h * 2048, os, P); stg_sc1(rs, ov + h * 2048 + 1024, os, P); stg_sc1(rs, ov + 4096, os, P);
}
DEV void wait_flags(rsrc_t rs, unsigned voff, unsigned soff, const Tmo& tm) {
  unsigned sp = 0, v;
  do { FENCE(); v = __builtin_amdgcn_raw_buffer_load_b32(rs, voff, soff, SC1); } while (__builtin_amdgcn_ballot_w64(v != tm.epoch) != 0 && !give_up(sp, tm));
}
DEV void build_wq_read(LAS unsigned char* L, unsigned stage, int w, int lane, v4u (&fr)[3]) {
  const int gam = lane >> 4, q = (lane >> 2) & 3, pp = lane & 3, a = 2 * (gam & 1) + (pp >> 1), h = pp & 1;
  const int eoff = (w < 3) ? 40 * w + 20 * h + 4 * a : (a == 0 ? 40 : a == 1 ? 80 : 0) + 20 * h + 16;
#pragma unroll
  for (int it = 0; it < 3; ++it) {
    const int kap = 2 * it + (gam >> 1), kc = kap < 4 ? kap : 4;
    const unsigned ad = stage + (kc * 8 + q) * 256 + eoff * 2;
    const v2u t0 = ldstr(L, ad), t1 = ldstr(L, ad + 1024);
    fr[it] = v4u{t0.x, t0.y, t1.x, t1.y};
  }
}
DEV float sum8(v4u v, float c) { c = dot2(v.x, 0x3F803F80u, c); c = dot2(v.y, 0x3F803F80u, c); c = dot2(v.z, 0x3F803F80u, c); return dot2(v.w, 0x3F803F80u, c); }
DEV void build_wq_store(rsrc_t rs, int m, int rg, int cg, int w, int lane, int r0, int r1, const v4u (&fr)[3]) {
  const int row = lane & 31, hi = lane >> 5;
  const unsigned tb = w == 0 ? L_P0 : (w - 1) * 1024, ts = w == 0 ? 1024 : KSTEP, th = w == 3 ? 384 : 512;
  const bool in = row >= r0 && row < r1;
#pragma unroll
  for (int it = 0; it < 3; ++it) {
    const int c = 5 * rg + 2 * it + hi;
    stg_sc1(rs, (in && (it < 2 || !hi)) ? (c >> 1) * ts + (c & 1) * th + row * 16 : 0x80000000u, WS_WQ + (m * 16 + cg) * WQ_SZ + tb, fr[it]);
  }
  float cs = sum8(fr[1], sum8(fr[0], 0.f));
  cs = hi ? cs : sum8(fr[2], cs);
  auto t = __builtin_amdgcn_permlane32_swap(f2u(cs), f2u(cs), false, false);
  __builtin_amdgcn_raw_buffer_store_b32(f2u(u2f(t[0]) + u2f(t[1])), rs, (in && !hi) ? row * 4 : 0x80000000u, WS_CSP + ((m * 16 + cg) * 16 + rg) * 512 + w * 128, SC1);
}
DEV void probe_issue(unsigned& d, rsrc_t rs, unsigned voff, unsigned soff) {
  asm volatile("buffer_load_dword %0, %1, %2, %3 offen sc1" : "=v"(d) : "v"(voff), "s"(rs), "s"(soff) : "memory");
}
template <int N> DEV void probe_wait(unsigned& d) { asm volatile("s_waitcnt vmcnt(%1)" : "+v"(d) : "i"(N) : "memory"); }
#define PSL 2
template <int R> DEV unsigned probe_turn(rsrc_t rs, unsigned voff, unsigned soff) {
  unsigned v;
  asm volatile("buffer_load_dword a%c4, %1, %2, %3 offen sc1\n\ts_waitcnt vmcnt(1)\n\tv_accvgpr_read_b32 %0, a%c5" : "=v"(v) : "v"(voff), "s"(rs), "s"(soff), "i"(R), "i"(R ^ 1) : "memory");
  return v;
}
template <int R> DEV void poll_flag(rsrc_t rs, unsigned voff, unsigned soff, const Tmo& tm) {
  unsigned sp = 0;
  asm volatile("buffer_load_dword a%c3, %0, %1, %2 offen sc1" :: "v"(voff), "s"(rs), "s"(soff), "i"(R) : "memory");
  for (;;) {
    __builtin_amdgcn_s_sleep(PSL);
    if (__builtin_amdgcn_ballot_w64(probe_turn<R + 1>(rs, voff, soff) != tm.epoch) == 0) break;
    __builtin_amdgcn_s_sleep(PSL);
    if (__builtin_amdgcn_ballot_w64(probe_turn<R>(rs, voff, soff) != tm.epoch) == 0) break;
    if (give_up(sp, tm)) break;
  }
}
template <int R> DEV void poll_probe(rsrc_t rs, unsigned voff, unsigned soff, const Tmo& tm) {
  unsigned sp = 0;
  asm volatile("buffer_load_dword a%c3, %0, %1, %2 offen sc1" :: "v"(voff), "s"(rs), "s"(soff), "i"(R) : "memory");
  for (;;) {
    __builtin_amdgcn_s_sleep(PSL);
    if (!any_sent(probe_turn<R + 1>(rs, voff, soff))) break;
    __builtin_amdgcn_s_sleep(PSL);
    if (!any_sent(probe_turn<R>(rs, voff, soff))) break;
    if (give_up(sp, tm)) break;
  }
}
template <int I> struct IC { static constexpr int v = I; };
template <int I, int N, class F> DEV void sfor(F&& f) { if constexpr (I < N) { f(IC<I>{}); sfor<I + 1, N>(f); } }
#define SFOR(var, n) sfor<0, n>([&](auto var##_) __attribute__((always_inline)) { constexpr int var = decltype(var##_)::v;
#define SEND })
template <bool NORM, class F> DEV float phi_map(const float (&a)[20], float s, float post, unsigned (&ph)[20], F slot) {
  float mx = max3f(a[0], a[1], a[2]), mn = min3f(a[0], a[1], a[2]), mx2 = max3f(a[3], a[4], a[5]), mn2 = min3f(a[3], a[4], a[5]);
#pragma unroll
  for (int i = 6; i < 18; i += 4) { mx = max3f(mx, a[i], a[i + 1]); mn = min3f(mn, a[i], a[i + 1]); mx2 = max3f(mx2, a[i + 2], a[i + 3]); mn2 = min3f(mn2, a[i + 2], a[i + 3]); }
  mx = max3f(mx, a[18], a[19]); mn = min3f(mn, a[18], a[19]); mx = max3f(mx, mx2, mx2); mn = min3f(mn, mn2, mn2);
  const float bp = -s * mx, bn = s * mn, ns = -s;
  float sp = 0.f;
  SFOR(sg, 2)
    const f32x2 sc = sg ? f32x2{ns, ns} : f32x2{s, s}, bi = sg ? f32x2{bn, bn} : f32x2{bp, bp};
    f32x2 e[10];
    slot(IC<5 * sg>{});
#pragma unroll
    for (int i = 0; i < 10; ++i) e[i] = pkfma(f32x2{a[2 * i], a[2 * i + 1]}, sc, bi);
    SB();
    SFOR(i, 10)
      if constexpr (i == 3 || i == 7) slot(IC<5 * sg + 1 + (i >> 2)>{});
      e[i] = f32x2{__builtin_amdgcn_exp2f(e[i][0]), __builtin_amdgcn_exp2f(e[i][1])};
    SEND;
    f32x2 s0 = pkadd(e[0], e[1]), s1 = pkadd(e[2], e[3]), s2 = pkadd(e[4], e[5]);
    s0 = pkadd(s0, e[6]); s1 = pkadd(s1, e[7]); s2 = pkadd(s2, e[8]); s0 = pkadd(s0, e[9]);
    slot(IC<5 * sg + 3>{});
    if constexpr (!NORM && sg == 0) {
#pragma unroll
      for (int i = 0; i < 10; ++i) ph[i] = pk2(e[i][0], e[i][1]);
    }
    s0 = pkadd(pkadd(s0, s1), s2);
    const float sm = s0[0] + s0[1];
    if constexpr (NORM || sg == 1) {
      const float r = __builtin_amdgcn_rcpf(sm) * (NORM ? post : sp);
#pragma unroll
      for (int i = 0; i < 10; ++i) e[i] = pkmul(e[i], f32x2{r, r});
      SB();
#pragma unroll
      for (int i = 0; i < 10; ++i) ph[10 * sg + i] = pk2(e[i][0], e[i][1]);
    }
    sp = sg ? sp : sm;
    slot(IC<5 * sg + 4>{});
  SEND;
  return sp;
}

#define BARE() asm volatile("s_barrier" ::: "memory")
template <int OFF> DEV void dsr(v4u& d, unsigned addr) { asm volatile("ds_read_b128 %0, %1 offset:%2" : "=v"(d) : "v"(addr), "i"(OFF)); }
template <int R, int OFF, bool COH> DEV void lda(rsrc_t rs, unsigned voff, unsigned soff) {
  if constexpr (COH) asm volatile("buffer_load_dwordx4 a[%c0:%c1], %2, %3, %4 offen offset:%5 sc1" :: "i"(R), "i"(R + 3), "v"(voff), "s"(rs), "s"(soff), "i"(OFF) : "memory");
  else asm volatile("buffer_load_dwordx4 a[%c0:%c1], %2, %3, %4 offen offset:%5" :: "i"(R), "i"(R + 3), "v"(voff), "s"(rs), "s"(soff), "i"(OFF) : "memory");
}
template <int R, int OFF> DEV void lda1(rsrc_t rs, unsigned voff, unsigned soff) {
  asm volatile("buffer_load_dword a%c0, %1, %2, %3 offen offset:%4 sc1" :: "i"(R), "v"(voff), "s"(rs), "s"(soff), "i"(OFF) : "memory");
}
template <int R> DEV float rda1() { float t; asm volatile("v_accvgpr_read_b32 %0, a%c1" : "=v"(t) : "i"(R)); return t; }
DEV void ldst(v2u (&st)[4], rsrc_t rs, unsigned voff, unsigned soff) {
  asm volatile("buffer_load_dwordx2 %0, %4, %5, %6 offen sc1\n\tbuffer_load_dwordx2 %1, %4, %5, %6 offen offset:1024 sc1\n\t"
               "buffer_load_dwordx2 %2, %4, %5, %6 offen offset:2048 sc1\n\tbuffer_load_dwordx2 %3, %4, %5, %6 offen offset:3072 sc1"
               : "=&v"(st[0]), "=&v"(st[1]), "=&v"(st[2]), "=&v"(st[3]) : "v"(voff), "s"(rs), "s"(soff) : "memory");
}
template <int N> DEV unsigned st_wait(v2u (&st)[4]) {
  asm volatile("s_waitcnt vmcnt(%4)" : "+v"(st[0]), "+v"(st[1]), "+v"(st[2]), "+v"(st[3]) : "i"(N) : "memory");
  return max3u(max3u(st[0].x, st[0].y, st[1].x), max3u(st[1].y, st[2].x, st[2].y), umax(st[3].x, st[3].y));
}
DEV void sbump(unsigned& so) { asm volatile("s_add_u32 %0, %0, 0x1000" : "+s"(so) :: "scc"); }
template <bool Z, int R> DEV void mf32a(f32x16& c, const v4u& a) {
  if constexpr (Z) asm volatile("v_mfma_f32_32x32x16_bf16 %0, %1, a[%c2:%c3], 0" : "=&v"(c) : "v"(a), "i"(R), "i"(R + 3));
  else asm volatile("v_mfma_f32_32x32x16_bf16 %0, %1, a[%c2:%c3], %0" : "+v"(c) : "v"(a), "i"(R), "i"(R + 3));
}
template <bool Z> DEV void mf32v(f32x16& c, const v4u& a, const v4u& b) {
  if constexpr (Z) asm volatile("v_mfma_f32_32x32x16_bf16 %0, %1, %2, 0" : "=&v"(c) : "v"(a), "v"(b));
  else asm volatile("v_mfma_f32_32x32x16_bf16 %0, %1, %2, %0" : "+v"(c) : "v"(a), "v"(b));
}
template <bool Z, int R> DEV void mf16a(f32x4& c, const v4u& b) {
  if constexpr (Z) asm volatile("v_mfma_f32_16x16x32_bf16 %0, a[%c2:%c3], %1, 0" : "=&v"(c) : "v"(b), "i"(R), "i"(R + 3));
  else asm volatile("v_mfma_f32_16x16x32_bf16 %0, a[%c2:%c3], %1, %0" : "+v"(c) : "v"(b), "i"(R), "i"(R + 3));
}
template <bool Z> DEV void mf16v(f32x4& c, const v4u& a, const v4u& b) {
  if constexpr (Z) asm volatile("v_mfma_f32_16x16x32_bf16 %0, %1, %2, 0" : "=&v"(c) : "v"(a), "v"(b));
  else asm volatile("v_mfma_f32_16x16x32_bf16 %0, %1, %2, %0" : "+v"(c) : "v"(a), "v"(b));
}
template <int N> DEV void vmc() { asm volatile("s_waitcnt vmcnt(%0)" :: "i"(N) : "memory"); }
template <int I> DEV void dmaw(rsrc_t rw, unsigned l16, unsigned wb) {
  asm volatile("s_or_b32 m0, %0, %1\n\ts_nop 0\n\tbuffer_load_dwordx4 %2, %3, m0 offen" DSC1 " lds" :: "s"(wb), "i"(I * 4096), "v"(l16), "s"(rw) : "memory", "scc");
}
template <int N> DEV void lgkm() { asm volatile("s_waitcnt lgkmcnt(%0)" :: "i"(N)); }
#define MFMA_SETTLE() asm volatile("s_nop 7\n\ts_nop 3")
DEV bool any_nan(float x) { return __builtin_amdgcn_ballot_w64(x != x) != 0; }
#define PFD 3

template <int S, int NW, int VM, int VI> DEV void p2pair(f32x16& a0, v4u& w0, v4u& w1, unsigned bq, unsigned bcs, rsrc_t r, unsigned l16, unsigned so, const v4u& pv) {
#define P2_M0 "s_waitcnt lgkmcnt(%c[nw])\n\tv_mfma_f32_32x32x16_bf16 %[c], %[w0], a[%c[r0]:%c[r1]], "
#define P2_R0 "\n\tds_read_b128 %[w0], %[a0] offset:%c[o0]\n\t"
#define P2_M1 "v_mfma_f32_32x32x16_bf16 %[c], %[w1], a[%c[r2]:%c[r3]], %[c]"
#define P2_R1 "\n\tds_read_b128 %[w1], %[bq] offset:%c[o1]"
#define P2_IN [nw] "i"(NW), [r0] "i"(4 * S), [r1] "i"(4 * S + 3), [r2] "i"(4 * S + 4), [r3] "i"(4 * S + 7), [a0] "v"(S + 6 == 40 ? bcs : bq), [bq] "v"(bq), \
              [o0] "i"(S + 6 < 40 ? (S + 6) * 1024 : 0), [o1] "i"((S + 7) * 1024), [rs] "s"(r), [l16] "v"(l16), [so] "s"(so), [d0] "i"(VM == 1 ? 160 + 4 * VI : 0), [d1] "i"(VM == 1 ? 163 + 4 * VI : 3), \
              [vo] "i"((VI & 3) * 1024), [dm] "i"(VM == 2 ? VI * 4096 : VM == 3 ? VI : 0), [pv] "v"(pv), [p2] "v"(v2u{pv.x, pv.y})
#define P2_LD "buffer_load_dwordx4 a[%c[d0]:%c[d1]], %[l16], %[rs], %[so] offen offset:%c[vo] sc1\n\t"
#define P2_DMA "s_or_b32 m0, %[so], %[dm]\n\t"
#define P2_DMB "buffer_load_dwordx4 %[l16], %[rs], m0 offen" DSC1 " lds\n\t"
#define P2_ST "buffer_store_dwordx4 %[pv], %[l16], %[rs], %[so] offen offset:%c[dm] sc1\n\t"
#define P2_S2 "buffer_store_dwordx2 %[p2], %[l16], %[rs], %[so] offen sc1\n\t"
  if constexpr (S == 0) {
    static_assert(S != 0 || VM == 0, "");
    asm volatile(P2_M0 "0" P2_R0 P2_M1 P2_R1 : [c] "=&v"(a0), [w0] "+v"(w0), [w1] "+v"(w1) : P2_IN : "memory");
  } else if constexpr (S + 7 < 40) {
    if constexpr (VM == 0) asm volatile(P2_M0 "%[c]" P2_R0 P2_M1 P2_R1 : [c] "+v"(a0), [w0] "+v"(w0), [w1] "+v"(w1) : P2_IN : "memory");
    else if constexpr (VM == 1) asm volatile(P2_M0 "%[c]" P2_R0 P2_LD P2_M1 P2_R1 : [c] "+v"(a0), [w0] "+v"(w0), [w1] "+v"(w1) : P2_IN : "memory");
    else if constexpr (VM == 3) asm volatile(P2_M0 "%[c]" P2_R0 P2_ST P2_M1 P2_R1 : [c] "+v"(a0), [w0] "+v"(w0), [w1] "+v"(w1) : P2_IN : "memory");
    else if constexpr (VM == 4) asm volatile(P2_M0 "%[c]" P2_R0 P2_S2 P2_M1 P2_R1 : [c] "+v"(a0), [w0] "+v"(w0), [w1] "+v"(w1) : P2_IN : "memory");
    else asm volatile(P2_M0 "%[c]\n\t" P2_DMA "ds_read_b128 %[w0], %[a0] offset:%c[o0]\n\t" P2_DMB P2_M1 P2_R1 : [c] "+v"(a0), [w0] "+v"(w0), [w1] "+v"(w1) : P2_IN : "memory", "scc");
  } else if constexpr (S + 6 == 40) {
    static_assert(S + 6 != 40 || VM == 0, "");
    asm volatile(P2_M0 "%[c]" P2_R0 P2_M1 : [c] "+v"(a0), [w0] "+v"(w0), [w1] "+v"(w1) : P2_IN : "memory");
  } else {
    static_assert(VM == 0, "");
    asm volatile(P2_M0 "%[c]\n\t" P2_M1 : [c] "+v"(a0), [w0] "+v"(w0), [w1] "+v"(w1) : P2_IN : "memory");
  }
}
template <bool Z, int RA, int RB> DEV void mf16aa(f32x4& c) {
  if constexpr (Z) asm volatile("v_mfma_f32_16x16x32_bf16 %0, a[%c1:%c2], a[%c3:%c4], 0" : "=v"(c) : "i"(RA), "i"(RA + 3), "i"(RB), "i"(RB + 3));
  else asm volatile("v_mfma_f32_16x16x32_bf16 %0, a[%c1:%c2], a[%c3:%c4], %0" : "+v"(c) : "i"(RA), "i"(RA + 3), "i"(RB), "i"(RB + 3));
}
template <int RB> DEV void mf16va(f32x4& c, const v4u& a) { asm volatile("v_mfma_f32_16x16x32_bf16 %0, %1, a[%c2:%c3], %0" : "+v"(c) : "v"(a), "i"(RB), "i"(RB + 3)); }
template <bool Z, int RB> DEV void mf16vz(f32x4& c, const v4u& a) {
  if constexpr (Z) asm volatile("v_mfma_f32_16x16x32_bf16 %0, %1, a[%c2:%c3], 0" : "=&v"(c) : "v"(a), "i"(RB), "i"(RB + 3));
  else asm volatile("v_mfma_f32_16x16x32_bf16 %0, %1, a[%c2:%c3], %0" : "+v"(c) : "v"(a), "i"(RB), "i"(RB + 3));
}
#define DSW(addr, off, val) asm volatile("ds_write_b128 %0, %1 offset:%2" :: "v"(addr), "v"(val), "i"(off) : "memory")
#define ADM 12
#define ODM 0
#define GDM 10
#define DLY_O 24
#define DLY_X 13
#define HLP 17
#define DLY_OB 28
#define KFP 12
#define KCK 3
#define LW 8
#define DX1 8
#define DO1 12
#define PX 1
#define P2W 13
#define KQS 4
#define DO_0 36
#define DO_1 24
#define DO_2 19
#define DO_3 18
#define DX_0 20
#define DX_1 10
#define DX_2 12
#define KPS 8

__global__ void __launch_bounds__(256, 1) k_main(Params p) {
  extern __shared__ __attribute__((aligned(16))) unsigned char smem[];
  LAS unsigned char* L = (LAS unsigned char*)smem;
  {
    const unsigned long long pc = __builtin_amdgcn_s_getpc();
    const unsigned t16 = __builtin_amdgcn_workitem_id_x() * 16;
    asm volatile("global_load_dwordx4 a[0:3], %0, %1\n\tglobal_load_dwordx4 a[4:7], %0, %1 offset:4080" :: "v"(t16), "s"(pc) : "memory");
    asm volatile("global_load_dwordx4 a[8:11], %0, %1" :: "v"(t16 + 8192 + ((__builtin_amdgcn_workgroup_id_x() >> 3) % 6) * 4096), "s"(pc) : "memory");
    SB();
  }
  const int b = __builtin_amdgcn_workgroup_id_x(), w = __builtin_amdgcn_readfirstlane(__builtin_amdgcn_workitem_id_x() >> 6);
  const int rg = map_rg(b), cg = map_cg(b), half = o_half(cg), c8 = o_c8(cg);
  const rsrc_t rs = __builtin_amdgcn_make_buffer_rsrc(p.ws, 0, WS_TOTAL, 0x00020000);
  asm volatile("" ::: "a255");
  unsigned vKa, vVa, vHv, vImg, vA0, vA2, vB, vS0, vS1, vK0, vK1, vF0, vF1, vKv, vKp, vOv, vOp, vXv, vSt, vB3, vK2, vK3, vO3, vX3, vSp, vA0x, vA2x, vBx;
  v4u vSel;
  auto addresses = [&]() __attribute__((always_inline)) {
    RELANE();
    const int g = lane >> 4, hh = w >> 1, dt = w & 1, q = (lane >> 2) & 3, p4 = lane & 3, r = lane & 15;
    const unsigned r_ = h * 144 + 2 + 32 * w + tok;
    vKa = L_K + r_ * 48; vVa = L_V + r_ * 80; vHv = (w == 3 && tok >= 16) ? (h * 16 + (tok - 16)) * 16 : 0x80000000u;
    const unsigned t_ = 32 * w + tok, key = (t_ & 7) ^ (((t_ >> 3) & 1) << 2);
    vImg = L_SCR + (h * 128 + t_) * 128 + (key << 4);
    const unsigned k0 = q ^ ((g & 1) << 2), rb0 = L_SCR + (hh * 128 + 8 * g + q) * 128, cB = dt ? 7 : 5 + (p4 >> 1);
    vA0 = rb0 + ((p4 ^ k0) << 4); vA2 = rb0 + ((4 ^ k0) << 4) + (p4 & 1) * 8; vB = rb0 + ((cB ^ k0) << 4) + (p4 & 1) * 8;
    vS0 = dt ? (r < 8 ? 2560 + ((hh * 5 + g) * 8 + r) * 16 : 0x80000000u) : ((hh * 5 + g) * 16 + r) * 16;
    vS1 = g ? 0x80000000u : dt ? (r < 8 ? 2560 + ((hh * 5 + 4) * 8 + r) * 16 : 0x80000000u) : ((hh * 5 + 4) * 16 + r) * 16;
    const unsigned ty0 = L_KVA + r * 16 + g * 8, ty1 = L_KVA + (r >> 3) * 400 + 256 + (r & 7) * 16 + g * 8;
    vK0 = w < 3 ? ty0 + w * 1600 : ty1 + 800;
    vK1 = w < 2 ? ty0 + w * 1600 + 800 : ty1 + (w == 2 ? 0 : 2400);
    if (h) vK0 = vK1 = L_RED + 8192 + tid * 8;
    vK2 = vK0 + (w < 3 ? 400 : 800); vK3 = vK1 + (w < 2 ? 400 : 800);
    vA0x = (vA0 ^ 64) + 512; vA2x = (vA2 ^ 64) + 512; vBx = (vB ^ 64) + 512;
#pragma unroll
    for (int j = 0; j < 4; ++j) vSel[j] = r == 2 * j ? 0x00003F80u : r == 2 * j + 1 ? 0x3F800000u : 0u;
    const bool match = ((tok >> 2) & 1) == h;
    const unsigned row = 4 * (tok >> 3) + (tok & 3);
    vF0 = L_KVA + h * 2000 + (match ? row * 16 : 384); vF1 = L_KVA + h * 2000 + (match && row < 8 ? 256 + row * 16 : 384);
    vKv = g * 4096 + r * 16;
    vKp = (lane >> 2) * 4096 + 3712 + (lane & 3) * 4;
    vOv = (tok >> 4) * 20480 + (tok & 15) * 16 + h * 2048; vOp = g * 20480 + q * 256 + p4 * 4;
    vXv = ((w & 1) * 16 + r) * 16 + g * 1024; vSt = h * 4096 + tok * 8; vB3 = 2048 + h * 384 + tok * 16;
    vO3 = h ? 0x80000000u : vOv; vX3 = (g & 1) ? 0x80000000u : (vXv & 0x1f0) + (h << 9); vSp = g ? 0x80000000u : r * 8;
    asm volatile("" : "+v"(vK2), "+v"(vK3), "+v"(vO3), "+v"(vX3), "+v"(vSp), "+v"(vA0x), "+v"(vA2x), "+v"(vBx), "+v"(vSel));
    asm volatile("" : "+v"(vKa), "+v"(vVa), "+v"(vHv), "+v"(vImg), "+v"(vA0), "+v"(vA2), "+v"(vB), "+v"(vS0), "+v"(vS1), "+v"(vK0));
    asm volatile("" : "+v"(vK1), "+v"(vF0), "+v"(vF1), "+v"(vKv), "+v"(vKp), "+v"(vOv), "+v"(vOp), "+v"(vXv), "+v"(vSt), "+v"(vB3));
    if (tid < 10) sts128(L, L_KVA + tid * 400 + 384, v4u{0u, 0u, 0u, 0u});
  };
  unsigned xr[10];
  Tmo tm;
  { RELANE();
    const int l = 0; (void)l;
    STAMP(0);
    constexpr unsigned SG_B = L_P0 + 2048, SG_O = SG_B + 10240, SG_P = SG_O + 10240, OOR = 0x80000000u;
    const int j = (tid >> 4) & 7, cc = lane & 15, seg = (cc * 13) >> 6, g = lane >> 4;
    v4u ra[3], rb[3], ro[3], rp;
    const unsigned wcol = (seg * 640 + 40 * cg + 8 * (cc - 5 * seg)) * 2;
    unsigned wrow[3];
#pragma unroll
    for (int i = 0; i < 3; ++i) {
      const int kap = 2 * i + (w >> 1) < 4 ? 2 * i + (w >> 1) : 4, lc = 5 * (rg & 1) + kap;
      const int t8 = lc >> 3, rb0 = 80 * (rg >> 1) + (1 - t8) * (20 * (lc >> 1) + 8 * (lc & 1)) + t8 * (40 * (lc - 8) + 16);
      wrow[i] = (rb0 + j + t8 * (16 * (j >> 2))) * 3840 + wcol;
    }
    { const rsrc_t rA = __builtin_amdgcn_make_buffer_rsrc((void*)p.wqA, 0, 640 * 1920 * 2, 0x00020000);
#pragma unroll
      for (int i = 0; i < 3; ++i) ra[i] = ldg(rA, wrow[i], 0); }
    load_x_in(p.x, rg, cg, w, lane, xr);
    const unsigned ep = __builtin_amdgcn_raw_buffer_load_b32(rs, 0, WS_CTRL + 128, SC1);
    FENCE(); SB();
    { const rsrc_t rB = __builtin_amdgcn_make_buffer_rsrc((void*)p.wqB, 0, 640 * 1920 * 2, 0x00020000);
      const rsrc_t rO = __builtin_amdgcn_make_buffer_rsrc((void*)(half ? p.woB : p.woA), 0, 640 * 640 * 2, 0x00020000);
      const rsrc_t rP = __builtin_amdgcn_make_buffer_rsrc((void*)(p.wphi + cg * 800), 0, 1600, 0x00020000);
#pragma unroll
      for (int i = 0; i < 3; ++i) rb[i] = ldg(rB, wrow[i], 0);
#pragma unroll
      for (int i = 0; i < 3; ++i) { const int so = 2 * i + (w >> 1) < 4 ? 2 * i + (w >> 1) : 4, t4 = so >> 2;
        ro[i] = ldg(rO, (40 * rg + (1 - t4) * (20 * (so >> 1) + 8 * (so & 1)) + t4 * 16 + j + t4 * (16 * (j >> 2))) * 1280 + (80 * c8 + 8 * (cc < 10 ? cc : 0)) * 2, 0); }
      rp = ldg(rP, tid * 16, 0);
    FENCE(); SB();
    STAMP(53);
    const unsigned sa = tid * 16;
#pragma unroll
    for (int i = 0; i < 3; ++i) if (i < 2 || w < 2) sts128(L, sa + i * 4096, ra[i]);
    BAR();
    STAMP(54);
    { v4u fr[3];
      build_wq_read(L, 0, w, lane, fr);
      publish_x(rs, WS_X, WS_ST0, rg, cg, w, lane, xr);
      build_wq_store(rs, 0, rg, cg, w, lane, 0, w < 3 ? 32 : 24, fr); }
    tm.ws = p.ws; tm.epoch = __builtin_amdgcn_readfirstlane(ep) + 0x9E3779B1u;
    STAMP(55);
#pragma unroll
    for (int i = 0; i < 3; ++i) if (i < 2 || w < 2) { sts128(L, SG_B + sa + i * 4096, rb[i]); sts128(L, SG_O + sa + i * 4096, ro[i]); }
    if (w < 2) sts128(L, SG_P + sa, rp);
    }
    DRAIN();
    BAR();
    if (tid == 0) __builtin_amdgcn_raw_buffer_store_b32(tm.epoch, rs, 0, WS_FLAG + (cg * 16 + rg) * 128, SC1);
    STAMP(60);
    v4u fv[4], fb[3], fo[2];
    {
      const int sg = w >> 1, hw = w & 1;
      v4u bf[2], af[4];
#pragma unroll
      for (int ct = 0; ct < 2; ++ct) {
        const int q = (lane >> 2) & 3, pp = lane & 3, fcol = ct ? 16 : 4 * pp;
        const int d0 = 8 * g + q < 19 ? 8 * g + q : 19, d1 = 8 * g + 4 + q < 19 ? 8 * g + 4 + q : 19;
        const v2u t0 = ldstr(L, SG_P + hw * 800 + d0 * 40 + fcol * 2), t1 = ldstr(L, SG_P + hw * 800 + d1 * 40 + fcol * 2);
        bf[ct] = v4u{g < 3 ? t0.x : 0u, g < 3 ? t0.y : 0u, g < 2 ? t1.x : 0u, g < 2 ? t1.y : 0u};
      }
#pragma unroll
      for (int i = 0; i < 4; ++i) { const int r = (i < 2 ? 8 * (cc >> 2) : 32) + 4 * (i & 1) + (cc & 3); const unsigned a = SG_B + r * 256 + sg * 80 + hw * 40 + g * 16;
        const v2u t0 = lds64(L, a), t1 = lds64(L, a + 8); af[i] = v4u{t0.x, t0.y, t1.x, t1.y}; }
      build_wq_read(L, SG_B, w, lane, fb);
      { const int q = (lane >> 2) & 3, pp = lane & 3;
#pragma unroll
        for (int it = 0; it < 2; ++it) {
          const int pc = it * 16 + (tid >> 4), pcc = pc < 25 ? pc : 24, so = (pcc * 13) >> 6, ft = pcc - 5 * so;
          const unsigned a = SG_O + (so * 8 + q) * 256 + 40 * pp + 8 * ft;
          const v2u t0 = ldstr(L, a), t1 = ldstr(L, a + 1024);
          fo[it] = v4u{t0.x, t0.y, t1.x, t1.y};
        } }
      f32x4 d[8];
      asm volatile("s_nop 1" : "+v"(bf[0]), "+v"(bf[1]), "+v"(af[0]), "+v"(af[1]), "+v"(af[2]), "+v"(af[3]));
      SFOR(i, 8) mf16v<true>(d[i], af[i & 3], bf[i >> 2]); SEND;
      asm volatile("s_nop 7");
      SB();
#pragma unroll
      for (int i = 0; i < 4; ++i) fv[i] = v4u{pk2(d[2 * i][0], d[2 * i][1]), pk2(d[2 * i][2], d[2 * i][3]), pk2(d[2 * i + 1][0], d[2 * i + 1][1]), pk2(d[2 * i + 1][2], d[2 * i + 1][3])};
    }
    STAMP(58);
    { const int sg = w >> 1, hw = w & 1;
#pragma unroll
      for (int ct = 0; ct < 2; ++ct) {
        const int row = ct ? (sg ? 0 : 16) + 4 * hw + (cc & 3) : 8 * (cc >> 2) + 4 * hw + (cc & 3);
        const unsigned tb = ct ? 2048 : (sg ? 0 : L_P0), ts = (ct || sg) ? KSTEP : 1024, th = ct ? 384 : 512;
        const bool in = ct == 0 || cc < 4;
#pragma unroll
        for (int u = 0; u < 2; ++u) { const int c = 5 * rg + (u ? 4 : g);
          stg_sc1(rs, (in && (u == 0 || g == 0)) ? (c >> 1) * ts + (c & 1) * th + row * 16 : OOR, WS_WQ + (16 + cg) * WQ_SZ + tb, fv[2 * ct + u]); }
        float cs = sum8(fv[2 * ct], 0.f);
        cs = g ? cs : sum8(fv[2 * ct + 1], cs);
        { auto t = __builtin_amdgcn_permlane16_swap(f2u(cs), f2u(cs), false, false); cs = u2f(t[0]) + u2f(t[1]); }
        { auto t = __builtin_amdgcn_permlane32_swap(f2u(cs), f2u(cs), false, false); cs = u2f(t[0]) + u2f(t[1]); }
        __builtin_amdgcn_raw_buffer_store_b32(f2u(cs), rs, (in && g == 0) ? row * 4 : OOR, WS_CSP + ((16 + cg) * 16 + rg) * 512 + (ct ? 3 : sg) * 128, SC1);
      }
      if (w >= 2) build_wq_store(rs, 1, rg, cg, w, lane, w == 2 ? 0 : 8, w == 2 ? 32 : 16, fb);
#pragma unroll
      for (int it = 0; it < 2; ++it) { const int pc = it * 16 + (tid >> 4);
        stg_sc1(rs, pc < 25 ? pc * 1024 + cc * 16 : OOR, WS_WO + (half * 8 + c8) * WO_SZ + (rg >> 2) * 25600 + (rg & 3) * 256, fo[it]); }
    }
    poison_o(rs, WS_O, rg, cg, w, lane);
    stg_sc1(rs, l16, WS_KV + (cg * 16 + rg) * 4096 + w * 1024, v4u{SENT, SENT, SENT, SENT});
    stg_sc1(rs, l16, WS_HALO + (rg * 16 + cg) * 4096 + w * 1024, v4u{SENT, SENT, SENT, SENT});
    addresses();
    STAMP(61);
    if (w == 1) wait_flags(rs, (h == 0 ? cc * 16 + rg : cg * 16 + cc) * 128, WS_FLAG, tm);
    BAR();
    STAMP(62);
    const rsrc_t rw = __builtin_amdgcn_make_buffer_rsrc(p.ws + WS_WQ + cg * WQ_SZ, 0, WQ_SZ, 0x00020000);
    SFOR(i, 22 + LW) dmaw<i>(rw, l16, w * 1024); SEND;
    float csv[16];
#pragma unroll
    for (int r = 0; r < 16; ++r) csv[r] = u2f(__builtin_amdgcn_raw_buffer_load_b32(rs, (tid & 127) * 4 + (r & 7) * 512, WS_CSP + cg * 8192 + (r >> 3) * 4096, SC1));
    FENCE(); SB();
    STAMP(63);
    FENCE(); SB();
    STAMP(57);
    { float cs = 0.f;
#pragma unroll
      for (int r = 0; r < 16; ++r) cs += csv[r];
      const unsigned ch = pk2(cs, 0.f) & 0xffffu, cl = pk2(cs - lo16(ch), 0.f) & 0xffffu;
      asm volatile("" :: "v"(cs));
      if (tid < 128) sts128(L, L_CS + tid * 16, v4u{ch | (cl << 16), ch, 0u, 0u}); }
    BAR();
    if (tid == 0) __builtin_amdgcn_raw_buffer_store_b32(tm.epoch, rs, 0, WS_FLAG + (256 + cg * 16 + rg) * 128, SC1);
  }

  unsigned sX = WS_X + (rg * 4 + w) * 40960, sSt = WS_ST0 + (rg * 1024 + w * 32) * 8, sWo = WS_WO + (c8 * 4 + w) * 25600;
  unsigned sOp = WS_O + ((rg * 8 + 2 * w) * 4 + (cg >> 2)) * 5120 + (cg & 3) * 256, sOr = WS_O + ((rg * 8 + half * 4) * 4 + w) * 5120;
  unsigned sXp = WS_X + XB + ((rg * 4 + half * 2 + (w >> 1)) * 40 + 5 * c8) * 1024, sSp = WS_ST1 + ((rg * 8 + c8) * 128 + half * 64 + w * 16) * 8;
  pin_s(sX); pin_s(sSt); pin_s(sWo); pin_s(sOp); pin_s(sOr); pin_s(sXp); pin_s(sSp);
  auto layer = [&](auto m_, const int ll) __attribute__((always_inline)) {
    constexpr int m = decltype(m_)::v;
    const int l = 2 * ll + m;
    const rsrc_t rw = __builtin_amdgcn_make_buffer_rsrc(p.ws + WS_WQ + ((m ^ 1) * 16 + cg) * WQ_SZ, 0, l < 3 ? WQ_SZ : 0u, 0x00020000);
    const unsigned wb1k = w * 1024;
#define DM(i) do { SB(); dmaw<(i)>(rw, l16, wb1k); SB(); } while (0)
    unsigned wos;
#define LDWO(i) do { lda<160 + 4 * (i), ((i) & 3) * 1024, true>(rs, l16, wos); if constexpr (((i) & 3) == 3) sbump(wos); } while (0)
#define P2VM(pp) ((pp) >= 2 && (pp) <= 16 && (pp) != 6 && (pp) != 11 ? 1 : 0)
#define P2VI(pp) (P2VM(pp) ? 7 + (pp) - 2 - ((pp) > 6) - ((pp) > 11) : 0)
#define P2CNT(pp) ((pp) < 2 ? 0 : (pp) - 1 - ((pp) >= 6) - ((pp) >= 11))
#define P2PAIR(pp, nw) do { p2pair<2 * (pp), nw, P2VM(pp), P2VI(pp)>(a0, wq[(2 * (pp)) % 6], wq[(2 * (pp) + 1) % 6], bq, bcq, rs, l16, wos, mfr); \
                            if constexpr (P2VM(pp) == 1 && (P2VI(pp) & 3) == 3) sbump(wos); } while (0)
    v4u wo24, mfr, wq[6];
    unsigned bq, bcq;
    f32x16 a0, a1, a2, a3;
    float rstd, sd, o[20];
    unsigned osb[4], ops, cXp, cSp, cOp, cWo;
    {
      RELANE();
      STAMP(1 + 12 * l);
      asm volatile("" : "+v"(vSt), "+v"(vB3));
      const unsigned xs = sX, sts = sSt;
#define LDX(s) do { lda<4 * (s), ((s) & 3) * 1024, true>(rs, l16, xso); if constexpr (((s) & 3) == 3) sbump(xso); } while (0)
      const unsigned stv = vSt;
      constexpr int NB = PFD + 1, HI = 23;
      constexpr int XR = 15 - XW;
      const unsigned b12 = l16, b3 = vB3;
      unsigned b12h, b3h, bcs;
      unsigned f1 = 0, f1v = 0;
      const rsrc_t rl = __builtin_amdgcn_make_buffer_rsrc(p.ws + WS_WQ + (m * 16 + cg) * WQ_SZ, 0, WQ_SZ, 0x00020000);
      auto pass1 = [&](auto hot_) __attribute__((always_inline)) {
        constexpr bool HOT = decltype(hot_)::v;
        v2u st[4];
        unsigned xso;
        auto window = [&](auto bar_) __attribute__((always_inline)) {
          xso = xs;
          ldst(st, rs, stv, sts);
          SFOR(s, XW) SB(); LDX(s); if constexpr (decltype(bar_)::v && (s == 1 || s == 5)) { SB(); BARE(); } SEND;
          FENCE(); SB();
        };
        wos = sWo;
        window(IC<HOT>{});
        v4u wb[NB][3];
        auto rd = [&](auto s_, auto t_) __attribute__((always_inline)) {
          constexpr int s = decltype(s_)::v, t = decltype(t_)::v;
          if constexpr (s < 40) dsr<(s < HI ? s : s - HI) * KSTEP + (t == 1 ? 1024 : 0)>(wb[s % NB][t], t == 2 ? (s < HI ? b3 : b3h) : (s < HI ? b12 : b12h));
          else if constexpr (s == 40) dsr<(t + 1) * 512>(wb[s % NB][t], bcs);
        };
        SFOR(j, PFD) rd(IC<j>{}, IC<2>{}); rd(IC<j>{}, IC<1>{}); rd(IC<j>{}, IC<0>{}); SEND;
        if (__builtin_expect(any_sent(st_wait<XW>(st)), 0)) {
          unsigned n = 0;
          do { CNT(0); poll_probe<200>(rs, (lane >> 3) * 1024 + (lane & 4) * 32 + 4, sts, tm); window(IC<0>{}); } while (any_sent(st_wait<XW>(st)) && ++n < 64);
        }
        if (HOT && m == 1 && l == 1 && w < 2) {
          float cs = 0.f;
          SFOR(r, 16) cs += rda1<210 + r>(); SEND;
          const unsigned ch = pk2(cs, 0.f) & 0xffffu, cl = pk2(cs - lo16(ch), 0.f) & 0xffffu;
          sts128(L, L_CS + 2048 + tid * 16, v4u{ch | (cl << 16), ch, 0u, 0u});
        }
        float sa, sq, nm;
        auto stat = [&](auto s_) __attribute__((always_inline)) {
          constexpr int s = decltype(s_)::v;
          if constexpr (s == 1) { sa = u2f(st[0].x) + u2f(st[1].x); asm volatile("" : "+v"(sa)); sq = u2f(st[0].y) + u2f(st[1].y); asm volatile("" : "+v"(sq)); }
          if constexpr (s == 2) { sa += u2f(st[2].x); asm volatile("" : "+v"(sa)); sq += u2f(st[2].y); asm volatile("" : "+v"(sq)); }
          if constexpr (s == 3) { sa += u2f(st[3].x); asm volatile("" : "+v"(sa)); sq += u2f(st[3].y); asm volatile("" : "+v"(sq)); }
          if constexpr (s == 5) { auto x0 = __builtin_amdgcn_permlane32_swap(f2u(sa), f2u(sa), false, false); nm = u2f(x0[0]) + u2f(x0[1]); asm volatile("" : "+v"(nm)); }
          if constexpr (s == 6) { auto x1 = __builtin_amdgcn_permlane32_swap(f2u(sq), f2u(sq), false, false); sq = u2f(x1[0]) + u2f(x1[1]); asm volatile("" : "+v"(sq)); }
          if constexpr (s == 7) { nm *= -1.f / 640.f; sq = sq * (1.f / 640.f) - nm * nm; asm volatile("" : "+v"(nm), "+v"(sq)); }
          if constexpr (s == 8) { sq = __builtin_fmaxf(sq, 0.f) + 1e-5f; rstd = __builtin_amdgcn_rsqf(sq); asm volatile("" : "+v"(rstd), "+v"(sq)); }
          if constexpr (s == 9) { sd = sq * rstd; mfr.x = pk2(nm, nm); asm volatile("" : "+v"(sd), "+v"(mfr.x)); }
          if constexpr (s == 10) { mfr.y = pk2(nm - lo16(mfr.x), 0.f); asm volatile("" : "+v"(mfr.y)); }
          if constexpr (s == 11) { mfr = v4u{h ? 0u : mfr.x, h ? 0u : mfr.y, 0u, 0u}; asm volatile("" : "+v"(mfr)); }
          if constexpr (s == 12) { b12h = b12 + HI * KSTEP; b3h = b3 + HI * KSTEP; asm volatile("" : "+v"(b12h), "+v"(b3h)); }
          if constexpr (s == 13) { bcs = L_CS + m * 2048 + (l16 & 0x1f0); bq = L_P0 + l16; asm volatile("" : "+v"(bcs), "+v"(bq)); bcq = bcs; }
        };
        SFOR(s, 40)
          SB();
          if constexpr (HOT && m == 0 && s == 27) {
            if (l == 0) { const int ln = fresh_lane(); f1v = (ln < 16 ? ln * 16 + rg : (ln < 32 ? cg : (c8 >> 2) * 8 + ((ln >> 4) & 1) * 4 + (c8 & 3)) * 16 + (ln & 15)) * 128; probe_issue(f1, rs, f1v, WS_FLAG + 256 * 128); }
            SB();
          }
          if constexpr (HOT && m == 0 && s == 33) {
            if (l == 0) {
              probe_wait<6>(f1);
              if (__builtin_amdgcn_ballot_w64(f1 != tm.epoch) != 0) wait_flags(rs, f1v, WS_FLAG + 256 * 128, tm);
            }
            SB();
          }
          if constexpr (HOT && s == 10) { BARE(); SB(); }
          constexpr int c = s % NB;
          constexpr int issued = s <= XR ? XW + 2 * s : (15 + s < 40 ? 15 + s : 40);
          asm volatile("s_waitcnt vmcnt(%0) lgkmcnt(%1)" :: "i"(issued - 1 - s + (s > 25 ? s - 25 : 0)), "i"(s == 39 ? 6 : 3 * ((s + PFD <= 41 ? PFD : 41 - s) - 1)) : "memory");
          mf32a<s == 0, 4 * s>(a1, wb[c][0]);
          if constexpr (HOT && s == 0) STAMP(2 + 12 * l);
          rd(IC<s + PFD>{}, IC<2>{});
          if constexpr (s < XR) LDX(issued);
          SB();
          mf32a<s == 0, 4 * s>(a2, wb[c][1]);
          rd(IC<s + PFD>{}, IC<1>{});
          stat(IC<s>{});
          SB();
          mf32a<s == 0, 4 * s>(a3, wb[c][2]);
          rd(IC<s + PFD>{}, IC<0>{});
                    if constexpr (s < 25) LDX(issued + (s < XR ? 1 : 0));
          else if constexpr (s < 32) dmaw<30 + (s >= 25 ? s - 25 : 0)>(rl, l16, wb1k);
          else if constexpr (s == 32) dmaw<0>(rl, l16, wb1k + (w < 2 ? 37 : 36) * 4096);
          else LDWO(s - 33);
          if constexpr (s >= 38) { dsr<(s - 38) * 3072>(wq[(s - 38) * 3], bq); dsr<(s - 38) * 3072 + 1024>(wq[(s - 38) * 3 + 1], bq); dsr<(s - 38) * 3072 + 2048>(wq[(s - 38) * 3 + 2], bq); }
        SEND;
        SB();
        lgkm<6>();
        mf32v<false>(a1, wb[40 % NB][0], mfr); mf32v<false>(a3, wb[40 % NB][2], mfr); mf32v<false>(a2, wb[40 % NB][1], mfr);
        P2PAIR(0, 4); P2PAIR(1, 4);
        asm volatile("s_nop 3");
        SB();
        if constexpr (!HOT) lgkm<0>();
      };
      pass1(IC<1>{});
      if (__builtin_expect(any_nan(a1[0]), 0)) {
        unsigned att = 0;
        lgkm<0>();
        do { CNT(1); pass1(IC<0>{}); } while (any_nan(a1[0]) && ++att < 32);
      }
      STAMP(3 + 12 * l);
    }
    vmc<7>();
    BARE();
    if (l == 0) {
      RELANE();
    v4u PV;
    asm volatile("v_mov_b32 %0, -1\n\tv_mov_b32 %1, -1\n\tv_mov_b32 %2, -1\n\tv_mov_b32 %3, -1" : "=v"(PV.x), "=v"(PV.y), "=v"(PV.z), "=v"(PV.w));
      stg_sc1(rs, vXv, sXp, PV); stg_sc1(rs, vXv + 512, sXp, PV); stg_sc1(rs, vX3, sXp + 4096, PV);
      __builtin_amdgcn_raw_buffer_store_b64(v2u{PV.x, PV.y}, rs, vSp, sSp, SC1);
    }
#pragma unroll
    for (int i = 0; i < 4; ++i) osb[i] = sOr + ((w + 1 + i) & 3) * 20480;
    ops = sOr + 4096; cWo = sWo + ((7 + P2W) >> 2) * 4096; cXp = sXp; cSp = sSp; cOp = sOp;
    sX += XB; sSt += STB; sWo += m ? -8 * WO_SZ : 8 * WO_SZ; sOp += XB; sOr += XB; sXp += XB; sSp += STB;
    constexpr int NDM2 = 0;
    if constexpr (m == 0) {
      RELANE();
      asm volatile("" : "+v"(vKa), "+v"(vVa), "+v"(vHv));
      v4u kc0, kc1, kc2, hd[4];
      const unsigned hsrc = WS_HALO + (((l >> 1) * 16 + rg - 1) * 16 + cg) * 4096;
#define VCH(i) ((i) < 4 ? v4u{f2u(a2[4 * (i)]), f2u(a2[4 * (i) + 1]), f2u(a2[4 * (i) + 2]), f2u(a2[4 * (i) + 3])} : v4u{f2u(a3[4]), f2u(a3[5]), f2u(a3[6]), f2u(a3[7])})
      {
        f32x2 t[10];
#pragma unroll
        for (int i = 0; i < 8; ++i) t[i] = pkmul(f32x2{a1[2 * i], a1[2 * i + 1]}, f32x2{rstd, rstd});
#pragma unroll
        for (int i = 0; i < 2; ++i) t[8 + i] = pkmul(f32x2{a3[2 * i], a3[2 * i + 1]}, f32x2{rstd, rstd});
        kc0 = v4u{pk2(t[0][0], t[0][1]), pk2(t[1][0], t[1][1]), pk2(t[2][0], t[2][1]), pk2(t[3][0], t[3][1])};
        kc1 = v4u{pk2(t[4][0], t[4][1]), pk2(t[5][0], t[5][1]), pk2(t[6][0], t[6][1]), pk2(t[7][0], t[7][1])};
        kc2 = v4u{pk2(t[8][0], t[8][1]), pk2(t[9][0], t[9][1]), 0u, 0u};
#pragma unroll
        for (int i = 0; i < 8; ++i) { const f32x2 u = pkmul(f32x2{a2[2 * i], a2[2 * i + 1]}, f32x2{rstd, rstd}); a2[2 * i] = u[0]; a2[2 * i + 1] = u[1]; }
#pragma unroll
        for (int i = 2; i < 4; ++i) { const f32x2 u = pkmul(f32x2{a3[2 * i], a3[2 * i + 1]}, f32x2{rstd, rstd}); a3[2 * i] = u[0]; a3[2 * i + 1] = u[1]; }
        asm volatile("" : "+v"(kc0), "+v"(kc1), "+v"(kc2), "+v"(a2), "+v"(a3)); }
      unsigned qp[10];
      const rsrc_t rh = __builtin_amdgcn_make_buffer_rsrc(p.ws + hsrc, 0, rg > 0 ? 4096u : 0u, 0x00020000);
      if (w == 3) {
        const unsigned hs = hsrc + 65536;
        stg_sc1(rs, vHv, hs, kc0); stg_sc1(rs, vHv + 512, hs, kc1); stg_sc1(rs, vHv + 1024, hs, kc2);
#pragma unroll
        for (int i = 0; i < 5; ++i) stg_sc1(rs, vHv + 1536 + i * 512, hs, VCH(i));
      }
      SFOR(p_, 18)
        constexpr int pp = p_ + 2;
        SB();
        constexpr int nwr = (pp - 1 >= 2 ? (pp - 1 > 16 ? 16 : pp - 1) / 2 : 0) - (pp - 4 >= 2 ? (pp - 4 > 16 ? 16 : pp - 4) / 2 : 0);
        P2PAIR(pp, (2 * pp + 6 <= 41 ? 4 : 39 - 2 * pp) + nwr);
        if constexpr (pp == 2) DSW(vKa, 14 * 48, kc0);
        if constexpr (pp == 4) DSW(vKa, 14 * 48 + 16, kc1);
        if constexpr (pp == 6) DSW(vKa, 14 * 48 + 32, kc2);
        if constexpr (pp >= 8 && pp <= 16 && !(pp & 1)) { constexpr int vi = pp >= 8 ? (pp - 8) / 2 : 0; DSW(vVa, 14 * 80 + vi * 16, VCH(vi)); }
        if constexpr (pp == 11) { qp[8] = pk2(a3[8], a3[9]); qp[9] = pk2(a3[10], a3[11]); asm volatile("" : "+v"(qp[8]), "+v"(qp[9])); }
        if constexpr (pp == HLP) {
          if (w == 0) {
#pragma unroll
            for (int i = 0; i < 4; ++i) hd[i] = ldg_sc1(rh, l16 + i * 1024, 0);
          }
        }
      SEND;
      SB();
      lgkm<0>();
      mf32v<false>(a0, wq[40 % 6], mfr);
      vmc<P2W>();
      BARE();
      const unsigned ka = vKa, va = vVa;
      const float fq = rstd * (LOG2E * 0.22360679774997896f);
      STAMP(5 + 12 * l);
      if (w == 0) {
        const unsigned q_ = ((lane >> 4) & 1) * 144 + (lane & 15);
        unsigned h0 = L_K + q_ * 48 + h * 16, h1 = h ? L_V + q_ * 80 : L_K + q_ * 48 + 32, h2 = L_V + q_ * 80 + 16 + h * 16;
        asm volatile("" : "+v"(h0), "+v"(h1), "+v"(h2));
        unsigned mm = 0;
#pragma unroll
        for (int i = 0; i < 4; ++i) mm = mx4(mm, hd[i]);
        if (__builtin_expect(any_sent(mm), 0)) {
          unsigned sp = 0;
          do {
            FENCE();
            mm = 0;
#pragma unroll
            for (int i = 0; i < 4; ++i) hd[i] = ldg_sc1(rh, l16 + i * 1024, 0);
#pragma unroll
            for (int i = 0; i < 4; ++i) mm = mx4(mm, hd[i]);
            CNT(2);
          } while (any_sent(mm) && !give_up(sp, tm));
        }
        sts128(L, h0, hd[0]);
        sts128(L, h1, hd[1]);
        sts128(L, h2, hd[2]);
        sts128(L, h2 + 32, hd[3]);
      }
      STAMP(4 + 12 * l);
      float lg[8];
      v4u kb[8][2], vb[8][5];
      v2u kt[8];
      auto rdk = [&](auto j_) __attribute__((always_inline)) { constexpr int j = decltype(j_)::v;
        kb[j][0] = lds128(L, ka + (7 - j) * 96); kb[j][1] = lds128(L, ka + (7 - j) * 96 + 16); kt[j] = lds64(L, ka + (7 - j) * 96 + 32); };
      auto rdv = [&](auto j_) __attribute__((always_inline)) { constexpr int j = decltype(j_)::v;
#pragma unroll
        for (int i = 0; i < 5; ++i) vb[j][i] = lds128(L, va + (7 - j) * 160 + i * 16); };
      SB();
      rdk(IC<0>{}); rdk(IC<1>{}); rdk(IC<2>{}); rdk(IC<3>{});
      SB();
#pragma unroll
      for (int i = 0; i < 8; ++i) qp[i] = pk2(a0[2 * i], a0[2 * i + 1]);
      SFOR(jj, 4)
        SB();
        if constexpr (jj < ADM) DM(NDM2 + jj);
        if constexpr (jj < 2) { rdk(IC<2 * jj + 4>{}); rdk(IC<2 * jj + 5>{}); } else rdv(IC<jj - 2>{});
        SB();
        float s0 = 0.f, s1 = 0.f;
#pragma unroll
        for (int i = 0; i < 8; ++i) { s0 = dot2(kb[2 * jj][i >> 2][i & 3], qp[i], s0); s1 = dot2(kb[2 * jj + 1][i >> 2][i & 3], qp[i], s1); }
#pragma unroll
        for (int i = 0; i < 2; ++i) { s0 = dot2(kt[2 * jj][i], qp[8 + i], s0); s1 = dot2(kt[2 * jj + 1][i], qp[8 + i], s1); }
        asm volatile("" : "+v"(s0), "+v"(s1));
        lg[2 * jj] = s0; lg[2 * jj + 1] = s1;
      SEND;
      SB();
      if constexpr (4 < ADM) DM(NDM2 + 4);
      rdv(IC<2>{});
      SB();
      if (rg == 0 && w == 0) {
#pragma unroll
        for (int j = 1; j < 8; ++j) lg[j] = tok >= 2 * j ? lg[j] : -__builtin_inff();
      }
      const float mx = max3f(max3f(lg[0], lg[1], lg[2]), max3f(lg[3], lg[4], lg[5]), max3f(lg[6], lg[7], lg[7]));
      const float nb = -fq * mx;
#pragma unroll
      for (int j = 0; j < 8; j += 2) { const f32x2 t = pkfma(f32x2{lg[j], lg[j + 1]}, f32x2{fq, fq}, f32x2{nb, nb}); lg[j] = t[0]; lg[j + 1] = t[1]; }
      SB();
#pragma unroll
      for (int j = 0; j < 8; ++j) lg[j] = __builtin_amdgcn_exp2f(lg[j]);
      SB();
      if constexpr (5 < ADM) DM(NDM2 + 5);
      SB();
      const float inv = __builtin_amdgcn_rcpf(((lg[0] + lg[1]) + (lg[2] + lg[3])) + ((lg[4] + lg[5]) + (lg[6] + lg[7])));
      f32x2 oa[10];
      SFOR(j, 8)
        SB();
        if constexpr (6 + j < ADM) DM(NDM2 + 6 + j);
        if constexpr (j < 5) rdv(IC<j + 3>{});
        SB();
        const float pj = lg[j];
        SFOR(i, 5)
          const f32x4 v = __builtin_bit_cast(f32x4, vb[j][i]);
          if constexpr (j == 0) { oa[2 * i] = pkmul(f32x2{pj, pj}, f32x2{v[0], v[1]}); oa[2 * i + 1] = pkmul(f32x2{pj, pj}, f32x2{v[2], v[3]}); }
          else { oa[2 * i] = pkfma(f32x2{pj, pj}, f32x2{v[0], v[1]}, oa[2 * i]); oa[2 * i + 1] = pkfma(f32x2{pj, pj}, f32x2{v[2], v[3]}, oa[2 * i + 1]); }
        SEND;
      SEND;
#pragma unroll
      for (int i = 0; i < 10; ++i) oa[i] = pkmul(oa[i], f32x2{inv, inv});
#pragma unroll
      for (int i = 0; i < 10; ++i) { o[2 * i] = oa[i][0]; o[2 * i + 1] = oa[i][1]; }
    } else {
      RELANE();
      asm volatile("" : "+v"(vImg), "+v"(vA0), "+v"(vA2), "+v"(vB), "+v"(vS0), "+v"(vS1), "+v"(vK0), "+v"(vK1), "+v"(vF0), "+v"(vF1), "+v"(vKv), "+v"(vKp));
      const float sl = rstd * LOG2E;
      {
        const unsigned rb = vImg;
        float kk[20];
#pragma unroll
        for (int i = 0; i < 16; ++i) kk[i] = a1[i];
#pragma unroll
        for (int i = 0; i < 4; ++i) kk[16 + i] = a3[i];
        unsigned ph[20];
        phi_map<true>(kk, sl, rstd, ph, [&](auto i_) __attribute__((always_inline)) { constexpr int i = decltype(i_)::v; SB();
          if constexpr (i < ADM) dmaw<NDM2 + i>(rw, l16, wb1k);
          if (i == 0) sts128(L, rb ^ (5 << 4), v4u{pk2(a2[0], a2[1]), pk2(a2[2], a2[3]), pk2(a2[4], a2[5]), pk2(a2[6], a2[7])});
          if (i == 1) sts128(L, rb ^ (6 << 4), v4u{pk2(a2[8], a2[9]), pk2(a2[10], a2[11]), pk2(a2[12], a2[13]), pk2(a2[14], a2[15])});
          if (i == 2) sts128(L, rb ^ (7 << 4), v4u{pk2(a3[4], a3[5]), pk2(a3[6], a3[7]), pk2(sd, 0.f), 0u});
          if (i == 5) sts128(L, rb ^ (0 << 4), v4u{ph[0], ph[1], ph[2], ph[3]});
          if (i == 6) sts128(L, rb ^ (1 << 4), v4u{ph[4], ph[5], ph[6], ph[7]});
          SB(); });
#pragma unroll
        for (int c = 2; c < 5; ++c) sts128(L, rb ^ (c << 4), v4u{ph[4 * c], ph[4 * c + 1], ph[4 * c + 2], ph[4 * c + 3]});
      }
      {
        const unsigned aA0 = vA0, aA2 = vA2, aB = vB;
        asm volatile("" : "+v"(vA0x), "+v"(vA2x), "+v"(vBx), "+v"(vK2), "+v"(vK3), "+v"(vSel));
        const unsigned aA0x = vA0x, aA2x = vA2x, aBx = vBx;
        BAR();
        f32x4 d0, d1, d2;
        v2u t[4][8];
        auto trd = [&](auto k_) __attribute__((always_inline)) {
          constexpr int ks = decltype(k_)::v; constexpr unsigned o_ = ks * 4096;
          t[ks][0] = ldstr(L, aB + o_); t[ks][1] = ldstr(L, aBx + o_); t[ks][6] = ldstr(L, aA2 + o_); t[ks][7] = ldstr(L, aA2x + o_);
          t[ks][2] = ldstr(L, aA0 + o_); t[ks][3] = ldstr(L, aA0x + o_); t[ks][4] = ldstr(L, aA0 + 8 + o_); t[ks][5] = ldstr(L, aA0x + 8 + o_);
          SB();
        };
        trd(IC<0>{});
        SFOR(ks, 4)
          if constexpr (ks < 3) trd(IC<ks + 1>{});
          const v4u bb = {t[ks][0].x, t[ks][0].y, t[ks][1].x, t[ks][1].y};
          mf16v<ks == 0>(d2, v4u{t[ks][6].x, t[ks][6].y, t[ks][7].x, t[ks][7].y}, bb);
          mf16v<ks == 0>(d0, v4u{t[ks][2].x, t[ks][2].y, t[ks][3].x, t[ks][3].y}, bb);
          mf16v<ks == 0>(d1, v4u{t[ks][4].x, t[ks][4].y, t[ks][5].x, t[ks][5].y}, bb);
          SB();
        SEND;
        asm volatile("s_nop 7");
        SB();
        const unsigned e0 = pk2(d2[0], d2[1]), e1 = pk2(d2[2], d2[3]);
        auto s0 = __builtin_amdgcn_permlane16_swap(e0, e0, false, false);
        auto s1 = __builtin_amdgcn_permlane16_swap(e1, e1, false, false);
        const unsigned ks_ = WS_KV + (((l >> 1) * 16 + cg) * 16 + rg) * 4096;
        stg_sc1(rs, vS0, ks_, v4u{pk2(d0[0], d0[1]), pk2(d0[2], d0[3]), pk2(d1[0], d1[1]), pk2(d1[2], d1[3])});
        stg_sc1(rs, vS1, ks_, v4u{e0, e1, s0[1], s1[1]});
      }
      STAMP(4 + 12 * l);
      if (l == 1) {
    v4u PV;
    asm volatile("v_mov_b32 %0, -1\n\tv_mov_b32 %1, -1\n\tv_mov_b32 %2, -1\n\tv_mov_b32 %3, -1" : "=v"(PV.x), "=v"(PV.y), "=v"(PV.z), "=v"(PV.w));
#pragma unroll
        for (int u = 0; u < 2; ++u) {
          stg_sc1(rs, vOv, sOp + u * XB, PV); stg_sc1(rs, vOv + 1024, sOp + u * XB, PV); stg_sc1(rs, vO3, sOp + u * XB + 4096, PV);
          stg_sc1(rs, vXv, cXp + u * XB, PV); stg_sc1(rs, vXv + 512, cXp + u * XB, PV); stg_sc1(rs, vX3, cXp + u * XB + 4096, PV);
          __builtin_amdgcn_raw_buffer_store_b64(v2u{PV.x, PV.y}, rs, vSp, cSp + u * STB, SC1);
        }
      }
      const unsigned ks_ = WS_KV + ((l >> 1) * 16 + cg) * 65536 + (w < 3 ? w * 1024 : 11 * 256);
      const unsigned kvv = vKv;
      unsigned kpr, kso[4];
#pragma unroll
      for (int i = 0; i < 4; ++i) kso[i] = ks_ + i * 16384;
#define LDKP(r) lda<4 * (r), ((r) & 3) * 256, true>(rs, kvv, kso[(r) >> 2])
      const unsigned kps = WS_KV + ((l >> 1) * 16 + cg) * 65536, kpv = vKp;
      SFOR(p_, 18) constexpr int pp = p_ + 2; SB(); P2PAIR(pp, (2 * pp + 6 <= 41 ? 4 : 39 - 2 * pp));
      SEND;
      SB();
      lgkm<0>();
      mf32v<false>(a0, wq[40 % 6], mfr);
      MFMA_SETTLE();
      FENCE(); SB();
      STAMP(5 + 12 * l);
      unsigned ph[20];
      float eps;
      { float qq[20];
#pragma unroll
        for (int i = 0; i < 4; ++i) qq[16 + i] = a3[8 + i];
#pragma unroll
        for (int i = 0; i < 16; ++i) qq[i] = a0[i];
        eps = 1e-6f * phi_map<false>(qq, sl, 1.f, ph, [&](auto i_) __attribute__((always_inline)) {
          if constexpr (decltype(i_)::v == KQS) { SB(); probe_issue(kpr, rs, kpv, kps); SFOR(r, 16) LDKP(r); SEND; SB(); } });
      }
      v4u f[10];
#pragma unroll
      for (int i = 0; i < 20; i += 4) asm volatile("" : "+v"(ph[i]), "+v"(ph[i + 1]), "+v"(ph[i + 2]), "+v"(ph[i + 3]));
      SB();
      probe_wait<16>(kpr);
      if (__builtin_expect(any_sent(kpr), 0)) { CNT(3); poll_probe<100>(rs, kpv, kps, tm); SFOR(r, 16) LDKP(r); SEND; }
      f32x4 sm[4];
      auto ksum = [&]() __attribute__((always_inline)) {
        SFOR(r, 16) if constexpr ((r & 3) == 0) vmc<12 - r>(); mf16vz<(r < 4), 4 * r>(sm[r & 3], vSel); SEND;
        asm volatile("s_nop 7");
        SB();
        return any_nan(((sm[0][0] + sm[1][0]) + sm[2][0]) + sm[3][0]);
      };
      if (__builtin_expect(ksum(), 0)) {
        unsigned sp = 0;
        do { CNT(1); SFOR(r, 16) LDKP(r); SEND; } while (ksum() && !give_up(sp, tm));
      }
      {
        v2u t2[4];
#pragma unroll
        for (int j = 0; j < 4; ++j) t2[j] = v2u{pk2(sm[j][0], sm[j][1]), pk2(sm[j][2], sm[j][3])};
        asm volatile("ds_write_b64 %0, %4\n\tds_write_b64 %1, %5\n\tds_write_b64 %2, %6\n\tds_write_b64 %3, %7" :: "v"(vK0), "v"(vK2), "v"(vK1), "v"(vK3), "v"(t2[0]), "v"(t2[1]), "v"(t2[2]), "v"(t2[3]) : "memory");
      }
      SFOR(i, (ADM > 10 ? ADM - 10 : 0)) DM(10 + i); SEND;
      BAR();
#pragma unroll
      for (int s = 0; s < 5; ++s) { f[2 * s + 1] = lds128(L, vF1 + s * 400); f[2 * s] = lds128(L, vF0 + s * 400); }
      f32x16 n0, n1;
      SFOR(s, 5)
        const v4u bb = {ph[4 * s], ph[4 * s + 1], ph[4 * s + 2], ph[4 * s + 3]};
        mf32v<s == 0>(n1, f[2 * s + 1], bb); mf32v<s == 0>(n0, f[2 * s], bb);
      SEND;
      MFMA_SETTLE();
      SB();
      const float inv = __builtin_amdgcn_rcpf(n1[4] + eps);
#pragma unroll
      for (int i = 0; i < 2; ++i) { const f32x2 t = pkmul(f32x2{n1[2 * i], n1[2 * i + 1]}, f32x2{inv, inv}); o[16 + 2 * i] = t[0]; o[17 + 2 * i] = t[1]; }
#pragma unroll
      for (int i = 0; i < 8; ++i) { const f32x2 t = pkmul(f32x2{n0[2 * i], n0[2 * i + 1]}, f32x2{inv, inv}); o[2 * i] = t[0]; o[2 * i + 1] = t[1]; }
    }
    {
      RELANE();
      asm volatile("" : "+v"(vOv), "+v"(vOp), "+v"(vXv), "+v"(vO3), "+v"(vX3), "+v"(vSp));
      const unsigned os = cOp;
      stg_sc1(rs, vOv, os, v4u{pk2(o[0], o[1]), pk2(o[2], o[3]), pk2(o[4], o[5]), pk2(o[6], o[7])});
      stg_sc1(rs, vOv + 1024, os, v4u{pk2(o[8], o[9]), pk2(o[10], o[11]), pk2(o[12], o[13]), pk2(o[14], o[15])});
      const unsigned e0 = pk2(o[16], o[17]), e1 = pk2(o[18], o[19]);
      auto s0 = __builtin_amdgcn_permlane32_swap(e0, e0, false, false);
      auto s1 = __builtin_amdgcn_permlane32_swap(e1, e1, false, false);
      stg_sc1(rs, vO3, os + 4096, v4u{e0, e1, s0[1], s1[1]});
      STAMP(6 + 12 * l);
      if (m == 0 && l == 0) {
        v4u PV;
        asm volatile("v_mov_b32 %0, -1\n\tv_mov_b32 %1, -1\n\tv_mov_b32 %2, -1\n\tv_mov_b32 %3, -1" : "=v"(PV.x), "=v"(PV.y), "=v"(PV.z), "=v"(PV.w));
        stg_sc1(rs, vOv, sOp, PV); stg_sc1(rs, vOv + 1024, sOp, PV); stg_sc1(rs, vO3, sOp + 4096, PV);
        stg_sc1(rs, l16, WS_KV + (256 + cg * 16 + rg) * 4096 + w * 1024, PV);
        stg_sc1(rs, l16, WS_HALO + (256 + rg * 16 + cg) * 4096 + w * 1024, PV);
        __builtin_amdgcn_s_sleep(DO1 - 4);
      } else __builtin_amdgcn_s_sleep(DO1);
      wos = cWo;
      SFOR(i, 17 - P2W) LDWO(7 + P2W + i); SEND;
      asm volatile("buffer_load_dwordx4 %0, %1, %2, %3 offen sc1" : "=v"(wo24) : "v"(l16), "s"(rs), "s"(wos) : "memory");
      SFOR(i, ODM) DM(NDM2 + ADM + i); SEND;
      __builtin_amdgcn_s_sleep(m ? DO_3 : DO_2);
      if (!ll) __builtin_amdgcn_s_sleep(m ? DO_1 - DO_3 : DO_0 - DO_2);
      STAMP(7 + 12 * l);
      const unsigned l16b = l16 + 1024;
#define LDO(i, s) do { if constexpr ((s) == 0) lda<20 * (i), 0, true>(rs, l16, osb[i]); else lda<20 * (i) + 4 * (s), ((s) - 1) * 1024, true>(rs, l16b, osb[i]); } while (0)
      unsigned opr;
      const unsigned opv = vOp;
      probe_issue(opr, rs, opv, ops);
      SFOR(s, 5) LDO(0, s); if constexpr (s == 1) BARE(); SEND;
      BARE();
      SFOR(s, 5) LDO(1, s); SEND;
      SB();
      unsigned rwa[3], rra = L_RED + w * 15360 + l16;
      f32x2 rsum[10];
#pragma unroll
      for (int i = 0; i < 3; ++i) rwa[i] = L_RED + l16 + (((w + 1 + i) & 3) * 3 + i) * 5120;
#pragma unroll
      for (int i = 0; i < 10; ++i) rsum[i] = f32x2{lo16(xr[i]), hi16(xr[i])};
      asm volatile("" : "+v"(rsum[0]), "+v"(rsum[1]), "+v"(rsum[2]), "+v"(rsum[3]), "+v"(rsum[4]), "+v"(rsum[5]), "+v"(rsum[6]), "+v"(rsum[7]), "+v"(rsum[8]), "+v"(rsum[9]));
      asm volatile("" : "+v"(rwa[0]), "+v"(rwa[1]), "+v"(rwa[2]), "+v"(rra));
      f32x4 acc[2][5], part[15];
      probe_wait<10>(opr);
      asm volatile("" : "+v"(wo24));
      if (__builtin_expect(any_sent(opr), 0)) {
        CNT(4);
        poll_probe<100>(rs, opv, ops, tm);
        SFOR(s, 5) LDO(0, s); SEND;
        SFOR(s, 5) LDO(1, s); SEND;
      }
      auto redo = [&](auto i_) __attribute__((always_inline)) {
        constexpr int i = decltype(i_)::v;
        unsigned sp = 0;
        do {
          CNT(1);
          SFOR(s, 5) LDO(i, s); SEND;
          vmc<0>();
          SFOR(j, 25) if constexpr (j < 24) mf16aa<(j < 5), 160 + 4 * j, 20 * i + 4 * (j / 5)>(acc[i & 1][j % 5]); else mf16va<20 * i + 16>(acc[i & 1][4], wo24); SEND;
          asm volatile("s_nop 7");
          SB();
        } while (any_nan(acc[i & 1][4][0]) && !give_up(sp, tm));
      };
      constexpr int NDO = NDM2 + ADM + ODM;
      static_assert(NDO + GDM <= 22 && NDM2 + ADM + ODM <= 17 && GDM <= 10 && ADM <= 14, "");
      SFOR(i, 4)
        SFOR(j, 25)
          constexpr int s = j / 5, ft = j % 5, WS0 = 4, wstep = 4;
          SB();
          constexpr int gi = 5 * i + s;
          if constexpr (ft == 0) vmc<(gi < 10 ? 9 : 19 - gi) + (gi > 20 - GDM ? gi - (20 - GDM) : 0)>();
          if constexpr (j < 24) mf16aa<s == 0, 160 + 4 * j, 20 * i + 4 * s>(acc[i & 1][ft]); else mf16va<20 * i + 16>(acc[i & 1][ft], wo24);
          if constexpr (i == 0 && j == 0) STAMP(8 + 12 * l);
          if constexpr (ft == 2) {
            if constexpr (i < 2) LDO(i + 2, s);
            else if constexpr (gi >= 20 - GDM) dmaw<NDO + gi - (20 - GDM)>(rw, l16, wb1k);
          }
          if constexpr (i > 0 && j == WS0 - 1) { asm volatile("s_nop 1"); SB(); if (__builtin_expect(any_nan(acc[(i - 1) & 1][4][0]), 0)) redo(IC<i - 1>{}); }
          if constexpr (i > 0 && j >= WS0 && j <= WS0 + 4 * wstep && (j - WS0) % wstep == 0)
            DSW(rwa[i > 0 ? i - 1 : 0], ((j - WS0) / wstep) * 1024, acc[(i - 1) & 1][(j - WS0) / wstep]);
          if constexpr (i == 3 && j == 23) { SB(); BAR(); }
          if constexpr (i == 3 && j >= 23) {
#pragma unroll
            for (int q = (j - 23) * 8; q < (j - 23) * 8 + 8 && q < 15; ++q) part[q] = __builtin_bit_cast(f32x4, lds128(L, rra + (q % 3) * 5120 + (q / 3) * 1024));
          }
        SEND;
      SEND;
      SB();
      STAMP(9 + 12 * l);
#pragma unroll
      for (int ft = 0; ft < 5; ++ft)
#pragma unroll
        for (int q = 0; q < 3; ++q) { rsum[2 * ft] = pkadd(rsum[2 * ft], f32x2{part[ft * 3 + q][0], part[ft * 3 + q][1]}); rsum[2 * ft + 1] = pkadd(rsum[2 * ft + 1], f32x2{part[ft * 3 + q][2], part[ft * 3 + q][3]}); }
      asm volatile("" : "+v"(rsum[8]), "+v"(rsum[9]));
      SB();
      if (__builtin_expect(any_nan(acc[1][4][0]), 0)) redo(IC<3>{});
      auto fin = [&](int ft) __attribute__((always_inline)) {
        const f32x2 lo = pkadd(rsum[2 * ft], f32x2{acc[1][ft][0], acc[1][ft][1]}), hi = pkadd(rsum[2 * ft + 1], f32x2{acc[1][ft][2], acc[1][ft][3]});
        xr[2 * ft] = pk2(lo[0], lo[1]); xr[2 * ft + 1] = pk2(hi[0], hi[1]);
      };
      fin(0); fin(1);
      if (l < 3) {
        stg_sc1(rs, vXv, cXp, v4u{xr[0], xr[1], xr[2], xr[3]});
        SB();
        fin(2); fin(3);
        stg_sc1(rs, vXv + 512, cXp, v4u{xr[4], xr[5], xr[6], xr[7]});
        SB();
        fin(4);
        { auto s0 = __builtin_amdgcn_permlane16_swap(xr[8], xr[8], false, false);
          auto s1 = __builtin_amdgcn_permlane16_swap(xr[9], xr[9], false, false);
          stg_sc1(rs, vX3, cXp + 4096, v4u{xr[8], xr[9], s0[1], s1[1]}); }
        SB();
        { float sa = 0.f, sq = 0.f;
#pragma unroll
          for (int i = 0; i < 10; ++i) { sa = dot2(xr[i], 0x3F803F80u, sa); sq = dot2(xr[i], xr[i], sq); }
          { auto a = __builtin_amdgcn_permlane16_swap(f2u(sa), f2u(sa), false, false); sa = u2f(a[0]) + u2f(a[1]);
            auto c = __builtin_amdgcn_permlane16_swap(f2u(sq), f2u(sq), false, false); sq = u2f(c[0]) + u2f(c[1]); }
          { auto a = __builtin_amdgcn_permlane32_swap(f2u(sa), f2u(sa), false, false); sa = u2f(a[0]) + u2f(a[1]);
            auto c = __builtin_amdgcn_permlane32_swap(f2u(sq), f2u(sq), false, false); sq = u2f(c[0]) + u2f(c[1]); }
          __builtin_amdgcn_raw_buffer_store_b64(v2u{f2u(sa), f2u(sq)}, rs, vSp, cSp, SC1); }
        STAMP(10 + 12 * l);
        if (m == 0 && l == 0 && w < 2) {
          const unsigned cv = tid * 4, cv2 = cv + 4096;
          SFOR(r, 16) lda1<210 + r, (r & 7) * 512>(rs, r < 8 ? cv : cv2, WS_CSP + (16 + cg) * 8192); SEND;
        }
        BARE();
        __builtin_amdgcn_s_sleep(DX1);
        SFOR(i, 22 - NDO - GDM + LW) DM(NDO + GDM + i); SEND;
        __builtin_amdgcn_s_sleep(m ? DX_1 : DX_2);
        if (!m && !ll) __builtin_amdgcn_s_sleep(DX_0 - DX_2);
      } else {
        fin(2); fin(3); fin(4);
        GAS v2u* dst = (GAS v2u*)(p.out + (size_t)(rg * 128 + half * 64 + w * 16 + (lane & 15)) * 640 + c8 * 80 + (lane >> 4) * 20);
#pragma unroll
        for (int i = 0; i < 5; ++i) dst[i] = v2u{xr[2 * i], xr[2 * i + 1]};
        if (b == 0 && w == 0) __builtin_amdgcn_raw_buffer_store_b32(tm.epoch, rs, lane ? 0x80000000u : 0u, WS_CTRL + 128, SC1);
      }
      STAMP(11 + 12 * l);
    }
  };
#pragma nounroll
  for (int ll = 0; ll < 2; ++ll) { layer(IC<0>{}, ll); layer(IC<1>{}, ll); }
}

extern "C" void kernel_launch(void* const* d_in, const int* in_sizes, int n_in, void* d_out, int out_size, void* d_ws, size_t ws_size, hipStream_t stream) {
  Params p;
  p.x = (const u16*)d_in[0]; p.wqA = (const u16*)d_in[1]; p.woA = (const u16*)d_in[2];
  p.wqB = (const u16*)d_in[4]; p.wphi = (const u16*)d_in[5]; p.woB = (const u16*)d_in[6];
  p.out = (u16*)d_out; p.ws = (unsigned char*)d_ws;
  (void)hipFuncSetAttribute((const void*)k_main, hipFuncAttributeMaxDynamicSharedMemorySize, LDS_ALLOC);
  k_main<<<256, 256, LDS_ALLOC, stream>>>(p);
}
```
